```python
import torch

def _softmax(x, axis=-1):
    return torch.softmax(x, dim=axis)
def _gelu(x):
    return torch.nn.functional.gelu(x, approximate="tanh")
def _silu(x):
    return torch.nn.functional.silu(x)
def _sigmoid(x):
    return torch.sigmoid(x)
def _layernorm(x, axis=-1, eps=1e-5):
    m = x.mean(dim=axis, keepdim=True)
    v = x.var(dim=axis, keepdim=True, unbiased=False)
    return (x - m) / torch.sqrt(v + eps)
def _scatter_add(tgt, idx, src, axis=0):
    return tgt.clone().index_add_(axis, idx, src)
def _segment_sum(src, seg, n):
    out = torch.zeros((n,) + src.shape[1:], dtype=src.dtype)
    return out.index_add_(0, seg, src)
def _pad(x, pw):
    flat = tuple(v for pair in reversed(pw) for v in pair)
    return torch.nn.functional.pad(x, flat)

kernel_name = 'standalone__gqa_softmax__near_ridge'

def setup_inputs():

    _w = (lambda a: (lambda t: t.to(torch.bfloat16) if t.is_floating_point() else t)(torch.as_tensor(a)))
    B, Hq, Hkv, Sq, Sk, D = 16, 32, 32, 368, 512, 192
    return {
        "q": _w((lambda *s: torch.randn(*s, dtype=torch.bfloat16) * 0.8)(B, Hq, Sq, D)),
        "k": _w((lambda *s: torch.randn(*s, dtype=torch.bfloat16) * 0.8)(B, Hkv, Sk, D)),
        "v": _w((lambda *s: torch.randn(*s, dtype=torch.bfloat16) * 0.05)(B, Hkv, Sk, D)),
    }

def reference(q, k, v):
    B, Hq, Sq, D = q.shape
    Hkv = k.shape[1]
    rep = Hq // Hkv
    k = (lambda x, n, axis: torch.repeat_interleave(x, n, dim=axis))(k, rep, axis=1)
    v = (lambda x, n, axis: torch.repeat_interleave(x, n, dim=axis))(v, rep, axis=1)
    scale = D ** -0.5
    scores = torch.einsum("bhqd,bhkd->bhqk", q, k) * scale
    p = _softmax(scores, axis=-1)
    return torch.einsum("bhqk,bhkd->bhqd", p, v)
```

```cpp
#include <hip/hip_runtime.h>
#include <hip/hip_bf16.h>
#include <stdint.h>

namespace {

typedef float    f32x16 __attribute__((ext_vector_type(16)));
typedef float    f32x2  __attribute__((ext_vector_type(2)));
typedef __bf16   bf16x8 __attribute__((ext_vector_type(8)));
typedef __bf16   bf16x4 __attribute__((ext_vector_type(4)));
typedef __bf16   bf16x2 __attribute__((ext_vector_type(2)));
typedef unsigned u32x4  __attribute__((ext_vector_type(4)));
typedef __attribute__((address_space(3))) const char* lds_cptr;

constexpr int SQ = 368, SK = 512, DD = 192, NHEADS = 512;
constexpr unsigned ROWB = DD * 2;
constexpr unsigned TILEB = 32 * ROWB;
constexpr unsigned KHEADB = SK * ROWB;
constexpr unsigned QHEADB = SQ * ROWB;
constexpr unsigned KBYTES = NHEADS * KHEADB;
constexpr unsigned LDS_K = 0, LDS_V = 3 * TILEB, LDS_BYTES = 6 * TILEB;
constexpr unsigned OOB = 0x40000000u;
constexpr float C2 = 0.07216878364870322f * 1.4426950408889634f;
constexpr float THR_RAW = 8.0f / C2;

#define DI __device__ __forceinline__
#define SBAR() __builtin_amdgcn_sched_barrier(0)
#define MFMA(a, b, c) __builtin_amdgcn_mfma_f32_32x32x16_bf16(a, b, c, 0, 0, 0)
#define WAIT_BAR(N) asm volatile("s_waitcnt vmcnt(" #N ") lgkmcnt(0)\n\ts_barrier" ::: "memory")
#define MX3(a, b, c) __builtin_fmaxf(__builtin_fmaxf((a), (b)), (c))

DI unsigned cvtpk(float lo, float hi) {
  f32x2 v = {lo, hi}; bf16x2 b = __builtin_convertvector(v, bf16x2); return __builtin_bit_cast(unsigned, b);
}
DI bf16x4 vtr(lds_cptr p) {
  return __builtin_amdgcn_ds_read_tr16_b64_v4bf16((__attribute__((address_space(3))) bf16x4*)p);
}
template <int STEP>
DI void dma3(unsigned m0base, unsigned voff, __amdgpu_buffer_rsrc_t srd, unsigned soff) {
  asm volatile("s_mov_b32 m0, %0\n\ts_nop 0\n\tbuffer_load_dwordx4 %1, %2, %3 offen lds\n\t"
               "s_add_u32 m0, %0, %4\n\ts_nop 0\n\tbuffer_load_dwordx4 %1, %2, %3 offen offset:128 lds\n\t"
               "s_add_u32 m0, %0, %5\n\ts_nop 0\n\tbuffer_load_dwordx4 %1, %2, %3 offen offset:256 lds"
               :: "s"(m0base), "v"(voff), "s"(srd), "s"(soff), "i"(STEP - 128), "i"(2 * (STEP - 128)) : "scc", "memory");
}

struct Ctx {
  bf16x8 q[12];
  f32x16 o[6];
  float mref, thr, lsum;
  unsigned kb[4], vb;
  unsigned kvoff, vvoff, qovoff;
  unsigned kdst, vdst;
  unsigned sl_cur, sl_prev;
  __amdgpu_buffer_rsrc_t srdK, srdV;
};

DI void qload(Ctx& c, __amdgpu_buffer_rsrc_t srd, unsigned soff) {
#define QL(s) asm volatile("buffer_load_dwordx4 %0, %1, %2, %3 offen offset:%4 nt" : "=v"(c.q[s]) : "v"(c.qovoff), "s"(srd), "s"(soff), "i"(32 * (s)) : "memory")
  QL(0); QL(1); QL(2); QL(3); QL(4); QL(5); QL(6); QL(7); QL(8); QL(9); QL(10); QL(11);
#undef QL
}
#define QPIN(c) "+v"(c.q[0]), "+v"(c.q[1]), "+v"(c.q[2]), "+v"(c.q[3]), "+v"(c.q[4]), "+v"(c.q[5]), "+v"(c.q[6]), "+v"(c.q[7]), "+v"(c.q[8]), "+v"(c.q[9]), "+v"(c.q[10]), "+v"(c.q[11])

template <int MODE>
DI void tile(Ctx& c, unsigned vpf, unsigned kpf, bool cold, __amdgpu_buffer_rsrc_t srdQn, unsigned qsoffn) {
  if constexpr (MODE == 0) WAIT_BAR(15);
  else WAIT_BAR(12);
  dma3<1024>(c.vdst + c.sl_prev, c.vvoff, c.srdV, vpf);
  f32x16 s = {};
  {
    lds_cptr kp[4];
#pragma unroll
    for (int j = 0; j < 4; ++j) kp[j] = (lds_cptr)(uintptr_t)(c.kb[j] + c.sl_cur);
#pragma unroll
    for (int st = 0; st < 12; ++st) {
      const bf16x8 kf = *(const __attribute__((address_space(3))) bf16x8*)(kp[st & 3] + 4096 * (st >> 2));
      s = MFMA(kf, c.q[st], s);
    }
  }
  if constexpr (MODE == 2) { SBAR(); qload(c, srdQn, qsoffn); SBAR(); }
  float ma = MX3(s[0], s[1], s[2]), mb = MX3(s[3], s[4], s[5]);
  ma = MX3(ma, s[6], s[7]); mb = MX3(mb, s[8], s[9]); ma = MX3(ma, s[10], s[11]); mb = MX3(mb, s[12], s[13]);
  float mloc = MX3(ma, s[14], s[15]); mloc = __builtin_fmaxf(mloc, mb);
  if constexpr (MODE == 0) {
    auto rr = __builtin_amdgcn_permlane32_swap(__float_as_uint(mloc), __float_as_uint(mloc), false, false);
    c.mref = __builtin_fmaxf(__uint_as_float(rr[0]), __uint_as_float(rr[1]));
    c.thr = c.mref + THR_RAW; c.lsum = 0.f;
  } else {
    if (__builtin_expect(__any(mloc > c.thr), 0)) {
      auto rr = __builtin_amdgcn_permlane32_swap(__float_as_uint(mloc), __float_as_uint(mloc), false, false);
      const float mn = MX3(c.mref, __uint_as_float(rr[0]), __uint_as_float(rr[1]));
      const float al = __builtin_amdgcn_exp2f((c.mref - mn) * C2);
      c.mref = mn; c.thr = mn + THR_RAW; c.lsum *= al;
#pragma unroll
      for (int d = 0; d < 6; ++d)
#pragma unroll
        for (int r = 0; r < 16; ++r) c.o[d][r] *= al;
    }
  }
  const float nm = -c.mref * C2;
  float ls0 = 0.f, ls1 = 0.f;
#pragma unroll
  for (int r = 0; r < 16; r += 2) {
    s[r] = __builtin_amdgcn_exp2f(__builtin_fmaf(s[r], C2, nm));
    s[r + 1] = __builtin_amdgcn_exp2f(__builtin_fmaf(s[r + 1], C2, nm));
    ls0 += s[r]; ls1 += s[r + 1];
  }
  c.lsum += ls0 + ls1;
  u32x4 pw0 = {cvtpk(s[0], s[1]), cvtpk(s[2], s[3]), cvtpk(s[4], s[5]), cvtpk(s[6], s[7])};
  u32x4 pw1 = {cvtpk(s[8], s[9]), cvtpk(s[10], s[11]), cvtpk(s[12], s[13]), cvtpk(s[14], s[15])};
  if constexpr (MODE == 0) { if (cold) WAIT_BAR(12); else WAIT_BAR(18); }
  else if constexpr (MODE == 2) WAIT_BAR(24);
  else WAIT_BAR(12);
  dma3<4096>(c.kdst + c.sl_cur, c.kvoff, c.srdK, kpf);
  {
    const lds_cptr vp = (lds_cptr)(uintptr_t)(c.vb + c.sl_cur);
#pragma unroll
    for (int ks = 0; ks < 2; ++ks) {
      const bf16x8 pf = __builtin_bit_cast(bf16x8, ks ? pw1 : pw0);
#pragma unroll
      for (int db = 0; db < 6; ++db) {
        const bf16x4 a = vtr(vp + 3072 * (2 * ks) + 512 * db), b = vtr(vp + 3072 * (2 * ks + 1) + 512 * db);
        const bf16x8 vf = __builtin_shufflevector(a, b, 0, 1, 2, 3, 4, 5, 6, 7);
        if (MODE == 0 && ks == 0) c.o[db] = MFMA(vf, pf, f32x16{});
        else c.o[db] = MFMA(vf, pf, c.o[db]);
      }
    }
  }
  c.sl_prev = c.sl_cur;
  c.sl_cur = (c.sl_cur == 2 * TILEB) ? 0u : c.sl_cur + TILEB;
}

__global__ __launch_bounds__(256, 2)
void attn_fwd(const void* __restrict__ Q, const void* __restrict__ K, const void* __restrict__ V, void* __restrict__ O) {
  extern __shared__ __attribute__((aligned(16))) char lds[];
  const unsigned tid = threadIdx.x, lane = tid & 63u, r32 = lane & 31u, hi = lane >> 5;
  const unsigned wid = __builtin_amdgcn_readfirstlane(tid >> 6);
  const unsigned lds0 = (unsigned)(uintptr_t)lds;
  Ctx c;
#pragma unroll
  for (int j = 0; j < 4; ++j) c.kb[j] = lds0 + LDS_K + 128u * r32 + 16u * ((2u * j + hi) ^ ((r32 >> 1) & 7u));
  c.vb = lds0 + LDS_V + 64u * ((lane >> 2) & 3u) + 256u * hi + 32u * ((lane >> 4) & 1u) + 16u * (lane & 1u) + 8u * ((lane >> 1) & 1u);
  c.kvoff = (8u * wid + (lane >> 3)) * ROWB + 16u * ((lane & 7u) ^ ((4u * wid + (lane >> 4)) & 7u));
  c.vvoff = (8u * wid + ((lane >> 2) & 7u)) * ROWB + 64u * hi + 16u * (lane & 3u);
  c.qovoff = (32u * wid + r32) * ROWB + 16u * hi;
  c.kdst = lds0 + LDS_K + 1024u * wid;
  c.vdst = lds0 + LDS_V + 3072u * wid;
  c.srdK = __builtin_amdgcn_make_buffer_rsrc((void*)K, (short)0, (int)KBYTES, 0x00020000);
  c.srdV = __builtin_amdgcn_make_buffer_rsrc((void*)V, (short)0, (int)KBYTES, 0x00020000);
  c.sl_cur = 0; c.sl_prev = 2 * TILEB;

  const unsigned xcd = blockIdx.x & 7u, wi = blockIdx.x >> 3;
  unsigned bi = wi;
  unsigned head = xcd * 64u + bi / 3u, chunk = bi % 3u;
  {
    const __amdgpu_buffer_rsrc_t srdQ = __builtin_amdgcn_make_buffer_rsrc((void*)Q, (short)0, (int)((head + 1u) * QHEADB), 0x00020000);
    qload(c, srdQ, head * QHEADB + chunk * 128u * ROWB);
    const unsigned hb = head * KHEADB;
    dma3<4096>(c.kdst, c.kvoff, c.srdK, hb);
    dma3<1024>(c.vdst, c.vvoff, c.srdV, hb);
    dma3<4096>(c.kdst + TILEB, c.kvoff, c.srdK, hb + TILEB);
    dma3<1024>(c.vdst + TILEB, c.vvoff, c.srdV, hb + TILEB);
    dma3<4096>(c.kdst + 2 * TILEB, c.kvoff, c.srdK, hb + 2 * TILEB);
    asm volatile("s_waitcnt vmcnt(12)" : QPIN(c) :: "memory");
  }
  for (int blk = 0; blk < 3; ++blk) {
    const unsigned hb = head * KHEADB;
    const unsigned bn = bi + 64u;
    const unsigned headn = xcd * 64u + bn / 3u, chunkn = bn % 3u;
    const bool has_next = blk < 2;
    const unsigned hbn = has_next ? headn * KHEADB : OOB;
    const __amdgpu_buffer_rsrc_t srdQn = __builtin_amdgcn_make_buffer_rsrc((void*)Q, (short)0, (int)(has_next ? (headn + 1u) * QHEADB : 0u), 0x00020000);
    const unsigned qsoffn = has_next ? headn * QHEADB + chunkn * 128u * ROWB : 0u;

    tile<0>(c, hb + 2 * TILEB, hb + 3 * TILEB, blk == 0, srdQn, qsoffn);
    for (int t = 1; t < 15; ++t) {
      const unsigned vpf = (t + 2 < 16) ? hb + (t + 2) * TILEB : hbn + (t + 2 - 16) * TILEB;
      const unsigned kpf = (t + 3 < 16) ? hb + (t + 3) * TILEB : hbn + (t + 3 - 16) * TILEB;
      tile<1>(c, vpf, kpf, false, srdQn, qsoffn);
    }
    tile<2>(c, hbn + TILEB, hbn + 2 * TILEB, false, srdQn, qsoffn);

    {
      auto rr = __builtin_amdgcn_permlane32_swap(__float_as_uint(c.lsum), __float_as_uint(c.lsum), false, false);
      const float inv = __builtin_amdgcn_rcpf(__uint_as_float(rr[0]) + __uint_as_float(rr[1]));
      const __amdgpu_buffer_rsrc_t srdO = __builtin_amdgcn_make_buffer_rsrc((char*)O + (head * QHEADB + chunk * 128u * ROWB), (short)0, (int)(QHEADB - chunk * 128u * ROWB), 0x00020000);
#pragma unroll
      for (int db = 0; db < 6; ++db)
#pragma unroll
        for (int e = 0; e < 2; ++e) {
          const f32x16& o = c.o[db];
          u32x4 w = {cvtpk(o[8 * e + 0] * inv, o[8 * e + 1] * inv), cvtpk(o[8 * e + 2] * inv, o[8 * e + 3] * inv),
                     cvtpk(o[8 * e + 4] * inv, o[8 * e + 5] * inv), cvtpk(o[8 * e + 6] * inv, o[8 * e + 7] * inv)};
          __builtin_amdgcn_raw_buffer_store_b128(w, srdO, c.qovoff + 64u * db + 32u * e, 0, 2);
        }
    }
    asm volatile("s_waitcnt vmcnt(15)" : QPIN(c) :: "memory");
    bi = bn; head = headn; chunk = chunkn;
  }
}

}

extern "C" void kernel_launch(void* const* d_in, const int* in_sizes, int n_in, void* d_out, int out_size,
                              void* d_ws, size_t ws_size, hipStream_t stream) {
  (void)in_sizes; (void)n_in; (void)out_size; (void)d_ws; (void)ws_size;
  (void)hipFuncSetAttribute((const void*)attn_fwd, hipFuncAttributeMaxDynamicSharedMemorySize, (int)LDS_BYTES);
  hipLaunchKernelGGL(attn_fwd, dim3(512), dim3(256), LDS_BYTES, stream, d_in[0], d_in[1], d_in[2], d_out);
}
```

```cpp
#include <hip/hip_runtime.h>
#include <stdint.h>

namespace {
typedef unsigned u32x4 __attribute__((ext_vector_type(4)));
typedef unsigned u32x2 __attribute__((ext_vector_type(2)));
constexpr unsigned ROWB = 384, TILEB = 12288, KHEADB = 196608, QHEADB = 141312, KBYTES = 512u * KHEADB;
constexpr unsigned LDS_K = 0, LDS_V = 4 * TILEB, LDS_BUF = 8 * TILEB, LDS_BYTES = 12 * TILEB;

__global__ __launch_bounds__(256, 1)
void attn_fwd(const void* __restrict__ Q, const void* __restrict__ K, const void* __restrict__ V, void* __restrict__ O) {
  extern __shared__ __attribute__((aligned(16))) char lds[];
  const unsigned tid = threadIdx.x, lane = tid & 63u, r32 = lane & 31u, hi = lane >> 5;
  const unsigned wid = __builtin_amdgcn_readfirstlane(tid >> 6);
  const unsigned lds0 = (unsigned)(uintptr_t)lds;
  unsigned kb[4], kv[3], vv[3], ga[2], fa[4];
#pragma unroll
  for (unsigned j = 0; j < 4; ++j) kb[j] = lds0 + LDS_K + 3072u * (r32 >> 3) + 128u * (r32 & 7u) + 16u * ((2u * j + hi) ^ ((r32 >> 1) & 7u));
  unsigned vb = lds0 + LDS_V + 64u * ((lane >> 2) & 3u) + 256u * hi + 32u * ((lane >> 4) & 1u) + 16u * (lane & 1u) + 8u * ((lane >> 1) & 1u);
#pragma unroll
  for (unsigned b = 0; b < 3; ++b) {
    kv[b] = (8u * wid + (lane >> 3)) * ROWB + 16u * ((lane & 7u) ^ ((4u * wid + (lane >> 4)) & 7u)) + 2048u - 896u * b;
    vv[b] = (8u * wid + ((lane >> 2) & 7u)) * ROWB + 64u * hi + 16u * (lane & 3u) + 2048u - 896u * b;
  }
#pragma unroll
  for (unsigned k = 0; k < 2; ++k) {
    const unsigned row = 8u * k + (lane >> 3);
    const unsigned sg = (row & 6u) | ((row ^ (row >> 3)) & 1u);
    ga[k] = (64u * (wid & 1u) + row) * ROWB + 16u * ((lane & 7u) ^ sg);
  }
  unsigned buf = lds0 + LDS_BUF + TILEB * wid;
  const unsigned sgf = (r32 & 6u) | ((r32 ^ (r32 >> 3)) & 1u);
#pragma unroll
  for (unsigned j = 0; j < 4; ++j) fa[j] = buf + 128u * r32 + 16u * ((2u * j + hi) ^ sgf);
  unsigned la = buf + 16u * lane;
  unsigned kwb = lds0 + LDS_K + 3072u * wid, vwb = lds0 + LDS_V + 3072u * wid;
  const uintptr_t Kp = (uintptr_t)K - 2048u, Vp = (uintptr_t)V - 2048u;
  u32x4 srdK = {(unsigned)Kp, (unsigned)(Kp >> 32) & 0xffffu, KBYTES + 2048u, 0x20000u};
  u32x4 srdV = {(unsigned)Vp, (unsigned)(Vp >> 32) & 0xffffu, KBYTES + 2048u, 0x20000u};
  u32x2 qptr = {(unsigned)(uintptr_t)Q, (unsigned)((uintptr_t)Q >> 32) & 0xffffu};
  u32x2 optr = {(unsigned)(uintptr_t)O, (unsigned)((uintptr_t)O >> 32) & 0xffffu};
  const unsigned xcd = blockIdx.x & 7u, wi = blockIdx.x >> 3, g = wid >> 1;
  unsigned grp = g * TILEB;
  unsigned ba[3], bb[3], st[3], qo[3], qn[3];
#pragma unroll
  for (unsigned r = 0; r < 3; ++r) {
    const unsigned u = wi < 30u ? 3u * (10u * r + wi / 3u) + (wi + r) % 3u : (r < 2u ? 3u * (30u + r) + 2u * (wi - 30u) : 3u * wi + 1u);
    const unsigned hA = xcd * 64u + (2u * u) / 3u, hB = xcd * 64u + (2u * u + 1u) / 3u;
    const bool split = hA != hB;
    ba[r] = hA * KHEADB; bb[r] = split ? hB * KHEADB : hA * KHEADB + TILEB; st[r] = split ? TILEB : 2u * TILEB;
    const unsigned b = 2u * u + g, head = xcd * 64u + b / 3u, chunk = b % 3u;
    qo[r] = head * QHEADB + chunk * 128u * ROWB; qn[r] = QHEADB - chunk * 128u * ROWB;
  }

  asm volatile(
    "s_add_u32 s24, s82, s75\n\t"
    "s_addc_u32 s25, s83, 0\n\t"
    "s_mov_b32 s26, s78\n\t"
    "s_mov_b32 s27, 0x20000\n\t"
    "s_mov_b32 s28, s84\n\t"
    "s_mov_b32 s29, s85\n\t"
    "s_mov_b32 s30, 0\n\t"
    "s_mov_b32 s31, 0x20000\n\t"
    "s_mov_b32 s86, 6144\n\t"
    "s_mov_b32 s87, 12288\n\t"
    "s_mov_b32 s43, 18432\n\t"
    "s_mov_b32 s36, s66\n\t"
    "s_mov_b32 s37, s69\n\t"
    "s_add_u32 s38, s66, s72\n\t"
    "s_add_u32 s39, s69, s72\n\t"
    "s_cmp_eq_u32 s72, 12288\n\t"
    "s_cselect_b32 s56, s81, 0\n\t"
    "s_mov_b32 s59, 0\n\t"
    "s_mov_b32 s62, 0\n\t"
    "s_mov_b32 s63, 0x3dd53b95\n\t"
    "s_mov_b32 s64, 0x49800000\n\t"
    "v_add_u32 v230, s56, v230\n\t"
    "v_add_u32 v231, s56, v231\n\t"
    "v_add_u32 v232, s56, v232\n\t"
    "v_add_u32 v233, s56, v233\n\t"
    "v_add_u32 v234, s56, v234\n\t"
    "s_mov_b32 m0, s46\n\t"
    "s_nop 0\n\t"
    "buffer_load_dwordx4 v235, s[16:19], s36 offen lds\n\t"
    "buffer_load_dwordx4 v236, s[16:19], s36 offen offset:1024 lds\n\t"
    "buffer_load_dwordx4 v237, s[16:19], s36 offen offset:2048 lds\n\t"
    "s_add_u32 m0, s46, 12288\n\t"
    "s_nop 0\n\t"
    "buffer_load_dwordx4 v235, s[16:19], s37 offen lds\n\t"
    "buffer_load_dwordx4 v236, s[16:19], s37 offen offset:1024 lds\n\t"
    "buffer_load_dwordx4 v237, s[16:19], s37 offen offset:2048 lds\n\t"
    "s_add_u32 m0, s61, 0\n\t"
    "s_nop 0\n\t"
    "buffer_load_dwordx4 v248, s[24:27], 0 offen nt lds\n\t"
    "s_add_u32 m0, s61, 1024\n\t"
    "s_nop 0\n\t"
    "buffer_load_dwordx4 v249, s[24:27], 0 offen nt lds\n\t"
    "s_add_u32 m0, s61, 2048\n\t"
    "s_nop 0\n\t"
    "buffer_load_dwordx4 v248, s[24:27], s86 offen nt lds\n\t"
    "s_add_u32 m0, s61, 3072\n\t"
    "s_nop 0\n\t"
    "buffer_load_dwordx4 v249, s[24:27], s86 offen nt lds\n\t"
    "s_add_u32 m0, s61, 3968\n\t"
    "s_nop 0\n\t"
    "buffer_load_dwordx4 v248, s[24:27], 0 offen offset:128 nt lds\n\t"
    "s_add_u32 m0, s61, 4992\n\t"
    "s_nop 0\n\t"
    "buffer_load_dwordx4 v249, s[24:27], 0 offen offset:128 nt lds\n\t"
    "s_add_u32 m0, s61, 6016\n\t"
    "s_nop 0\n\t"
    "buffer_load_dwordx4 v248, s[24:27], s86 offen offset:128 nt lds\n\t"
    "s_add_u32 m0, s61, 7040\n\t"
    "s_nop 0\n\t"
    "buffer_load_dwordx4 v249, s[24:27], s86 offen offset:128 nt lds\n\t"
    "s_add_u32 m0, s61, 7936\n\t"
    "s_nop 0\n\t"
    "buffer_load_dwordx4 v248, s[24:27], 0 offen offset:256 nt lds\n\t"
    "s_add_u32 m0, s61, 8960\n\t"
    "s_nop 0\n\t"
    "buffer_load_dwordx4 v249, s[24:27], 0 offen offset:256 nt lds\n\t"
    "s_add_u32 m0, s61, 9984\n\t"
    "s_nop 0\n\t"
    "buffer_load_dwordx4 v248, s[24:27], s86 offen offset:256 nt lds\n\t"
    "s_add_u32 m0, s61, 11008\n\t"
    "s_nop 0\n\t"
    "buffer_load_dwordx4 v249, s[24:27], s86 offen offset:256 nt lds\n\t"
    "buffer_load_dwordx4 a[208:211], v248, s[24:27], s87 offen nt\n\t"
    "buffer_load_dwordx4 a[212:215], v249, s[24:27], s87 offen nt\n\t"
    "buffer_load_dwordx4 a[216:219], v248, s[24:27], s43 offen nt\n\t"
    "buffer_load_dwordx4 a[220:223], v249, s[24:27], s43 offen nt\n\t"
    "buffer_load_dwordx4 a[224:227], v248, s[24:27], s87 offen offset:128 nt\n\t"
    "buffer_load_dwordx4 a[228:231], v249, s[24:27], s87 offen offset:128 nt\n\t"
    "buffer_load_dwordx4 a[232:235], v248, s[24:27], s43 offen offset:128 nt\n\t"
    "buffer_load_dwordx4 a[236:239], v249, s[24:27], s43 offen offset:128 nt\n\t"
    "buffer_load_dwordx4 a[240:243], v248, s[24:27], s87 offen offset:256 nt\n\t"
    "buffer_load_dwordx4 a[244:247], v249, s[24:27], s87 offen offset:256 nt\n\t"
    "buffer_load_dwordx4 a[248:251], v248, s[24:27], s43 offen offset:256 nt\n\t"
    "buffer_load_dwordx4 a[252:255], v249, s[24:27], s43 offen offset:256 nt\n\t"
    "s_waitcnt vmcnt(24) lgkmcnt(0)\n\t"
    "s_barrier\n\t"
    "ds_read_b128 a[96:99], v230 offset:0\n\t"
    "ds_read_b128 a[100:103], v231 offset:0\n\t"
    "ds_read_b128 a[104:107], v232 offset:0\n\t"
    "ds_read_b128 a[108:111], v233 offset:0\n\t"
    "ds_read_b128 a[112:115], v230 offset:1024\n\t"
    "ds_read_b128 a[116:119], v231 offset:1024\n\t"
    "ds_read_b128 a[120:123], v232 offset:1024\n\t"
    "ds_read_b128 a[124:127], v233 offset:1024\n\t"
    "ds_read_b128 a[128:131], v230 offset:2048\n\t"
    "ds_read_b128 a[132:135], v231 offset:2048\n\t"
    "ds_read_b128 a[136:139], v232 offset:2048\n\t"
    "ds_read_b128 a[140:143], v233 offset:2048\n\t"
    "s_waitcnt vmcnt(12)\n\t"
    "ds_read_b128 a[0:3], v244 offset:0\n\t"
    "ds_read_b128 a[4:7], v245 offset:0\n\t"
    "ds_read_b128 a[8:11], v246 offset:0\n\t"
    "ds_read_b128 a[12:15], v247 offset:0\n\t"
    "ds_read_b128 a[16:19], v244 offset:4096\n\t"
    "ds_read_b128 a[20:23], v245 offset:4096\n\t"
    "ds_read_b128 a[24:27], v246 offset:4096\n\t"
    "ds_read_b128 a[28:31], v247 offset:4096\n\t"
    "ds_read_b128 a[32:35], v244 offset:8192\n\t"
    "ds_read_b128 a[36:39], v245 offset:8192\n\t"
    "ds_read_b128 a[40:43], v246 offset:8192\n\t"
    "ds_read_b128 a[44:47], v247 offset:8192\n\t"
    "s_add_u32 m0, s46, 24576\n\t"
    "s_nop 0\n\t"
    "buffer_load_dwordx4 v235, s[16:19], s38 offen lds\n\t"
    "buffer_load_dwordx4 v236, s[16:19], s38 offen offset:1024 lds\n\t"
    "buffer_load_dwordx4 v237, s[16:19], s38 offen offset:2048 lds\n\t"
    "s_add_u32 m0, s46, 36864\n\t"
    "s_nop 0\n\t"
    "buffer_load_dwordx4 v235, s[16:19], s39 offen lds\n\t"
    "buffer_load_dwordx4 v236, s[16:19], s39 offen offset:1024 lds\n\t"
    "buffer_load_dwordx4 v237, s[16:19], s39 offen offset:2048 lds\n\t"
    "s_mov_b32 m0, s49\n\t"
    "s_nop 0\n\t"
    "buffer_load_dwordx4 v238, s[20:23], s36 offen lds\n\t"
    "buffer_load_dwordx4 v239, s[20:23], s36 offen offset:1024 lds\n\t"
    "buffer_load_dwordx4 v240, s[20:23], s36 offen offset:2048 lds\n\t"
    "s_add_u32 m0, s49, 12288\n\t"
    "s_nop 0\n\t"
    "buffer_load_dwordx4 v238, s[20:23], s37 offen lds\n\t"
    "buffer_load_dwordx4 v239, s[20:23], s37 offen offset:1024 lds\n\t"
    "buffer_load_dwordx4 v240, s[20:23], s37 offen offset:2048 lds\n\t"
    "s_add_u32 m0, s49, 24576\n\t"
    "s_nop 0\n\t"
    "buffer_load_dwordx4 v238, s[20:23], s38 offen lds\n\t"
    "buffer_load_dwordx4 v239, s[20:23], s38 offen offset:1024 lds\n\t"
    "buffer_load_dwordx4 v240, s[20:23], s38 offen offset:2048 lds\n\t"
    "s_add_u32 m0, s49, 36864\n\t"
    "s_nop 0\n\t"
    "buffer_load_dwordx4 v238, s[20:23], s39 offen lds\n\t"
    "buffer_load_dwordx4 v239, s[20:23], s39 offen offset:1024 lds\n\t"
    "buffer_load_dwordx4 v240, s[20:23], s39 offen offset:2048 lds\n\t"
    "s_waitcnt vmcnt(18)\n\t"
    "ds_write_b128 v241, a[208:211] offset:0\n\t"
    "ds_write_b128 v241, a[212:215] offset:1024\n\t"
    "ds_write_b128 v241, a[216:219] offset:2048\n\t"
    "ds_write_b128 v241, a[220:223] offset:3072\n\t"
    "ds_write_b128 v241, a[224:227] offset:4096\n\t"
    "ds_write_b128 v241, a[228:231] offset:5120\n\t"
    "ds_write_b128 v241, a[232:235] offset:6144\n\t"
    "ds_write_b128 v241, a[236:239] offset:7168\n\t"
    "ds_write_b128 v241, a[240:243] offset:8192\n\t"
    "ds_write_b128 v241, a[244:247] offset:9216\n\t"
    "ds_write_b128 v241, a[248:251] offset:10240\n\t"
    "ds_write_b128 v241, a[252:255] offset:11264\n\t"
    "ds_read_b128 a[48:51], v244 offset:0\n\t"
    "ds_read_b128 a[52:55], v245 offset:0\n\t"
    "ds_read_b128 a[56:59], v246 offset:0\n\t"
    "ds_read_b128 a[60:63], v247 offset:0\n\t"
    "ds_read_b128 a[64:67], v244 offset:4096\n\t"
    "ds_read_b128 a[68:71], v245 offset:4096\n\t"
    "ds_read_b128 a[72:75], v246 offset:4096\n\t"
    "ds_read_b128 a[76:79], v247 offset:4096\n\t"
    "ds_read_b128 a[80:83], v244 offset:8192\n\t"
    "ds_read_b128 a[84:87], v245 offset:8192\n\t"
    "ds_read_b128 a[88:91], v246 offset:8192\n\t"
    "ds_read_b128 a[92:95], v247 offset:8192\n\t"
    "s_waitcnt lgkmcnt(0)\n\t"
    "s_cmp_eq_u32 s72, 12288\n\t"
    "s_cbranch_scc1 .Ld_u1c\n\t"
    "s_branch .Ld_u0c\n\t"
    ".Ld_u0:\n\t"
    ".Ld_u0x:\n\t"
    "s_nop 3\n\t"
    ".Ld_u0c:\n\t"
    "s_add_u32 s32, s66, 49152\n\t"
    "s_add_u32 s34, s66, 49152\n\t"
    "s_mov_b32 s36, s67\n\t"
    "s_mov_b32 s37, s70\n\t"
    "s_add_u32 s38, s67, s73\n\t"
    "s_add_u32 s39, s70, s73\n\t"
    "s_cmp_eq_u32 s73, 12288\n\t"
    "s_cselect_b32 s54, s81, 0\n\t"
    "s_cmp_lg_u32 s73, 0\n\t"
    "s_cselect_b32 s42, 100665344, 0\n\t"
    "s_add_u32 s24, s82, s76\n\t"
    "s_addc_u32 s25, s83, 0\n\t"
    "s_mov_b32 s26, s79\n\t"
    "s_waitcnt lgkmcnt(14)\n\t"
    "v_mfma_f32_32x32x16_bf16 v[128:143], a[96:99], a[0:3], 0\n\t"
    "v_mul_f32 v32, v242, v32\n\t"
    "v_mul_f32 v33, v242, v33\n\t"
    "v_mul_f32 v34, v242, v34\n\t"
    "v_mul_f32 v35, v242, v35\n\t"
    "v_mul_f32 v36, v242, v36\n\t"
    "v_mul_f32 v37, v242, v37\n\t"
    "v_mul_f32 v38, v242, v38\n\t"
    "v_mul_f32 v39, v242, v39\n\t"
    "v_cvt_pk_bf16_f32 v32, v32, v33\n\t"
    "s_waitcnt lgkmcnt(13)\n\t"
    "v_mfma_f32_32x32x16_bf16 v[128:143], a[100:103], a[4:7], v[128:143]\n\t"
    "v_cvt_pk_bf16_f32 v33, v34, v35\n\t"
    "v_cvt_pk_bf16_f32 v34, v36, v37\n\t"
    "v_cvt_pk_bf16_f32 v35, v38, v39\n\t"
    "ds_write_b128 v244, v[32:35] offset:4096\n\t"
    "v_mul_f32 v40, v242, v40\n\t"
    "v_mul_f32 v41, v242, v41\n\t"
    "v_mul_f32 v42, v242, v42\n\t"
    "v_mul_f32 v43, v242, v43\n\t"
    "v_mul_f32 v44, v242, v44\n\t"
    "s_waitcnt lgkmcnt(13)\n\t"
    "v_mfma_f32_32x32x16_bf16 v[128:143], a[104:107], a[8:11], v[128:143]\n\t"
    "v_mul_f32 v45, v242, v45\n\t"
    "v_mul_f32 v46, v242, v46\n\t"
    "v_mul_f32 v47, v242, v47\n\t"
    "v_cvt_pk_bf16_f32 v40, v40, v41\n\t"
    "v_cvt_pk_bf16_f32 v41, v42, v43\n\t"
    "v_cvt_pk_bf16_f32 v42, v44, v45\n\t"
    "v_cvt_pk_bf16_f32 v43, v46, v47\n\t"
    "ds_write_b128 v245, v[40:43] offset:4096\n\t"
    "v_mul_f32 v48, v242, v48\n\t"
    "v_mul_f32 v49, v242, v49\n\t"
    "s_waitcnt lgkmcnt(13)\n\t"
    "v_mfma_f32_32x32x16_bf16 v[128:143], a[108:111], a[12:15], v[128:143]\n\t"
    "v_mul_f32 v50, v242, v50\n\t"
    "v_mul_f32 v51, v242, v51\n\t"
    "v_mul_f32 v52, v242, v52\n\t"
    "v_mul_f32 v53, v242, v53\n\t"
    "v_mul_f32 v54, v242, v54\n\t"
    "s_waitcnt vmcnt(22) lgkmcnt(0)\n\t"
    "s_barrier\n\t"
    "v_mfma_f32_32x32x16_bf16 v[128:143], a[112:115], a[16:19], v[128:143]\n\t"
    "s_mov_b32 m0, s46\n\t"
    "s_nop 0\n\t"
    "buffer_load_dwordx4 v235, s[16:19], s32 offen lds\n\t"
    "v_mul_f32 v55, v242, v55\n\t"
    "v_cvt_pk_bf16_f32 v48, v48, v49\n\t"
    "v_cvt_pk_bf16_f32 v49, v50, v51\n\t"
    "v_cvt_pk_bf16_f32 v50, v52, v53\n\t"
    "v_cvt_pk_bf16_f32 v51, v54, v55\n\t"
    "ds_write_b128 v246, v[48:51] offset:4096\n\t"
    "v_mfma_f32_32x32x16_bf16 v[128:143], a[116:119], a[20:23], v[128:143]\n\t"
    "v_mul_f32 v56, v242, v56\n\t"
    "v_mul_f32 v57, v242, v57\n\t"
    "v_mul_f32 v58, v242, v58\n\t"
    "v_mul_f32 v59, v242, v59\n\t"
    "v_mul_f32 v60, v242, v60\n\t"
    "v_mul_f32 v61, v242, v61\n\t"
    "v_mul_f32 v62, v242, v62\n\t"
    "v_mul_f32 v63, v242, v63\n\t"
    "v_cvt_pk_bf16_f32 v56, v56, v57\n\t"
    "v_mfma_f32_32x32x16_bf16 v[128:143], a[120:123], a[24:27], v[128:143]\n\t"
    "v_cvt_pk_bf16_f32 v57, v58, v59\n\t"
    "v_cvt_pk_bf16_f32 v58, v60, v61\n\t"
    "v_cvt_pk_bf16_f32 v59, v62, v63\n\t"
    "ds_write_b128 v247, v[56:59] offset:4096\n\t"
    "v_mul_f32 v64, v242, v64\n\t"
    "v_mul_f32 v65, v242, v65\n\t"
    "v_mul_f32 v66, v242, v66\n\t"
    "v_mul_f32 v67, v242, v67\n\t"
    "v_mul_f32 v68, v242, v68\n\t"
    "v_mfma_f32_32x32x16_bf16 v[128:143], a[124:127], a[28:31], v[128:143]\n\t"
    "v_mul_f32 v69, v242, v69\n\t"
    "v_mul_f32 v70, v242, v70\n\t"
    "v_mul_f32 v71, v242, v71\n\t"
    "v_cvt_pk_bf16_f32 v64, v64, v65\n\t"
    "v_cvt_pk_bf16_f32 v65, v66, v67\n\t"
    "v_cvt_pk_bf16_f32 v66, v68, v69\n\t"
    "v_cvt_pk_bf16_f32 v67, v70, v71\n\t"
    "ds_write_b128 v244, v[64:67] offset:8192\n\t"
    "v_mul_f32 v72, v242, v72\n\t"
    "v_mul_f32 v73, v242, v73\n\t"
    "v_mfma_f32_32x32x16_bf16 v[128:143], a[128:131], a[32:35], v[128:143]\n\t"
    "buffer_load_dwordx4 v236, s[16:19], s32 offen offset:1024 lds\n\t"
    "v_mul_f32 v74, v242, v74\n\t"
    "v_mul_f32 v75, v242, v75\n\t"
    "v_mul_f32 v76, v242, v76\n\t"
    "v_mul_f32 v77, v242, v77\n\t"
    "v_mul_f32 v78, v242, v78\n\t"
    "v_mul_f32 v79, v242, v79\n\t"
    "v_cvt_pk_bf16_f32 v72, v72, v73\n\t"
    "v_cvt_pk_bf16_f32 v73, v74, v75\n\t"
    "v_mfma_f32_32x32x16_bf16 v[128:143], a[132:135], a[36:39], v[128:143]\n\t"
    "v_cvt_pk_bf16_f32 v74, v76, v77\n\t"
    "v_cvt_pk_bf16_f32 v75, v78, v79\n\t"
    "ds_write_b128 v245, v[72:75] offset:8192\n\t"
    "v_mul_f32 v80, v242, v80\n\t"
    "v_mul_f32 v81, v242, v81\n\t"
    "v_mul_f32 v82, v242, v82\n\t"
    "v_mul_f32 v83, v242, v83\n\t"
    "v_mul_f32 v84, v242, v84\n\t"
    "v_mul_f32 v85, v242, v85\n\t"
    "v_mfma_f32_32x32x16_bf16 v[128:143], a[136:139], a[40:43], v[128:143]\n\t"
    "v_mul_f32 v86, v242, v86\n\t"
    "v_mul_f32 v87, v242, v87\n\t"
    "v_cvt_pk_bf16_f32 v80, v80, v81\n\t"
    "v_cvt_pk_bf16_f32 v81, v82, v83\n\t"
    "v_cvt_pk_bf16_f32 v82, v84, v85\n\t"
    "v_cvt_pk_bf16_f32 v83, v86, v87\n\t"
    "ds_write_b128 v246, v[80:83] offset:8192\n\t"
    "v_mul_f32 v88, v242, v88\n\t"
    "v_mul_f32 v89, v242, v89\n\t"
    "v_mfma_f32_32x32x16_bf16 v[128:143], a[140:143], a[44:47], v[128:143]\n\t"
    "v_mul_f32 v90, v242, v90\n\t"
    "v_mul_f32 v91, v242, v91\n\t"
    "v_mul_f32 v92, v242, v92\n\t"
    "v_mul_f32 v93, v242, v93\n\t"
    "v_mul_f32 v94, v242, v94\n\t"
    "v_mul_f32 v95, v242, v95\n\t"
    "v_cvt_pk_bf16_f32 v88, v88, v89\n\t"
    "v_cvt_pk_bf16_f32 v89, v90, v91\n\t"
    "v_cvt_pk_bf16_f32 v90, v92, v93\n\t"
    "v_mfma_f32_32x32x16_bf16 v[144:159], a[96:99], a[48:51], 0\n\t"
    "ds_read_b128 a[96:99], v230 offset:12288\n\t"
    "buffer_load_dwordx4 v237, s[16:19], s32 offen offset:2048 lds\n\t"
    "s_add_u32 s32, s32, 12288\n\t"
    "v_cvt_pk_bf16_f32 v91, v94, v95\n\t"
    "ds_write_b128 v247, v[88:91] offset:8192\n\t"
    "v_mul_f32 v96, v243, v96\n\t"
    "v_mul_f32 v97, v243, v97\n\t"
    "v_mul_f32 v98, v243, v98\n\t"
    "v_mul_f32 v99, v243, v99\n\t"
    "v_mul_f32 v100, v243, v100\n\t"
    "v_mfma_f32_32x32x16_bf16 v[144:159], a[100:103], a[52:55], v[144:159]\n\t"
    "ds_read_b128 a[100:103], v231 offset:12288\n\t"
    "v_mul_f32 v101, v243, v101\n\t"
    "v_mul_f32 v102, v243, v102\n\t"
    "v_mul_f32 v103, v243, v103\n\t"
    "v_cvt_pk_bf16_f32 v96, v96, v97\n\t"
    "v_cvt_pk_bf16_f32 v97, v98, v99\n\t"
    "v_cvt_pk_bf16_f32 v98, v100, v101\n\t"
    "v_cvt_pk_bf16_f32 v99, v102, v103\n\t"
    "v_accvgpr_write_b32 a208, v96\n\t"
    "v_mfma_f32_32x32x16_bf16 v[144:159], a[104:107], a[56:59], v[144:159]\n\t"
    "ds_read_b128 a[104:107], v232 offset:12288\n\t"
    "v_accvgpr_write_b32 a209, v97\n\t"
    "v_accvgpr_write_b32 a210, v98\n\t"
    "v_accvgpr_write_b32 a211, v99\n\t"
    "v_mul_f32 v104, v243, v104\n\t"
    "v_mul_f32 v105, v243, v105\n\t"
    "v_mul_f32 v106, v243, v106\n\t"
    "v_mul_f32 v107, v243, v107\n\t"
    "v_mul_f32 v108, v243, v108\n\t"
    "v_mfma_f32_32x32x16_bf16 v[144:159], a[108:111], a[60:63], v[144:159]\n\t"
    "ds_read_b128 a[108:111], v233 offset:12288\n\t"
    "v_mul_f32 v109, v243, v109\n\t"
    "v_mul_f32 v110, v243, v110\n\t"
    "v_mul_f32 v111, v243, v111\n\t"
    "v_cvt_pk_bf16_f32 v104, v104, v105\n\t"
    "v_cvt_pk_bf16_f32 v105, v106, v107\n\t"
    "v_cvt_pk_bf16_f32 v106, v108, v109\n\t"
    "v_cvt_pk_bf16_f32 v107, v110, v111\n\t"
    "v_accvgpr_write_b32 a212, v104\n\t"
    "v_mfma_f32_32x32x16_bf16 v[144:159], a[112:115], a[64:67], v[144:159]\n\t"
    "ds_read_b128 a[112:115], v230 offset:13312\n\t"
    "v_accvgpr_write_b32 a213, v105\n\t"
    "v_accvgpr_write_b32 a214, v106\n\t"
    "v_accvgpr_write_b32 a215, v107\n\t"
    "v_mul_f32 v112, v243, v112\n\t"
    "v_mul_f32 v113, v243, v113\n\t"
    "v_mul_f32 v114, v243, v114\n\t"
    "v_mul_f32 v115, v243, v115\n\t"
    "v_mul_f32 v116, v243, v116\n\t"
    "v_mfma_f32_32x32x16_bf16 v[144:159], a[116:119], a[68:71], v[144:159]\n\t"
    "ds_read_b128 a[116:119], v231 offset:13312\n\t"
    "v_mul_f32 v117, v243, v117\n\t"
    "v_mul_f32 v118, v243, v118\n\t"
    "v_mul_f32 v119, v243, v119\n\t"
    "v_cvt_pk_bf16_f32 v112, v112, v113\n\t"
    "v_cvt_pk_bf16_f32 v113, v114, v115\n\t"
    "v_cvt_pk_bf16_f32 v114, v116, v117\n\t"
    "v_cvt_pk_bf16_f32 v115, v118, v119\n\t"
    "v_accvgpr_write_b32 a216, v112\n\t"
    "v_accvgpr_write_b32 a217, v113\n\t"
    "v_mfma_f32_32x32x16_bf16 v[144:159], a[120:123], a[72:75], v[144:159]\n\t"
    "ds_read_b128 a[120:123], v232 offset:13312\n\t"
    "v_accvgpr_write_b32 a218, v114\n\t"
    "v_accvgpr_write_b32 a219, v115\n\t"
    "v_mul_f32 v120, v243, v120\n\t"
    "v_mul_f32 v121, v243, v121\n\t"
    "v_mul_f32 v122, v243, v122\n\t"
    "v_mul_f32 v123, v243, v123\n\t"
    "v_mul_f32 v124, v243, v124\n\t"
    "v_mul_f32 v125, v243, v125\n\t"
    "v_mfma_f32_32x32x16_bf16 v[144:159], a[124:127], a[76:79], v[144:159]\n\t"
    "ds_read_b128 a[124:127], v233 offset:13312\n\t"
    "v_mul_f32 v126, v243, v126\n\t"
    "v_mul_f32 v127, v243, v127\n\t"
    "v_cvt_pk_bf16_f32 v120, v120, v121\n\t"
    "v_cvt_pk_bf16_f32 v121, v122, v123\n\t"
    "v_cvt_pk_bf16_f32 v122, v124, v125\n\t"
    "v_cvt_pk_bf16_f32 v123, v126, v127\n\t"
    "v_accvgpr_write_b32 a220, v120\n\t"
    "v_accvgpr_write_b32 a221, v121\n\t"
    "v_mfma_f32_32x32x16_bf16 v[144:159], a[128:131], a[80:83], v[144:159]\n\t"
    "ds_read_b128 a[128:131], v230 offset:14336\n\t"
    "v_accvgpr_write_b32 a222, v122\n\t"
    "v_accvgpr_write_b32 a223, v123\n\t"
    "v_accvgpr_read_b32 v0, a144\n\t"
    "v_accvgpr_read_b32 v1, a145\n\t"
    "v_accvgpr_read_b32 v2, a146\n\t"
    "v_accvgpr_read_b32 v3, a147\n\t"
    "v_accvgpr_read_b32 v4, a148\n\t"
    "v_accvgpr_read_b32 v5, a149\n\t"
    "v_mfma_f32_32x32x16_bf16 v[144:159], a[132:135], a[84:87], v[144:159]\n\t"
    "ds_read_b128 a[132:135], v231 offset:14336\n\t"
    "v_accvgpr_read_b32 v6, a150\n\t"
    "v_accvgpr_read_b32 v7, a151\n\t"
    "v_mul_f32 v0, v243, v0\n\t"
    "v_mul_f32 v1, v243, v1\n\t"
    "v_mul_f32 v2, v243, v2\n\t"
    "v_mul_f32 v3, v243, v3\n\t"
    "v_mul_f32 v4, v243, v4\n\t"
    "v_mul_f32 v5, v243, v5\n\t"
    "v_mfma_f32_32x32x16_bf16 v[144:159], a[136:139], a[88:91], v[144:159]\n\t"
    "ds_read_b128 a[136:139], v232 offset:14336\n\t"
    "v_mul_f32 v6, v243, v6\n\t"
    "v_mul_f32 v7, v243, v7\n\t"
    "v_cvt_pk_bf16_f32 v0, v0, v1\n\t"
    "v_cvt_pk_bf16_f32 v1, v2, v3\n\t"
    "v_cvt_pk_bf16_f32 v2, v4, v5\n\t"
    "v_cvt_pk_bf16_f32 v3, v6, v7\n\t"
    "v_accvgpr_write_b32 a224, v0\n\t"
    "v_accvgpr_write_b32 a225, v1\n\t"
    "v_accvgpr_write_b32 a226, v2\n\t"
    "v_mfma_f32_32x32x16_bf16 v[144:159], a[140:143], a[92:95], v[144:159]\n\t"
    "ds_read_b128 a[140:143], v233 offset:14336\n\t"
    "v_accvgpr_write_b32 a227, v3\n\t"
    "v_accvgpr_read_b32 v8, a152\n\t"
    "v_accvgpr_read_b32 v9, a153\n\t"
    "v_accvgpr_read_b32 v10, a154\n\t"
    "v_accvgpr_read_b32 v11, a155\n\t"
    "v_accvgpr_read_b32 v12, a156\n\t"
    "v_accvgpr_read_b32 v13, a157\n\t"
    "s_waitcnt lgkmcnt(10)\n\t"
    "v_mfma_f32_32x32x16_bf16 v[160:175], a[96:99], a[0:3], 0\n\t"
    "v_max3_f32 v250, v128, v129, v130\n\t"
    "v_accvgpr_read_b32 v14, a158\n\t"
    "v_accvgpr_read_b32 v15, a159\n\t"
    "v_mul_f32 v8, v243, v8\n\t"
    "v_max3_f32 v251, v131, v132, v133\n\t"
    "v_mul_f32 v9, v243, v9\n\t"
    "v_mul_f32 v10, v243, v10\n\t"
    "v_max3_f32 v250, v250, v134, v135\n\t"
    "v_mul_f32 v11, v243, v11\n\t"
    "v_mul_f32 v12, v243, v12\n\t"
    "v_max3_f32 v251, v251, v136, v137\n\t"
    "v_mul_f32 v13, v243, v13\n\t"
    "v_mul_f32 v14, v243, v14\n\t"
    "v_mfma_f32_32x32x16_bf16 v[176:191], a[96:99], a[48:51], 0\n\t"
    "v_max3_f32 v250, v250, v138, v139\n\t"
    "v_mul_f32 v15, v243, v15\n\t"
    "v_cvt_pk_bf16_f32 v8, v8, v9\n\t"
    "v_max3_f32 v251, v251, v140, v141\n\t"
    "v_cvt_pk_bf16_f32 v9, v10, v11\n\t"
    "v_cvt_pk_bf16_f32 v10, v12, v13\n\t"
    "v_max3_f32 v250, v250, v142, v143\n\t"
    "v_cvt_pk_bf16_f32 v11, v14, v15\n\t"
    "v_accvgpr_write_b32 a228, v8\n\t"
    "v_max_f32 v250, v250, v251\n\t"
    "v_accvgpr_write_b32 a229, v9\n\t"
    "v_accvgpr_write_b32 a230, v10\n\t"
    "s_waitcnt lgkmcnt(9)\n\t"
    "v_mfma_f32_32x32x16_bf16 v[160:175], a[100:103], a[4:7], v[160:175]\n\t"
    "v_mov_b32 v251, v250\n\t"
    "s_nop 1\n\t"
    "v_permlane32_swap_b32 v250, v251\n\t"
    "v_accvgpr_write_b32 a231, v11\n\t"
    "v_accvgpr_read_b32 v16, a160\n\t"
    "v_max_f32 v250, v250, v251\n\t"
    "v_accvgpr_read_b32 v17, a161\n\t"
    "v_accvgpr_read_b32 v18, a162\n\t"
    "v_mul_f32_e64 v224, s63, -v250\n\t"
    "v_accvgpr_read_b32 v19, a163\n\t"
    "v_accvgpr_read_b32 v20, a164\n\t"
    "v_fma_f32 v216, v128, s63, v224\n\t"
    "v_accvgpr_read_b32 v21, a165\n\t"
    "v_mfma_f32_32x32x16_bf16 v[176:191], a[100:103], a[52:55], v[176:191]\n\t"
    "v_accvgpr_read_b32 v22, a166\n\t"
    "v_fma_f32 v217, v129, s63, v224\n\t"
    "v_accvgpr_read_b32 v23, a167\n\t"
    "v_mul_f32 v16, v243, v16\n\t"
    "v_exp_f32 v216, v216\n\t"
    "v_mul_f32 v17, v243, v17\n\t"
    "v_mul_f32 v18, v243, v18\n\t"
    "s_waitcnt vmcnt(12) lgkmcnt(0)\n\t"
    "s_barrier\n\t"
    "v_mfma_f32_32x32x16_bf16 v[160:175], a[104:107], a[8:11], v[160:175]\n\t"
    "ds_read_b64_tr_b16 a[96:97], v234 offset:0\n\t"
    "ds_read_b64_tr_b16 a[98:99], v234 offset:3072\n\t"
    "s_add_u32 m0, s46, 12288\n\t"
    "s_nop 0\n\t"
    "buffer_load_dwordx4 v235, s[16:19], s32 offen lds\n\t"
    "v_mul_f32 v19, v243, v19\n\t"
    "v_exp_f32 v217, v217\n\t"
    "v_mul_f32 v20, v243, v20\n\t"
    "v_mul_f32 v21, v243, v21\n\t"
    "v_fma_f32 v218, v130, s63, v224\n\t"
    "v_mul_f32 v22, v243, v22\n\t"
    "v_mul_f32 v23, v243, v23\n\t"
    "v_mfma_f32_32x32x16_bf16 v[176:191], a[104:107], a[56:59], v[176:191]\n\t"
    "ds_read_b64_tr_b16 a[100:101], v234 offset:512\n\t"
    "ds_read_b64_tr_b16 a[102:103], v234 offset:3584\n\t"
    "v_fma_f32 v219, v131, s63, v224\n\t"
    "v_cvt_pk_bf16_f32 v16, v16, v17\n\t"
    "v_cvt_pk_bf16_f32 v17, v18, v19\n\t"
    "v_exp_f32 v218, v218\n\t"
    "v_cvt_pk_bf16_f32 v18, v20, v21\n\t"
    "v_cvt_pk_bf16_f32 v19, v22, v23\n\t"
    "v_exp_f32 v219, v219\n\t"
    "v_accvgpr_write_b32 a232, v16\n\t"
    "v_accvgpr_write_b32 a233, v17\n\t"
    "v_mfma_f32_32x32x16_bf16 v[160:175], a[108:111], a[12:15], v[160:175]\n\t"
    "ds_read_b64_tr_b16 a[104:105], v234 offset:1024\n\t"
    "ds_read_b64_tr_b16 a[106:107], v234 offset:4096\n\t"
    "v_cvt_pk_bf16_f32 v192, v216, v217\n\t"
    "v_accvgpr_write_b32 a234, v18\n\t"
    "v_accvgpr_write_b32 a235, v19\n\t"
    "v_add_f32 v228, v216, v217\n\t"
    "v_accvgpr_read_b32 v24, a168\n\t"
    "v_accvgpr_read_b32 v25, a169\n\t"
    "v_fma_f32 v216, v132, s63, v224\n\t"
    "v_accvgpr_read_b32 v26, a170\n\t"
    "v_accvgpr_read_b32 v27, a171\n\t"
    "v_fma_f32 v217, v133, s63, v224\n\t"
    "v_mfma_f32_32x32x16_bf16 v[176:191], a[108:111], a[60:63], v[176:191]\n\t"
    "ds_read_b64_tr_b16 a[108:109], v234 offset:1536\n\t"
    "ds_read_b64_tr_b16 a[110:111], v234 offset:4608\n\t"
    "v_accvgpr_read_b32 v28, a172\n\t"
    "v_accvgpr_read_b32 v29, a173\n\t"
    "v_exp_f32 v216, v216\n\t"
    "v_accvgpr_read_b32 v30, a174\n\t"
    "v_accvgpr_read_b32 v31, a175\n\t"
    "v_exp_f32 v217, v217\n\t"
    "v_mul_f32 v24, v243, v24\n\t"
    "v_mul_f32 v25, v243, v25\n\t"
    "v_cvt_pk_bf16_f32 v193, v218, v219\n\t"
    "v_mfma_f32_32x32x16_bf16 v[160:175], a[112:115], a[16:19], v[160:175]\n\t"
    "buffer_load_dwordx4 v236, s[16:19], s32 offen offset:1024 lds\n\t"
    "v_mul_f32 v26, v243, v26\n\t"
    "v_mul_f32 v27, v243, v27\n\t"
    "v_add_f32 v228, v228, v218\n\t"
    "v_mul_f32 v28, v243, v28\n\t"
    "v_mul_f32 v29, v243, v29\n\t"
    "v_mul_f32 v30, v243, v30\n\t"
    "v_add_f32 v228, v228, v219\n\t"
    "v_mul_f32 v31, v243, v31\n\t"
    "v_cvt_pk_bf16_f32 v24, v24, v25\n\t"
    "v_fma_f32 v218, v134, s63, v224\n\t"
    "v_cvt_pk_bf16_f32 v25, v26, v27\n\t"
    "v_mfma_f32_32x32x16_bf16 v[176:191], a[112:115], a[64:67], v[176:191]\n\t"
    "ds_read_b64_tr_b16 a[112:113], v234 offset:2048\n\t"
    "ds_read_b64_tr_b16 a[114:115], v234 offset:5120\n\t"
    "v_cvt_pk_bf16_f32 v26, v28, v29\n\t"
    "v_fma_f32 v219, v135, s63, v224\n\t"
    "v_cvt_pk_bf16_f32 v27, v30, v31\n\t"
    "v_accvgpr_write_b32 a236, v24\n\t"
    "v_exp_f32 v218, v218\n\t"
    "v_accvgpr_write_b32 a237, v25\n\t"
    "v_accvgpr_write_b32 a238, v26\n\t"
    "v_exp_f32 v219, v219\n\t"
    "v_accvgpr_write_b32 a239, v27\n\t"
    "v_mfma_f32_32x32x16_bf16 v[160:175], a[116:119], a[20:23], v[160:175]\n\t"
    "v_accvgpr_read_b32 v32, a176\n\t"
    "v_cvt_pk_bf16_f32 v194, v216, v217\n\t"
    "v_accvgpr_read_b32 v33, a177\n\t"
    "v_accvgpr_read_b32 v34, a178\n\t"
    "v_add_f32 v228, v228, v216\n\t"
    "v_accvgpr_read_b32 v35, a179\n\t"
    "v_accvgpr_read_b32 v36, a180\n\t"
    "v_add_f32 v228, v228, v217\n\t"
    "v_accvgpr_read_b32 v37, a181\n\t"
    "v_accvgpr_read_b32 v38, a182\n\t"
    "s_nop 0\n\t"
    "v_accvgpr_read_b32 v39, a183\n\t"
    "v_mul_f32 v32, v243, v32\n\t"
    "v_mfma_f32_32x32x16_bf16 v[176:191], a[116:119], a[68:71], v[176:191]\n\t"
    "ds_read_b64_tr_b16 a[116:117], v234 offset:2560\n\t"
    "ds_read_b64_tr_b16 a[118:119], v234 offset:5632\n\t"
    "v_cvt_pk_bf16_f32 v195, v218, v219\n\t"
    "v_mul_f32 v33, v243, v33\n\t"
    "v_mul_f32 v34, v243, v34\n\t"
    "v_add_f32 v228, v228, v218\n\t"
    "v_mul_f32 v35, v243, v35\n\t"
    "v_mul_f32 v36, v243, v36\n\t"
    "v_add_f32 v228, v228, v219\n\t"
    "v_mul_f32 v37, v243, v37\n\t"
    "v_mul_f32 v38, v243, v38\n\t"
    "v_max3_f32 v250, v144, v145, v146\n\t"
    "v_mfma_f32_32x32x16_bf16 v[160:175], a[120:123], a[24:27], v[160:175]\n\t"
    "buffer_load_dwordx4 v237, s[16:19], s32 offen offset:2048 lds\n\t"
    "s_add_u32 s32, s32, 12288\n\t"
    "v_mul_f32 v39, v243, v39\n\t"
    "v_cvt_pk_bf16_f32 v32, v32, v33\n\t"
    "v_cvt_pk_bf16_f32 v33, v34, v35\n\t"
    "v_max3_f32 v251, v147, v148, v149\n\t"
    "v_cvt_pk_bf16_f32 v34, v36, v37\n\t"
    "v_cvt_pk_bf16_f32 v35, v38, v39\n\t"
    "v_max3_f32 v250, v250, v150, v151\n\t"
    "v_accvgpr_write_b32 a240, v32\n\t"
    "v_accvgpr_write_b32 a241, v33\n\t"
    "v_max3_f32 v251, v251, v152, v153\n\t"
    "v_accvgpr_write_b32 a242, v34\n\t"
    "v_mfma_f32_32x32x16_bf16 v[176:191], a[120:123], a[72:75], v[176:191]\n\t"
    "ds_read_b64_tr_b16 a[120:121], v234 offset:6144\n\t"
    "ds_read_b64_tr_b16 a[122:123], v234 offset:9216\n\t"
    "v_accvgpr_write_b32 a243, v35\n\t"
    "v_max3_f32 v250, v250, v154, v155\n\t"
    "v_accvgpr_read_b32 v40, a184\n\t"
    "v_accvgpr_read_b32 v41, a185\n\t"
    "v_max3_f32 v251, v251, v156, v157\n\t"
    "v_accvgpr_read_b32 v42, a186\n\t"
    "v_accvgpr_read_b32 v43, a187\n\t"
    "v_max3_f32 v250, v250, v158, v159\n\t"
    "v_accvgpr_read_b32 v44, a188\n\t"
    "v_accvgpr_read_b32 v45, a189\n\t"
    "v_mfma_f32_32x32x16_bf16 v[160:175], a[124:127], a[28:31], v[160:175]\n\t"
    "v_max_f32 v250, v250, v251\n\t"
    "v_accvgpr_read_b32 v46, a190\n\t"
    "v_accvgpr_read_b32 v47, a191\n\t"
    "v_mov_b32 v251, v250\n\t"
    "s_nop 1\n\t"
    "v_permlane32_swap_b32 v250, v251\n\t"
    "v_mul_f32 v40, v243, v40\n\t"
    "v_mul_f32 v41, v243, v41\n\t"
    "v_max_f32 v250, v250, v251\n\t"
    "v_mul_f32 v42, v243, v42\n\t"
    "v_mul_f32 v43, v243, v43\n\t"
    "v_mul_f32_e64 v225, s63, -v250\n\t"
    "v_mul_f32 v44, v243, v44\n\t"
    "v_mfma_f32_32x32x16_bf16 v[176:191], a[124:127], a[76:79], v[176:191]\n\t"
    "ds_read_b64_tr_b16 a[124:125], v234 offset:6656\n\t"
    "ds_read_b64_tr_b16 a[126:127], v234 offset:9728\n\t"
    "v_mul_f32 v45, v243, v45\n\t"
    "v_fma_f32 v216, v144, s63, v225\n\t"
    "v_mul_f32 v46, v243, v46\n\t"
    "v_mul_f32 v47, v243, v47\n\t"
    "v_fma_f32 v217, v145, s63, v225\n\t"
    "v_cvt_pk_bf16_f32 v40, v40, v41\n\t"
    "v_cvt_pk_bf16_f32 v41, v42, v43\n\t"
    "v_exp_f32 v216, v216\n\t"
    "v_cvt_pk_bf16_f32 v42, v44, v45\n\t"
    "v_cvt_pk_bf16_f32 v43, v46, v47\n\t"
    "v_mfma_f32_32x32x16_bf16 v[160:175], a[128:131], a[32:35], v[160:175]\n\t"
    "v_accvgpr_write_b32 a244, v40\n\t"
    "v_exp_f32 v217, v217\n\t"
    "v_accvgpr_write_b32 a245, v41\n\t"
    "v_accvgpr_write_b32 a246, v42\n\t"
    "v_fma_f32 v218, v146, s63, v225\n\t"
    "v_accvgpr_write_b32 a247, v43\n\t"
    "v_accvgpr_read_b32 v48, a192\n\t"
    "v_fma_f32 v219, v147, s63, v225\n\t"
    "v_accvgpr_read_b32 v49, a193\n\t"
    "v_accvgpr_read_b32 v50, a194\n\t"
    "v_exp_f32 v218, v218\n\t"
    "v_mfma_f32_32x32x16_bf16 v[176:191], a[128:131], a[80:83], v[176:191]\n\t"
    "ds_read_b64_tr_b16 a[128:129], v234 offset:7168\n\t"
    "ds_read_b64_tr_b16 a[130:131], v234 offset:10240\n\t"
    "v_accvgpr_read_b32 v51, a195\n\t"
    "v_accvgpr_read_b32 v52, a196\n\t"
    "v_exp_f32 v219, v219\n\t"
    "v_accvgpr_read_b32 v53, a197\n\t"
    "v_accvgpr_read_b32 v54, a198\n\t"
    "v_cvt_pk_bf16_f32 v196, v216, v217\n\t"
    "v_accvgpr_read_b32 v55, a199\n\t"
    "v_mul_f32 v48, v243, v48\n\t"
    "v_add_f32 v229, v216, v217\n\t"
    "v_mfma_f32_32x32x16_bf16 v[160:175], a[132:135], a[36:39], v[160:175]\n\t"
    "v_mul_f32 v49, v243, v49\n\t"
    "v_mul_f32 v50, v243, v50\n\t"
    "v_fma_f32 v216, v148, s63, v225\n\t"
    "v_mul_f32 v51, v243, v51\n\t"
    "v_mul_f32 v52, v243, v52\n\t"
    "v_fma_f32 v217, v149, s63, v225\n\t"
    "v_mul_f32 v53, v243, v53\n\t"
    "v_mul_f32 v54, v243, v54\n\t"
    "v_exp_f32 v216, v216\n\t"
    "v_mul_f32 v55, v243, v55\n\t"
    "v_cvt_pk_bf16_f32 v48, v48, v49\n\t"
    "v_mfma_f32_32x32x16_bf16 v[176:191], a[132:135], a[84:87], v[176:191]\n\t"
    "ds_read_b64_tr_b16 a[132:133], v234 offset:7680\n\t"
    "ds_read_b64_tr_b16 a[134:135], v234 offset:10752\n\t"
    "v_exp_f32 v217, v217\n\t"
    "v_cvt_pk_bf16_f32 v49, v50, v51\n\t"
    "v_cvt_pk_bf16_f32 v50, v52, v53\n\t"
    "v_cvt_pk_bf16_f32 v197, v218, v219\n\t"
    "v_cvt_pk_bf16_f32 v51, v54, v55\n\t"
    "v_accvgpr_write_b32 a248, v48\n\t"
    "v_add_f32 v229, v229, v218\n\t"
    "v_accvgpr_write_b32 a249, v49\n\t"
    "v_accvgpr_write_b32 a250, v50\n\t"
    "v_accvgpr_write_b32 a251, v51\n\t"
    "v_mfma_f32_32x32x16_bf16 v[160:175], a[136:139], a[40:43], v[160:175]\n\t"
    "v_add_f32 v229, v229, v219\n\t"
    "v_accvgpr_read_b32 v56, a200\n\t"
    "v_accvgpr_read_b32 v57, a201\n\t"
    "v_fma_f32 v218, v150, s63, v225\n\t"
    "v_accvgpr_read_b32 v58, a202\n\t"
    "v_accvgpr_read_b32 v59, a203\n\t"
    "v_fma_f32 v219, v151, s63, v225\n\t"
    "v_accvgpr_read_b32 v60, a204\n\t"
    "v_accvgpr_read_b32 v61, a205\n\t"
    "v_exp_f32 v218, v218\n\t"
    "v_accvgpr_read_b32 v62, a206\n\t"
    "v_accvgpr_read_b32 v63, a207\n\t"
    "v_mfma_f32_32x32x16_bf16 v[176:191], a[136:139], a[88:91], v[176:191]\n\t"
    "ds_read_b64_tr_b16 a[136:137], v234 offset:8192\n\t"
    "ds_read_b64_tr_b16 a[138:139], v234 offset:11264\n\t"
    "v_exp_f32 v219, v219\n\t"
    "v_mul_f32 v56, v243, v56\n\t"
    "v_mul_f32 v57, v243, v57\n\t"
    "v_cvt_pk_bf16_f32 v198, v216, v217\n\t"
    "v_mul_f32 v58, v243, v58\n\t"
    "v_mul_f32 v59, v243, v59\n\t"
    "v_add_f32 v229, v229, v216\n\t"
    "v_mul_f32 v60, v243, v60\n\t"
    "v_mul_f32 v61, v243, v61\n\t"
    "v_mfma_f32_32x32x16_bf16 v[160:175], a[140:143], a[44:47], v[160:175]\n\t"
    "v_add_f32 v229, v229, v217\n\t"
    "v_mul_f32 v62, v243, v62\n\t"
    "v_mul_f32 v63, v243, v63\n\t"
    "s_nop 0\n\t"
    "v_cvt_pk_bf16_f32 v56, v56, v57\n\t"
    "v_cvt_pk_bf16_f32 v57, v58, v59\n\t"
    "v_cvt_pk_bf16_f32 v199, v218, v219\n\t"
    "v_cvt_pk_bf16_f32 v58, v60, v61\n\t"
    "v_cvt_pk_bf16_f32 v59, v62, v63\n\t"
    "v_add_f32 v229, v229, v218\n\t"
    "v_accvgpr_write_b32 a252, v56\n\t"
    "v_accvgpr_write_b32 a253, v57\n\t"
    "v_add_f32 v229, v229, v219\n\t"
    "v_mfma_f32_32x32x16_bf16 v[176:191], a[140:143], a[92:95], v[176:191]\n\t"
    "ds_read_b64_tr_b16 a[140:141], v234 offset:8704\n\t"
    "ds_read_b64_tr_b16 a[142:143], v234 offset:11776\n\t"
    "v_accvgpr_write_b32 a254, v58\n\t"
    "v_accvgpr_write_b32 a255, v59\n\t"
    "s_waitcnt lgkmcnt(12)\n\t"
    "v_mfma_f32_32x32x16_bf16 v[0:15], a[96:99], v[192:195], 0\n\t"
    "ds_read_b128 v[220:223], v241 offset:0\n\t"
    "v_fma_f32 v216, v136, s63, v224\n\t"
    "v_fma_f32 v217, v137, s63, v224\n\t"
    "v_exp_f32 v216, v216\n\t"
    "v_exp_f32 v217, v217\n\t"
    "v_fma_f32 v218, v152, s63, v225\n\t"
    "v_mfma_f32_32x32x16_bf16 v[96:111], a[96:99], v[196:199], 0\n\t"
    "ds_read_b128 a[96:99], v230 offset:24576\n\t"
    "v_fma_f32 v219, v153, s63, v225\n\t"
    "v_exp_f32 v218, v218\n\t"
    "v_exp_f32 v219, v219\n\t"
    "v_cvt_pk_bf16_f32 v208, v216, v217\n\t"
    "v_mfma_f32_32x32x16_bf16 v[16:31], a[100:103], v[192:195], 0\n\t"
    "s_waitcnt lgkmcnt(1)\n\t"
    "buffer_store_dwordx4 v[220:223], v248, s[28:31], 0 offen nt\n\t"
    "ds_read_b128 v[252:255], v241 offset:1024\n\t"
    "v_add_f32 v228, v228, v216\n\t"
    "v_add_f32 v228, v228, v217\n\t"
    "v_fma_f32 v216, v138, s63, v224\n\t"
    "v_fma_f32 v217, v139, s63, v224\n\t"
    "v_exp_f32 v216, v216\n\t"
    "v_mfma_f32_32x32x16_bf16 v[112:127], a[100:103], v[196:199], 0\n\t"
    "ds_read_b128 a[100:103], v231 offset:24576\n\t"
    "v_exp_f32 v217, v217\n\t"
    "v_cvt_pk_bf16_f32 v212, v218, v219\n\t"
    "v_add_f32 v229, v229, v218\n\t"
    "v_add_f32 v229, v229, v219\n\t"
    "v_fma_f32 v218, v154, s63, v225\n\t"
    "v_mfma_f32_32x32x16_bf16 v[32:47], a[104:107], v[192:195], 0\n\t"
    "v_fma_f32 v219, v155, s63, v225\n\t"
    "v_exp_f32 v218, v218\n\t"
    "v_exp_f32 v219, v219\n\t"
    "v_cvt_pk_bf16_f32 v209, v216, v217\n\t"
    "v_add_f32 v228, v228, v216\n\t"
    "v_add_f32 v228, v228, v217\n\t"
    "v_mfma_f32_32x32x16_bf16 a[144:159], a[104:107], v[196:199], 0\n\t"
    "ds_read_b128 a[104:107], v232 offset:24576\n\t"
    "v_fma_f32 v216, v140, s63, v224\n\t"
    "v_fma_f32 v217, v141, s63, v224\n\t"
    "v_exp_f32 v216, v216\n\t"
    "v_exp_f32 v217, v217\n\t"
    "v_mfma_f32_32x32x16_bf16 v[48:63], a[108:111], v[192:195], 0\n\t"
    "s_waitcnt lgkmcnt(2)\n\t"
    "buffer_store_dwordx4 v[252:255], v249, s[28:31], 0 offen nt\n\t"
    "v_cvt_pk_bf16_f32 v213, v218, v219\n\t"
    "v_add_f32 v229, v229, v218\n\t"
    "v_add_f32 v229, v229, v219\n\t"
    "v_fma_f32 v218, v156, s63, v225\n\t"
    "v_fma_f32 v219, v157, s63, v225\n\t"
    "v_exp_f32 v218, v218\n\t"
    "v_mfma_f32_32x32x16_bf16 a[160:175], a[108:111], v[196:199], 0\n\t"
    "ds_read_b128 a[108:111], v233 offset:24576\n\t"
    "v_exp_f32 v219, v219\n\t"
    "v_cvt_pk_bf16_f32 v210, v216, v217\n\t"
    "v_add_f32 v228, v228, v216\n\t"
    "v_add_f32 v228, v228, v217\n\t"
    "v_fma_f32 v216, v142, s63, v224\n\t"
    "v_mfma_f32_32x32x16_bf16 v[64:79], a[112:115], v[192:195], 0\n\t"
    "ds_read_b128 v[220:223], v241 offset:2048\n\t"
    "v_fma_f32 v217, v143, s63, v224\n\t"
    "v_exp_f32 v216, v216\n\t"
    "v_exp_f32 v217, v217\n\t"
    "v_cvt_pk_bf16_f32 v214, v218, v219\n\t"
    "v_add_f32 v229, v229, v218\n\t"
    "v_mfma_f32_32x32x16_bf16 a[176:191], a[112:115], v[196:199], 0\n\t"
    "ds_read_b128 a[112:115], v230 offset:25600\n\t"
    "v_add_f32 v229, v229, v219\n\t"
    "v_fma_f32 v218, v158, s63, v225\n\t"
    "v_fma_f32 v219, v159, s63, v225\n\t"
    "v_exp_f32 v218, v218\n\t"
    "v_exp_f32 v219, v219\n\t"
    "v_mfma_f32_32x32x16_bf16 v[80:95], a[116:119], v[192:195], 0\n\t"
    "v_cvt_pk_bf16_f32 v211, v216, v217\n\t"
    "v_add_f32 v228, v228, v216\n\t"
    "v_add_f32 v228, v228, v217\n\t"
    "s_nop 0\n\t"
    "v_cvt_pk_bf16_f32 v215, v218, v219\n\t"
    "v_add_f32 v229, v229, v218\n\t"
    "v_add_f32 v229, v229, v219\n\t"
    "v_mfma_f32_32x32x16_bf16 a[192:207], a[116:119], v[196:199], 0\n\t"
    "ds_read_b128 a[116:119], v231 offset:25600\n\t"
    "v_mov_b32 v226, v228\n\t"
    "v_mov_b32 v227, v229\n\t"
    "v_fma_f32 v216, v160, s63, v224\n\t"
    "v_fma_f32 v217, v161, s63, v224\n\t"
    "v_exp_f32 v216, v216\n\t"
    "v_mfma_f32_32x32x16_bf16 v[0:15], a[120:123], v[208:211], v[0:15]\n\t"
    "s_waitcnt lgkmcnt(2)\n\t"
    "buffer_store_dwordx4 v[220:223], v248, s[28:31], s86 offen nt\n\t"
    "v_exp_f32 v217, v217\n\t"
    "v_fma_f32 v218, v176, s63, v225\n\t"
    "v_fma_f32 v219, v177, s63, v225\n\t"
    "v_exp_f32 v218, v218\n\t"
    "v_exp_f32 v219, v219\n\t"
    "v_mfma_f32_32x32x16_bf16 v[96:111], a[120:123], v[212:215], v[96:111]\n\t"
    "ds_read_b128 a[120:123], v232 offset:25600\n\t"
    "v_cvt_pk_bf16_f32 v200, v216, v217\n\t"
    "v_add_f32 v228, v216, v217\n\t"
    "v_fma_f32 v216, v162, s63, v224\n\t"
    "v_fma_f32 v217, v163, s63, v224\n\t"
    "v_exp_f32 v216, v216\n\t"
    "v_mfma_f32_32x32x16_bf16 v[16:31], a[124:127], v[208:211], v[16:31]\n\t"
    "ds_read_b128 v[252:255], v241 offset:3072\n\t"
    "v_exp_f32 v217, v217\n\t"
    "v_cvt_pk_bf16_f32 v204, v218, v219\n\t"
    "v_add_f32 v229, v218, v219\n\t"
    "v_fma_f32 v218, v178, s63, v225\n\t"
    "v_fma_f32 v219, v179, s63, v225\n\t"
    "v_mfma_f32_32x32x16_bf16 v[112:127], a[124:127], v[212:215], v[112:127]\n\t"
    "ds_read_b128 a[124:127], v233 offset:25600\n\t"
    "ds_write_b128 v244, a[208:211] offset:0\n\t"
    "v_exp_f32 v218, v218\n\t"
    "v_exp_f32 v219, v219\n\t"
    "v_cvt_pk_bf16_f32 v201, v216, v217\n\t"
    "v_add_f32 v228, v228, v216\n\t"
    "v_mfma_f32_32x32x16_bf16 v[32:47], a[128:131], v[208:211], v[32:47]\n\t"
    "v_add_f32 v228, v228, v217\n\t"
    "v_fma_f32 v216, v164, s63, v224\n\t"
    "v_fma_f32 v217, v165, s63, v224\n\t"
    "v_exp_f32 v216, v216\n\t"
    "v_exp_f32 v217, v217\n\t"
    "v_mfma_f32_32x32x16_bf16 a[144:159], a[128:131], v[212:215], a[144:159]\n\t"
    "ds_read_b128 a[128:131], v230 offset:26624\n\t"
    "ds_write_b128 v245, a[212:215] offset:0\n\t"
    "v_cvt_pk_bf16_f32 v205, v218, v219\n\t"
    "v_add_f32 v229, v229, v218\n\t"
    "v_add_f32 v229, v229, v219\n\t"
    "v_fma_f32 v218, v180, s63, v225\n\t"
    "v_fma_f32 v219, v181, s63, v225\n\t"
    "v_mfma_f32_32x32x16_bf16 v[48:63], a[132:135], v[208:211], v[48:63]\n\t"
    "s_waitcnt lgkmcnt(4)\n\t"
    "buffer_store_dwordx4 v[252:255], v249, s[28:31], s86 offen nt\n\t"
    "v_exp_f32 v218, v218\n\t"
    "v_exp_f32 v219, v219\n\t"
    "v_cvt_pk_bf16_f32 v202, v216, v217\n\t"
    "v_add_f32 v228, v228, v216\n\t"
    "v_add_f32 v228, v228, v217\n\t"
    "v_mfma_f32_32x32x16_bf16 a[160:175], a[132:135], v[212:215], a[160:175]\n\t"
    "ds_read_b128 a[132:135], v231 offset:26624\n\t"
    "ds_write_b128 v246, a[216:219] offset:0\n\t"
    "v_fma_f32 v216, v166, s63, v224\n\t"
    "v_fma_f32 v217, v167, s63, v224\n\t"
    "v_exp_f32 v216, v216\n\t"
    "v_exp_f32 v217, v217\n\t"
    "v_mfma_f32_32x32x16_bf16 v[64:79], a[136:139], v[208:211], v[64:79]\n\t"
    "v_cvt_pk_bf16_f32 v206, v218, v219\n\t"
    "v_add_f32 v229, v229, v218\n\t"
    "v_add_f32 v229, v229, v219\n\t"
    "v_fma_f32 v218, v182, s63, v225\n\t"
    "v_fma_f32 v219, v183, s63, v225\n\t"
    "v_exp_f32 v218, v218\n\t"
    "v_mfma_f32_32x32x16_bf16 a[176:191], a[136:139], v[212:215], a[176:191]\n\t"
    "ds_read_b128 a[136:139], v232 offset:26624\n\t"
    "ds_write_b128 v247, a[220:223] offset:0\n\t"
    "v_exp_f32 v219, v219\n\t"
    "v_cvt_pk_bf16_f32 v203, v216, v217\n\t"
    "v_add_f32 v228, v228, v216\n\t"
    "v_add_f32 v228, v228, v217\n\t"
    "s_nop 0\n\t"
    "v_mfma_f32_32x32x16_bf16 v[80:95], a[140:143], v[208:211], v[80:95]\n\t"
    "v_cvt_pk_bf16_f32 v207, v218, v219\n\t"
    "v_add_f32 v229, v229, v218\n\t"
    "v_add_f32 v229, v229, v219\n\t"
    "v_mfma_f32_32x32x16_bf16 a[192:207], a[140:143], v[212:215], a[192:207]\n\t"
    "ds_read_b128 a[140:143], v233 offset:26624\n\t"
    "ds_read_b128 v[220:223], v241 offset:0\n\t"
    "v_mfma_f32_32x32x16_bf16 v[128:143], a[96:99], a[0:3], 0\n\t"
    "v_fma_f32 v216, v168, s63, v224\n\t"
    "v_fma_f32 v217, v169, s63, v224\n\t"
    "v_exp_f32 v216, v216\n\t"
    "v_mfma_f32_32x32x16_bf16 v[144:159], a[96:99], a[48:51], 0\n\t"
    "s_waitcnt lgkmcnt(0)\n\t"
    "buffer_store_dwordx4 v[220:223], v248, s[28:31], s87 offen nt\n\t"
    "v_exp_f32 v217, v217\n\t"
    "v_fma_f32 v218, v184, s63, v225\n\t"
    "v_fma_f32 v219, v185, s63, v225\n\t"
    "v_mfma_f32_32x32x16_bf16 v[128:143], a[100:103], a[4:7], v[128:143]\n\t"
    "v_exp_f32 v218, v218\n\t"
    "v_exp_f32 v219, v219\n\t"
    "v_cvt_pk_bf16_f32 v208, v216, v217\n\t"
    "v_mfma_f32_32x32x16_bf16 v[144:159], a[100:103], a[52:55], v[144:159]\n\t"
    "v_add_f32 v228, v228, v216\n\t"
    "s_waitcnt vmcnt(17) lgkmcnt(0)\n\t"
    "s_barrier\n\t"
    "v_mfma_f32_32x32x16_bf16 v[128:143], a[104:107], a[8:11], v[128:143]\n\t"
    "ds_read_b64_tr_b16 a[96:97], v234 offset:12288\n\t"
    "ds_read_b64_tr_b16 a[98:99], v234 offset:15360\n\t"
    "s_mov_b32 m0, s49\n\t"
    "s_nop 0\n\t"
    "buffer_load_dwordx4 v238, s[20:23], s34 offen lds\n\t"
    "v_mfma_f32_32x32x16_bf16 v[144:159], a[104:107], a[56:59], v[144:159]\n\t"
    "ds_read_b64_tr_b16 a[100:101], v234 offset:12800\n\t"
    "ds_read_b64_tr_b16 a[102:103], v234 offset:15872\n\t"
    "v_add_f32 v228, v228, v217\n\t"
    "v_fma_f32 v216, v170, s63, v224\n\t"
    "v_fma_f32 v217, v171, s63, v224\n\t"
    "v_mfma_f32_32x32x16_bf16 v[128:143], a[108:111], a[12:15], v[128:143]\n\t"
    "ds_read_b64_tr_b16 a[104:105], v234 offset:13312\n\t"
    "ds_read_b64_tr_b16 a[106:107], v234 offset:16384\n\t"
    "v_exp_f32 v216, v216\n\t"
    "v_mfma_f32_32x32x16_bf16 v[144:159], a[108:111], a[60:63], v[144:159]\n\t"
    "ds_read_b64_tr_b16 a[108:109], v234 offset:13824\n\t"
    "ds_read_b64_tr_b16 a[110:111], v234 offset:16896\n\t"
    "v_exp_f32 v217, v217\n\t"
    "v_cvt_pk_bf16_f32 v212, v218, v219\n\t"
    "v_mfma_f32_32x32x16_bf16 v[128:143], a[112:115], a[16:19], v[128:143]\n\t"
    "buffer_load_dwordx4 v239, s[20:23], s34 offen offset:1024 lds\n\t"
    "v_add_f32 v229, v229, v218\n\t"
    "v_add_f32 v229, v229, v219\n\t"
    "v_fma_f32 v218, v186, s63, v225\n\t"
    "v_fma_f32 v219, v187, s63, v225\n\t"
    "v_mfma_f32_32x32x16_bf16 v[144:159], a[112:115], a[64:67], v[144:159]\n\t"
    "ds_read_b64_tr_b16 a[112:113], v234 offset:14336\n\t"
    "ds_read_b64_tr_b16 a[114:115], v234 offset:17408\n\t"
    "v_exp_f32 v218, v218\n\t"
    "v_mfma_f32_32x32x16_bf16 v[128:143], a[116:119], a[20:23], v[128:143]\n\t"
    "v_exp_f32 v219, v219\n\t"
    "v_cvt_pk_bf16_f32 v209, v216, v217\n\t"
    "v_add_f32 v228, v228, v216\n\t"
    "v_add_f32 v228, v228, v217\n\t"
    "v_fma_f32 v216, v172, s63, v224\n\t"
    "v_mfma_f32_32x32x16_bf16 v[144:159], a[116:119], a[68:71], v[144:159]\n\t"
    "ds_read_b64_tr_b16 a[116:117], v234 offset:14848\n\t"
    "ds_read_b64_tr_b16 a[118:119], v234 offset:17920\n\t"
    "v_fma_f32 v217, v173, s63, v224\n\t"
    "v_exp_f32 v216, v216\n\t"
    "v_mfma_f32_32x32x16_bf16 v[128:143], a[120:123], a[24:27], v[128:143]\n\t"
    "buffer_load_dwordx4 v240, s[20:23], s34 offen offset:2048 lds\n\t"
    "s_add_u32 s34, s34, 12288\n\t"
    "v_exp_f32 v217, v217\n\t"
    "v_mfma_f32_32x32x16_bf16 v[144:159], a[120:123], a[72:75], v[144:159]\n\t"
    "ds_read_b64_tr_b16 a[120:121], v234 offset:18432\n\t"
    "ds_read_b64_tr_b16 a[122:123], v234 offset:21504\n\t"
    "v_cvt_pk_bf16_f32 v213, v218, v219\n\t"
    "v_add_f32 v229, v229, v218\n\t"
    "v_add_f32 v229, v229, v219\n\t"
    "v_mfma_f32_32x32x16_bf16 v[128:143], a[124:127], a[28:31], v[128:143]\n\t"
    "v_fma_f32 v218, v188, s63, v225\n\t"
    "v_fma_f32 v219, v189, s63, v225\n\t"
    "v_exp_f32 v218, v218\n\t"
    "v_mfma_f32_32x32x16_bf16 v[144:159], a[124:127], a[76:79], v[144:159]\n\t"
    "ds_read_b64_tr_b16 a[124:125], v234 offset:18944\n\t"
    "ds_read_b64_tr_b16 a[126:127], v234 offset:22016\n\t"
    "v_exp_f32 v219, v219\n\t"
    "v_cvt_pk_bf16_f32 v210, v216, v217\n\t"
    "v_add_f32 v228, v228, v216\n\t"
    "v_mfma_f32_32x32x16_bf16 v[128:143], a[128:131], a[32:35], v[128:143]\n\t"
    "s_add_u32 m0, s46, 24576\n\t"
    "s_nop 0\n\t"
    "buffer_load_dwordx4 v235, s[16:19], s32 offen lds\n\t"
    "v_add_f32 v228, v228, v217\n\t"
    "v_fma_f32 v216, v174, s63, v224\n\t"
    "v_mfma_f32_32x32x16_bf16 v[144:159], a[128:131], a[80:83], v[144:159]\n\t"
    "ds_read_b64_tr_b16 a[128:129], v234 offset:19456\n\t"
    "ds_read_b64_tr_b16 a[130:131], v234 offset:22528\n\t"
    "v_fma_f32 v217, v175, s63, v224\n\t"
    "v_exp_f32 v216, v216\n\t"
    "v_mfma_f32_32x32x16_bf16 v[128:143], a[132:135], a[36:39], v[128:143]\n\t"
    "v_exp_f32 v217, v217\n\t"
    "v_cvt_pk_bf16_f32 v214, v218, v219\n\t"
    "v_add_f32 v229, v229, v218\n\t"
    "v_mfma_f32_32x32x16_bf16 v[144:159], a[132:135], a[84:87], v[144:159]\n\t"
    "ds_read_b64_tr_b16 a[132:133], v234 offset:19968\n\t"
    "ds_read_b64_tr_b16 a[134:135], v234 offset:23040\n\t"
    "v_add_f32 v229, v229, v219\n\t"
    "v_fma_f32 v218, v190, s63, v225\n\t"
    "v_fma_f32 v219, v191, s63, v225\n\t"
    "v_mfma_f32_32x32x16_bf16 v[128:143], a[136:139], a[40:43], v[128:143]\n\t"
    "buffer_load_dwordx4 v236, s[16:19], s32 offen offset:1024 lds\n\t"
    "v_exp_f32 v218, v218\n\t"
    "v_exp_f32 v219, v219\n\t"
    "v_mfma_f32_32x32x16_bf16 v[144:159], a[136:139], a[88:91], v[144:159]\n\t"
    "ds_read_b64_tr_b16 a[136:137], v234 offset:20480\n\t"
    "ds_read_b64_tr_b16 a[138:139], v234 offset:23552\n\t"
    "v_cvt_pk_bf16_f32 v211, v216, v217\n\t"
    "v_add_f32 v228, v228, v216\n\t"
    "v_add_f32 v228, v228, v217\n\t"
    "v_mfma_f32_32x32x16_bf16 v[128:143], a[140:143], a[44:47], v[128:143]\n\t"
    "s_nop 0\n\t"
    "v_cvt_pk_bf16_f32 v215, v218, v219\n\t"
    "v_add_f32 v229, v229, v218\n\t"
    "v_add_f32 v229, v229, v219\n\t"
    "v_mfma_f32_32x32x16_bf16 v[144:159], a[140:143], a[92:95], v[144:159]\n\t"
    "ds_read_b64_tr_b16 a[140:141], v234 offset:20992\n\t"
    "ds_read_b64_tr_b16 a[142:143], v234 offset:24064\n\t"
    "v_add_f32 v251, v228, v229\n\t"
    "v_cmp_nge_f32 vcc, s64, v251\n\t"
    "s_cbranch_vccnz .Ld_stub0\n\t"
    ".Ld_back0:\n\t"
    "s_waitcnt lgkmcnt(12)\n\t"
    "v_mfma_f32_32x32x16_bf16 v[0:15], a[96:99], v[200:203], v[0:15]\n\t"
    "buffer_load_dwordx4 v237, s[16:19], s32 offen offset:2048 lds\n\t"
    "s_add_u32 s32, s32, 12288\n\t"
    "v_add_f32 v226, v226, v228\n\t"
    "v_add_f32 v227, v227, v229\n\t"
    "v_mfma_f32_32x32x16_bf16 v[96:111], a[96:99], v[204:207], v[96:111]\n\t"
    "ds_read_b128 a[96:99], v230 offset:36864\n\t"
    "v_mfma_f32_32x32x16_bf16 v[16:31], a[100:103], v[200:203], v[16:31]\n\t"
    "v_mfma_f32_32x32x16_bf16 v[112:127], a[100:103], v[204:207], v[112:127]\n\t"
    "ds_read_b128 a[100:103], v231 offset:36864\n\t"
    "v_fma_f32 v216, v128, s63, v224\n\t"
    "v_fma_f32 v217, v129, s63, v224\n\t"
    "v_exp_f32 v216, v216\n\t"
    "v_mfma_f32_32x32x16_bf16 v[32:47], a[104:107], v[200:203], v[32:47]\n\t"
    "v_exp_f32 v217, v217\n\t"
    "v_fma_f32 v218, v144, s63, v225\n\t"
    "v_mfma_f32_32x32x16_bf16 a[144:159], a[104:107], v[204:207], a[144:159]\n\t"
    "ds_read_b128 a[104:107], v232 offset:36864\n\t"
    "v_fma_f32 v219, v145, s63, v225\n\t"
    "v_exp_f32 v218, v218\n\t"
    "v_mfma_f32_32x32x16_bf16 v[48:63], a[108:111], v[200:203], v[48:63]\n\t"
    "ds_read_b128 v[252:255], v241 offset:1024\n\t"
    "v_exp_f32 v219, v219\n\t"
    "v_cvt_pk_bf16_f32 v192, v216, v217\n\t"
    "v_add_f32 v228, v216, v217\n\t"
    "v_mfma_f32_32x32x16_bf16 a[160:175], a[108:111], v[204:207], a[160:175]\n\t"
    "ds_read_b128 a[108:111], v233 offset:36864\n\t"
    "v_fma_f32 v216, v130, s63, v224\n\t"
    "v_fma_f32 v217, v131, s63, v224\n\t"
    "v_exp_f32 v216, v216\n\t"
    "v_mfma_f32_32x32x16_bf16 v[64:79], a[112:115], v[200:203], v[64:79]\n\t"
    "v_exp_f32 v217, v217\n\t"
    "v_cvt_pk_bf16_f32 v196, v218, v219\n\t"
    "v_mfma_f32_32x32x16_bf16 a[176:191], a[112:115], v[204:207], a[176:191]\n\t"
    "ds_read_b128 a[112:115], v230 offset:37888\n\t"
    "v_add_f32 v229, v218, v219\n\t"
    "v_fma_f32 v218, v146, s63, v225\n\t"
    "v_fma_f32 v219, v147, s63, v225\n\t"
    "v_mfma_f32_32x32x16_bf16 v[80:95], a[116:119], v[200:203], v[80:95]\n\t"
    "s_waitcnt lgkmcnt(2)\n\t"
    "buffer_store_dwordx4 v[252:255], v249, s[28:31], s87 offen nt\n\t"
    "v_exp_f32 v218, v218\n\t"
    "v_exp_f32 v219, v219\n\t"
    "v_mfma_f32_32x32x16_bf16 a[192:207], a[116:119], v[204:207], a[192:207]\n\t"
    "ds_read_b128 a[116:119], v231 offset:37888\n\t"
    "v_cvt_pk_bf16_f32 v193, v216, v217\n\t"
    "v_add_f32 v228, v228, v216\n\t"
    "v_add_f32 v228, v228, v217\n\t"
    "v_mfma_f32_32x32x16_bf16 v[0:15], a[120:123], v[208:211], v[0:15]\n\t"
    "ds_read_b128 v[220:223], v241 offset:2048\n\t"
    "v_fma_f32 v216, v132, s63, v224\n\t"
    "v_fma_f32 v217, v133, s63, v224\n\t"
    "v_exp_f32 v216, v216\n\t"
    "v_mfma_f32_32x32x16_bf16 v[96:111], a[120:123], v[212:215], v[96:111]\n\t"
    "ds_read_b128 a[120:123], v232 offset:37888\n\t"
    "v_exp_f32 v217, v217\n\t"
    "v_cvt_pk_bf16_f32 v197, v218, v219\n\t"
    "v_mfma_f32_32x32x16_bf16 v[16:31], a[124:127], v[208:211], v[16:31]\n\t"
    "v_add_f32 v229, v229, v218\n\t"
    "v_add_f32 v229, v229, v219\n\t"
    "v_fma_f32 v218, v148, s63, v225\n\t"
    "v_fma_f32 v219, v149, s63, v225\n\t"
    "v_mfma_f32_32x32x16_bf16 v[112:127], a[124:127], v[212:215], v[112:127]\n\t"
    "ds_read_b128 a[124:127], v233 offset:37888\n\t"
    "v_exp_f32 v218, v218\n\t"
    "v_exp_f32 v219, v219\n\t"
    "v_mfma_f32_32x32x16_bf16 v[32:47], a[128:131], v[208:211], v[32:47]\n\t"
    "s_waitcnt lgkmcnt(2)\n\t"
    "buffer_store_dwordx4 v[220:223], v248, s[28:31], s43 offen nt\n\t"
    "v_cvt_pk_bf16_f32 v194, v216, v217\n\t"
    "v_add_f32 v228, v228, v216\n\t"
    "v_mfma_f32_32x32x16_bf16 a[144:159], a[128:131], v[212:215], a[144:159]\n\t"
    "ds_read_b128 a[128:131], v230 offset:38912\n\t"
    "v_add_f32 v228, v228, v217\n\t"
    "v_fma_f32 v216, v134, s63, v224\n\t"
    "v_fma_f32 v217, v135, s63, v224\n\t"
    "v_mfma_f32_32x32x16_bf16 v[48:63], a[132:135], v[208:211], v[48:63]\n\t"
    "ds_read_b128 v[252:255], v241 offset:3072\n\t"
    "v_exp_f32 v216, v216\n\t"
    "v_exp_f32 v217, v217\n\t"
    "v_mfma_f32_32x32x16_bf16 a[160:175], a[132:135], v[212:215], a[160:175]\n\t"
    "ds_read_b128 a[132:135], v231 offset:38912\n\t"
    "v_cvt_pk_bf16_f32 v198, v218, v219\n\t"
    "v_add_f32 v229, v229, v218\n\t"
    "v_add_f32 v229, v229, v219\n\t"
    "v_mfma_f32_32x32x16_bf16 v[64:79], a[136:139], v[208:211], v[64:79]\n\t"
    "v_fma_f32 v218, v150, s63, v225\n\t"
    "v_fma_f32 v219, v151, s63, v225\n\t"
    "v_exp_f32 v218, v218\n\t"
    "v_mfma_f32_32x32x16_bf16 a[176:191], a[136:139], v[212:215], a[176:191]\n\t"
    "ds_read_b128 a[136:139], v232 offset:38912\n\t"
    "v_exp_f32 v219, v219\n\t"
    "v_cvt_pk_bf16_f32 v195, v216, v217\n\t"
    "v_add_f32 v228, v228, v216\n\t"
    "v_mfma_f32_32x32x16_bf16 v[80:95], a[140:143], v[208:211], v[80:95]\n\t"
    "s_waitcnt lgkmcnt(2)\n\t"
    "buffer_store_dwordx4 v[252:255], v249, s[28:31], s43 offen nt\n\t"
    "v_add_f32 v228, v228, v217\n\t"
    "s_nop 0\n\t"
    "v_cvt_pk_bf16_f32 v199, v218, v219\n\t"
    "v_mfma_f32_32x32x16_bf16 a[192:207], a[140:143], v[212:215], a[192:207]\n\t"
    "ds_read_b128 a[140:143], v233 offset:38912\n\t"
    "v_add_f32 v229, v229, v218\n\t"
    "v_add_f32 v229, v229, v219\n\t"
    "v_mfma_f32_32x32x16_bf16 v[160:175], a[96:99], a[0:3], 0\n\t"
    "v_fma_f32 v216, v136, s63, v224\n\t"
    "v_fma_f32 v217, v137, s63, v224\n\t"
    "v_exp_f32 v216, v216\n\t"
    "v_mfma_f32_32x32x16_bf16 v[176:191], a[96:99], a[48:51], 0\n\t"
    "v_exp_f32 v217, v217\n\t"
    "v_fma_f32 v218, v152, s63, v225\n\t"
    "v_fma_f32 v219, v153, s63, v225\n\t"
    "v_exp_f32 v218, v218\n\t"
    "v_mfma_f32_32x32x16_bf16 v[160:175], a[100:103], a[4:7], v[160:175]\n\t"
    "v_exp_f32 v219, v219\n\t"
    "v_cvt_pk_bf16_f32 v208, v216, v217\n\t"
    "v_add_f32 v228, v228, v216\n\t"
    "v_mfma_f32_32x32x16_bf16 v[176:191], a[100:103], a[52:55], v[176:191]\n\t"
    "v_add_f32 v228, v228, v217\n\t"
    "s_waitcnt vmcnt(17) lgkmcnt(0)\n\t"
    "s_barrier\n\t"
    "v_mfma_f32_32x32x16_bf16 v[160:175], a[104:107], a[8:11], v[160:175]\n\t"
    "ds_read_b64_tr_b16 a[96:97], v234 offset:24576\n\t"
    "ds_read_b64_tr_b16 a[98:99], v234 offset:27648\n\t"
    "s_add_u32 m0, s49, 12288\n\t"
    "s_nop 0\n\t"
    "buffer_load_dwordx4 v238, s[20:23], s34 offen lds\n\t"
    "v_mfma_f32_32x32x16_bf16 v[176:191], a[104:107], a[56:59], v[176:191]\n\t"
    "ds_read_b64_tr_b16 a[100:101], v234 offset:25088\n\t"
    "ds_read_b64_tr_b16 a[102:103], v234 offset:28160\n\t"
    "v_fma_f32 v216, v138, s63, v224\n\t"
    "v_fma_f32 v217, v139, s63, v224\n\t"
    "v_mfma_f32_32x32x16_bf16 v[160:175], a[108:111], a[12:15], v[160:175]\n\t"
    "ds_read_b64_tr_b16 a[104:105], v234 offset:25600\n\t"
    "ds_read_b64_tr_b16 a[106:107], v234 offset:28672\n\t"
    "v_exp_f32 v216, v216\n\t"
    "v_exp_f32 v217, v217\n\t"
    "v_mfma_f32_32x32x16_bf16 v[176:191], a[108:111], a[60:63], v[176:191]\n\t"
    "ds_read_b64_tr_b16 a[108:109], v234 offset:26112\n\t"
    "ds_read_b64_tr_b16 a[110:111], v234 offset:29184\n\t"
    "v_cvt_pk_bf16_f32 v212, v218, v219\n\t"
    "v_add_f32 v229, v229, v218\n\t"
    "v_mfma_f32_32x32x16_bf16 v[160:175], a[112:115], a[16:19], v[160:175]\n\t"
    "buffer_load_dwordx4 v239, s[20:23], s34 offen offset:1024 lds\n\t"
    "v_add_f32 v229, v229, v219\n\t"
    "v_fma_f32 v218, v154, s63, v225\n\t"
    "v_fma_f32 v219, v155, s63, v225\n\t"
    "v_mfma_f32_32x32x16_bf16 v[176:191], a[112:115], a[64:67], v[176:191]\n\t"
    "ds_read_b64_tr_b16 a[112:113], v234 offset:26624\n\t"
    "ds_read_b64_tr_b16 a[114:115], v234 offset:29696\n\t"
    "v_exp_f32 v218, v218\n\t"
    "v_exp_f32 v219, v219\n\t"
    "v_mfma_f32_32x32x16_bf16 v[160:175], a[116:119], a[20:23], v[160:175]\n\t"
    "v_cvt_pk_bf16_f32 v209, v216, v217\n\t"
    "v_add_f32 v228, v228, v216\n\t"
    "v_add_f32 v228, v228, v217\n\t"
    "v_fma_f32 v216, v140, s63, v224\n\t"
    "v_mfma_f32_32x32x16_bf16 v[176:191], a[116:119], a[68:71], v[176:191]\n\t"
    "ds_read_b64_tr_b16 a[116:117], v234 offset:27136\n\t"
    "ds_read_b64_tr_b16 a[118:119], v234 offset:30208\n\t"
    "v_fma_f32 v217, v141, s63, v224\n\t"
    "v_exp_f32 v216, v216\n\t"
    "v_mfma_f32_32x32x16_bf16 v[160:175], a[120:123], a[24:27], v[160:175]\n\t"
    "buffer_load_dwordx4 v240, s[20:23], s34 offen offset:2048 lds\n\t"
    "s_add_u32 s34, s34, 12288\n\t"
    "v_exp_f32 v217, v217\n\t"
    "v_cvt_pk_bf16_f32 v213, v218, v219\n\t"
    "v_mfma_f32_32x32x16_bf16 v[176:191], a[120:123], a[72:75], v[176:191]\n\t"
    "ds_read_b64_tr_b16 a[120:121], v234 offset:30720\n\t"
    "ds_read_b64_tr_b16 a[122:123], v234 offset:33792\n\t"
    "v_add_f32 v229, v229, v218\n\t"
    "v_add_f32 v229, v229, v219\n\t"
    "v_mfma_f32_32x32x16_bf16 v[160:175], a[124:127], a[28:31], v[160:175]\n\t"
    "v_fma_f32 v218, v156, s63, v225\n\t"
    "v_fma_f32 v219, v157, s63, v225\n\t"
    "v_exp_f32 v218, v218\n\t"
    "v_exp_f32 v219, v219\n\t"
    "v_mfma_f32_32x32x16_bf16 v[176:191], a[124:127], a[76:79], v[176:191]\n\t"
    "ds_read_b64_tr_b16 a[124:125], v234 offset:31232\n\t"
    "ds_read_b64_tr_b16 a[126:127], v234 offset:34304\n\t"
    "v_cvt_pk_bf16_f32 v210, v216, v217\n\t"
    "v_add_f32 v228, v228, v216\n\t"
    "v_mfma_f32_32x32x16_bf16 v[160:175], a[128:131], a[32:35], v[160:175]\n\t"
    "s_add_u32 m0, s46, 36864\n\t"
    "s_nop 0\n\t"
    "buffer_load_dwordx4 v235, s[16:19], s32 offen lds\n\t"
    "v_add_f32 v228, v228, v217\n\t"
    "v_fma_f32 v216, v142, s63, v224\n\t"
    "v_mfma_f32_32x32x16_bf16 v[176:191], a[128:131], a[80:83], v[176:191]\n\t"
    "ds_read_b64_tr_b16 a[128:129], v234 offset:31744\n\t"
    "ds_read_b64_tr_b16 a[130:131], v234 offset:34816\n\t"
    "v_fma_f32 v217, v143, s63, v224\n\t"
    "v_exp_f32 v216, v216\n\t"
    "v_mfma_f32_32x32x16_bf16 v[160:175], a[132:135], a[36:39], v[160:175]\n\t"
    "v_exp_f32 v217, v217\n\t"
    "v_cvt_pk_bf16_f32 v214, v218, v219\n\t"
    "v_add_f32 v229, v229, v218\n\t"
    "v_mfma_f32_32x32x16_bf16 v[176:191], a[132:135], a[84:87], v[176:191]\n\t"
    "ds_read_b64_tr_b16 a[132:133], v234 offset:32256\n\t"
    "ds_read_b64_tr_b16 a[134:135], v234 offset:35328\n\t"
    "v_add_f32 v229, v229, v219\n\t"
    "v_fma_f32 v218, v158, s63, v225\n\t"
    "v_fma_f32 v219, v159, s63, v225\n\t"
    "v_mfma_f32_32x32x16_bf16 v[160:175], a[136:139], a[40:43], v[160:175]\n\t"
    "buffer_load_dwordx4 v236, s[16:19], s32 offen offset:1024 lds\n\t"
    "v_exp_f32 v218, v218\n\t"
    "v_exp_f32 v219, v219\n\t"
    "v_mfma_f32_32x32x16_bf16 v[176:191], a[136:139], a[88:91], v[176:191]\n\t"
    "ds_read_b64_tr_b16 a[136:137], v234 offset:32768\n\t"
    "ds_read_b64_tr_b16 a[138:139], v234 offset:35840\n\t"
    "v_cvt_pk_bf16_f32 v211, v216, v217\n\t"
    "v_add_f32 v228, v228, v216\n\t"
    "v_mfma_f32_32x32x16_bf16 v[160:175], a[140:143], a[44:47], v[160:175]\n\t"
    "v_add_f32 v228, v228, v217\n\t"
    "s_nop 0\n\t"
    "v_cvt_pk_bf16_f32 v215, v218, v219\n\t"
    "v_add_f32 v229, v229, v218\n\t"
    "v_add_f32 v229, v229, v219\n\t"
    "v_mfma_f32_32x32x16_bf16 v[176:191], a[140:143], a[92:95], v[176:191]\n\t"
    "ds_read_b64_tr_b16 a[140:141], v234 offset:33280\n\t"
    "ds_read_b64_tr_b16 a[142:143], v234 offset:36352\n\t"
    "v_add_f32 v251, v228, v229\n\t"
    "v_cmp_nge_f32 vcc, s64, v251\n\t"
    "s_cbranch_vccnz .Ld_stub1\n\t"
    ".Ld_back1:\n\t"
    "s_waitcnt lgkmcnt(12)\n\t"
    "v_mfma_f32_32x32x16_bf16 v[0:15], a[96:99], v[192:195], v[0:15]\n\t"
    "buffer_load_dwordx4 v237, s[16:19], s32 offen offset:2048 lds\n\t"
    "s_add_u32 s32, s32, 12288\n\t"
    "v_add_f32 v226, v226, v228\n\t"
    "v_add_f32 v227, v227, v229\n\t"
    "v_mfma_f32_32x32x16_bf16 v[96:111], a[96:99], v[196:199], v[96:111]\n\t"
    "ds_read_b128 a[96:99], v230 offset:0\n\t"
    "v_mfma_f32_32x32x16_bf16 v[16:31], a[100:103], v[192:195], v[16:31]\n\t"
    "v_mfma_f32_32x32x16_bf16 v[112:127], a[100:103], v[196:199], v[112:127]\n\t"
    "ds_read_b128 a[100:103], v231 offset:0\n\t"
    "v_fma_f32 v216, v160, s63, v224\n\t"
    "v_fma_f32 v217, v161, s63, v224\n\t"
    "v_exp_f32 v216, v216\n\t"
    "v_mfma_f32_32x32x16_bf16 v[32:47], a[104:107], v[192:195], v[32:47]\n\t"
    "s_add_u32 m0, s61, 0\n\t"
    "s_nop 0\n\t"
    "buffer_load_dwordx4 v248, s[24:27], s87 offen nt lds\n\t"
    "v_mfma_f32_32x32x16_bf16 a[144:159], a[104:107], v[196:199], a[144:159]\n\t"
    "ds_read_b128 a[104:107], v232 offset:0\n\t"
    "v_exp_f32 v217, v217\n\t"
    "v_fma_f32 v218, v176, s63, v225\n\t"
    "v_fma_f32 v219, v177, s63, v225\n\t"
    "v_mfma_f32_32x32x16_bf16 v[48:63], a[108:111], v[192:195], v[48:63]\n\t"
    "v_exp_f32 v218, v218\n\t"
    "v_exp_f32 v219, v219\n\t"
    "v_cvt_pk_bf16_f32 v200, v216, v217\n\t"
    "v_mfma_f32_32x32x16_bf16 a[160:175], a[108:111], v[196:199], a[160:175]\n\t"
    "ds_read_b128 a[108:111], v233 offset:0\n\t"
    "v_add_f32 v228, v216, v217\n\t"
    "v_fma_f32 v216, v162, s63, v224\n\t"
    "v_fma_f32 v217, v163, s63, v224\n\t"
    "v_mfma_f32_32x32x16_bf16 v[64:79], a[112:115], v[192:195], v[64:79]\n\t"
    "v_exp_f32 v216, v216\n\t"
    "v_exp_f32 v217, v217\n\t"
    "v_mfma_f32_32x32x16_bf16 a[176:191], a[112:115], v[196:199], a[176:191]\n\t"
    "ds_read_b128 a[112:115], v230 offset:1024\n\t"
    "v_cvt_pk_bf16_f32 v204, v218, v219\n\t"
    "v_add_f32 v229, v218, v219\n\t"
    "v_fma_f32 v218, v178, s63, v225\n\t"
    "v_fma_f32 v219, v179, s63, v225\n\t"
    "v_mfma_f32_32x32x16_bf16 v[80:95], a[116:119], v[192:195], v[80:95]\n\t"
    "s_add_u32 m0, s61, 1024\n\t"
    "s_nop 0\n\t"
    "buffer_load_dwordx4 v249, s[24:27], s87 offen nt lds\n\t"
    "v_exp_f32 v218, v218\n\t"
    "v_mfma_f32_32x32x16_bf16 a[192:207], a[116:119], v[196:199], a[192:207]\n\t"
    "ds_read_b128 a[116:119], v231 offset:1024\n\t"
    "v_exp_f32 v219, v219\n\t"
    "v_cvt_pk_bf16_f32 v201, v216, v217\n\t"
    "s_waitcnt lgkmcnt(10)\n\t"
    "v_mfma_f32_32x32x16_bf16 v[0:15], a[120:123], v[208:211], v[0:15]\n\t"
    "v_add_f32 v228, v228, v216\n\t"
    "v_add_f32 v228, v228, v217\n\t"
    "v_fma_f32 v216, v164, s63, v224\n\t"
    "v_fma_f32 v217, v165, s63, v224\n\t"
    "v_mfma_f32_32x32x16_bf16 v[96:111], a[120:123], v[212:215], v[96:111]\n\t"
    "ds_read_b128 a[120:123], v232 offset:1024\n\t"
    "v_exp_f32 v216, v216\n\t"
    "v_exp_f32 v217, v217\n\t"
    "v_mfma_f32_32x32x16_bf16 v[16:31], a[124:127], v[208:211], v[16:31]\n\t"
    "v_cvt_pk_bf16_f32 v205, v218, v219\n\t"
    "v_add_f32 v229, v229, v218\n\t"
    "v_add_f32 v229, v229, v219\n\t"
    "v_fma_f32 v218, v180, s63, v225\n\t"
    "v_mfma_f32_32x32x16_bf16 v[112:127], a[124:127], v[212:215], v[112:127]\n\t"
    "ds_read_b128 a[124:127], v233 offset:1024\n\t"
    "v_fma_f32 v219, v181, s63, v225\n\t"
    "v_exp_f32 v218, v218\n\t"
    "v_mfma_f32_32x32x16_bf16 v[32:47], a[128:131], v[208:211], v[32:47]\n\t"
    "v_exp_f32 v219, v219\n\t"
    "v_cvt_pk_bf16_f32 v202, v216, v217\n\t"
    "v_add_f32 v228, v228, v216\n\t"
    "v_add_f32 v228, v228, v217\n\t"
    "v_mfma_f32_32x32x16_bf16 a[144:159], a[128:131], v[212:215], a[144:159]\n\t"
    "ds_read_b128 a[128:131], v230 offset:2048\n\t"
    "v_fma_f32 v216, v166, s63, v224\n\t"
    "v_fma_f32 v217, v167, s63, v224\n\t"
    "v_mfma_f32_32x32x16_bf16 v[48:63], a[132:135], v[208:211], v[48:63]\n\t"
    "v_exp_f32 v216, v216\n\t"
    "v_exp_f32 v217, v217\n\t"
    "v_cvt_pk_bf16_f32 v206, v218, v219\n\t"
    "v_mfma_f32_32x32x16_bf16 a[160:175], a[132:135], v[212:215], a[160:175]\n\t"
    "ds_read_b128 a[132:135], v231 offset:2048\n\t"
    "s_add_u32 m0, s61, 2048\n\t"
    "s_nop 0\n\t"
    "buffer_load_dwordx4 v248, s[24:27], s43 offen nt lds\n\t"
    "v_add_f32 v229, v229, v218\n\t"
    "s_waitcnt lgkmcnt(10)\n\t"
    "v_mfma_f32_32x32x16_bf16 v[64:79], a[136:139], v[208:211], v[64:79]\n\t"
    "v_add_f32 v229, v229, v219\n\t"
    "v_fma_f32 v218, v182, s63, v225\n\t"
    "v_fma_f32 v219, v183, s63, v225\n\t"
    "v_exp_f32 v218, v218\n\t"
    "v_mfma_f32_32x32x16_bf16 a[176:191], a[136:139], v[212:215], a[176:191]\n\t"
    "ds_read_b128 a[136:139], v232 offset:2048\n\t"
    "v_exp_f32 v219, v219\n\t"
    "v_cvt_pk_bf16_f32 v203, v216, v217\n\t"
    "v_mfma_f32_32x32x16_bf16 v[80:95], a[140:143], v[208:211], v[80:95]\n\t"
    "v_add_f32 v228, v228, v216\n\t"
    "v_add_f32 v228, v228, v217\n\t"
    "s_nop 0\n\t"
    "v_cvt_pk_bf16_f32 v207, v218, v219\n\t"
    "v_mfma_f32_32x32x16_bf16 a[192:207], a[140:143], v[212:215], a[192:207]\n\t"
    "ds_read_b128 a[140:143], v233 offset:2048\n\t"
    "v_add_f32 v229, v229, v218\n\t"
    "v_add_f32 v229, v229, v219\n\t"
    "s_waitcnt lgkmcnt(10)\n\t"
    "v_mfma_f32_32x32x16_bf16 v[128:143], a[96:99], a[0:3], 0\n\t"
    "v_fma_f32 v216, v168, s63, v224\n\t"
    "v_fma_f32 v217, v169, s63, v224\n\t"
    "v_exp_f32 v216, v216\n\t"
    "v_mfma_f32_32x32x16_bf16 v[144:159], a[96:99], a[48:51], 0\n\t"
    "s_add_u32 m0, s61, 3072\n\t"
    "s_nop 0\n\t"
    "buffer_load_dwordx4 v249, s[24:27], s43 offen nt lds\n\t"
    "v_exp_f32 v217, v217\n\t"
    "v_fma_f32 v218, v184, s63, v225\n\t"
    "s_waitcnt lgkmcnt(9)\n\t"
    "v_mfma_f32_32x32x16_bf16 v[128:143], a[100:103], a[4:7], v[128:143]\n\t"
    "v_fma_f32 v219, v185, s63, v225\n\t"
    "v_exp_f32 v218, v218\n\t"
    "v_exp_f32 v219, v219\n\t"
    "v_mfma_f32_32x32x16_bf16 v[144:159], a[100:103], a[52:55], v[144:159]\n\t"
    "v_cvt_pk_bf16_f32 v208, v216, v217\n\t"
    "s_waitcnt vmcnt(24) lgkmcnt(0)\n\t"
    "s_barrier\n\t"
    "v_mfma_f32_32x32x16_bf16 v[128:143], a[104:107], a[8:11], v[128:143]\n\t"
    "ds_read_b64_tr_b16 a[96:97], v234 offset:36864\n\t"
    "ds_read_b64_tr_b16 a[98:99], v234 offset:39936\n\t"
    "s_add_u32 m0, s49, 24576\n\t"
    "s_nop 0\n\t"
    "buffer_load_dwordx4 v238, s[20:23], s34 offen lds\n\t"
    "v_mfma_f32_32x32x16_bf16 v[144:159], a[104:107], a[56:59], v[144:159]\n\t"
    "ds_read_b64_tr_b16 a[100:101], v234 offset:37376\n\t"
    "ds_read_b64_tr_b16 a[102:103], v234 offset:40448\n\t"
    "v_add_f32 v228, v228, v216\n\t"
    "v_add_f32 v228, v228, v217\n\t"
    "v_mfma_f32_32x32x16_bf16 v[128:143], a[108:111], a[12:15], v[128:143]\n\t"
    "ds_read_b64_tr_b16 a[104:105], v234 offset:37888\n\t"
    "ds_read_b64_tr_b16 a[106:107], v234 offset:40960\n\t"
    "v_fma_f32 v216, v170, s63, v224\n\t"
    "v_fma_f32 v217, v171, s63, v224\n\t"
    "v_exp_f32 v216, v216\n\t"
    "v_mfma_f32_32x32x16_bf16 v[144:159], a[108:111], a[60:63], v[144:159]\n\t"
    "ds_read_b64_tr_b16 a[108:109], v234 offset:38400\n\t"
    "ds_read_b64_tr_b16 a[110:111], v234 offset:41472\n\t"
    "v_exp_f32 v217, v217\n\t"
    "v_mfma_f32_32x32x16_bf16 v[128:143], a[112:115], a[16:19], v[128:143]\n\t"
    "buffer_load_dwordx4 v239, s[20:23], s34 offen offset:1024 lds\n\t"
    "v_cvt_pk_bf16_f32 v212, v218, v219\n\t"
    "v_add_f32 v229, v229, v218\n\t"
    "v_add_f32 v229, v229, v219\n\t"
    "v_fma_f32 v218, v186, s63, v225\n\t"
    "v_mfma_f32_32x32x16_bf16 v[144:159], a[112:115], a[64:67], v[144:159]\n\t"
    "ds_read_b64_tr_b16 a[112:113], v234 offset:38912\n\t"
    "ds_read_b64_tr_b16 a[114:115], v234 offset:41984\n\t"
    "v_fma_f32 v219, v187, s63, v225\n\t"
    "v_exp_f32 v218, v218\n\t"
    "v_mfma_f32_32x32x16_bf16 v[128:143], a[116:119], a[20:23], v[128:143]\n\t"
    "v_exp_f32 v219, v219\n\t"
    "v_cvt_pk_bf16_f32 v209, v216, v217\n\t"
    "v_add_f32 v228, v228, v216\n\t"
    "v_add_f32 v228, v228, v217\n\t"
    "v_mfma_f32_32x32x16_bf16 v[144:159], a[116:119], a[68:71], v[144:159]\n\t"
    "ds_read_b64_tr_b16 a[116:117], v234 offset:39424\n\t"
    "ds_read_b64_tr_b16 a[118:119], v234 offset:42496\n\t"
    "v_fma_f32 v216, v172, s63, v224\n\t"
    "v_fma_f32 v217, v173, s63, v224\n\t"
    "v_mfma_f32_32x32x16_bf16 v[128:143], a[120:123], a[24:27], v[128:143]\n\t"
    "buffer_load_dwordx4 v240, s[20:23], s34 offen offset:2048 lds\n\t"
    "s_add_u32 s34, s34, 12288\n\t"
    "v_exp_f32 v216, v216\n\t"
    "v_exp_f32 v217, v217\n\t"
    "v_mfma_f32_32x32x16_bf16 v[144:159], a[120:123], a[72:75], v[144:159]\n\t"
    "ds_read_b64_tr_b16 a[120:121], v234 offset:43008\n\t"
    "ds_read_b64_tr_b16 a[122:123], v234 offset:46080\n\t"
    "v_cvt_pk_bf16_f32 v213, v218, v219\n\t"
    "v_add_f32 v229, v229, v218\n\t"
    "v_add_f32 v229, v229, v219\n\t"
    "v_mfma_f32_32x32x16_bf16 v[128:143], a[124:127], a[28:31], v[128:143]\n\t"
    "v_fma_f32 v218, v188, s63, v225\n\t"
    "v_fma_f32 v219, v189, s63, v225\n\t"
    "v_exp_f32 v218, v218\n\t"
    "v_mfma_f32_32x32x16_bf16 v[144:159], a[124:127], a[76:79], v[144:159]\n\t"
    "ds_read_b64_tr_b16 a[124:125], v234 offset:43520\n\t"
    "ds_read_b64_tr_b16 a[126:127], v234 offset:46592\n\t"
    "v_exp_f32 v219, v219\n\t"
    "v_cvt_pk_bf16_f32 v210, v216, v217\n\t"
    "v_mfma_f32_32x32x16_bf16 v[128:143], a[128:131], a[32:35], v[128:143]\n\t"
    "s_mov_b32 m0, s46\n\t"
    "s_nop 0\n\t"
    "buffer_load_dwordx4 v235, s[16:19], s32 offen lds\n\t"
    "v_add_f32 v228, v228, v216\n\t"
    "v_add_f32 v228, v228, v217\n\t"
    "v_mfma_f32_32x32x16_bf16 v[144:159], a[128:131], a[80:83], v[144:159]\n\t"
    "ds_read_b64_tr_b16 a[128:129], v234 offset:44032\n\t"
    "ds_read_b64_tr_b16 a[130:131], v234 offset:47104\n\t"
    "v_fma_f32 v216, v174, s63, v224\n\t"
    "v_fma_f32 v217, v175, s63, v224\n\t"
    "v_exp_f32 v216, v216\n\t"
    "v_mfma_f32_32x32x16_bf16 v[128:143], a[132:135], a[36:39], v[128:143]\n\t"
    "v_exp_f32 v217, v217\n\t"
    "v_cvt_pk_bf16_f32 v214, v218, v219\n\t"
    "v_add_f32 v229, v229, v218\n\t"
    "v_mfma_f32_32x32x16_bf16 v[144:159], a[132:135], a[84:87], v[144:159]\n\t"
    "ds_read_b64_tr_b16 a[132:133], v234 offset:44544\n\t"
    "ds_read_b64_tr_b16 a[134:135], v234 offset:47616\n\t"
    "v_add_f32 v229, v229, v219\n\t"
    "v_fma_f32 v218, v190, s63, v225\n\t"
    "v_fma_f32 v219, v191, s63, v225\n\t"
    "v_mfma_f32_32x32x16_bf16 v[128:143], a[136:139], a[40:43], v[128:143]\n\t"
    "buffer_load_dwordx4 v236, s[16:19], s32 offen offset:1024 lds\n\t"
    "v_exp_f32 v218, v218\n\t"
    "v_exp_f32 v219, v219\n\t"
    "v_mfma_f32_32x32x16_bf16 v[144:159], a[136:139], a[88:91], v[144:159]\n\t"
    "ds_read_b64_tr_b16 a[136:137], v234 offset:45056\n\t"
    "ds_read_b64_tr_b16 a[138:139], v234 offset:48128\n\t"
    "v_cvt_pk_bf16_f32 v211, v216, v217\n\t"
    "v_add_f32 v228, v228, v216\n\t"
    "v_add_f32 v228, v228, v217\n\t"
    "v_mfma_f32_32x32x16_bf16 v[128:143], a[140:143], a[44:47], v[128:143]\n\t"
    "s_nop 0\n\t"
    "v_cvt_pk_bf16_f32 v215, v218, v219\n\t"
    "v_add_f32 v229, v229, v218\n\t"
    "v_add_f32 v229, v229, v219\n\t"
    "v_add_f32 v251, v228, v229\n\t"
    "v_mfma_f32_32x32x16_bf16 v[144:159], a[140:143], a[92:95], v[144:159]\n\t"
    "ds_read_b64_tr_b16 a[140:141], v234 offset:45568\n\t"
    "ds_read_b64_tr_b16 a[142:143], v234 offset:48640\n\t"
    "v_cmp_nge_f32 vcc, s64, v251\n\t"
    "s_cbranch_vccnz .Ld_stub2\n\t"
    ".Ld_back2:\n\t"
    "s_waitcnt lgkmcnt(12)\n\t"
    "v_mfma_f32_32x32x16_bf16 v[0:15], a[96:99], v[200:203], v[0:15]\n\t"
    "buffer_load_dwordx4 v237, s[16:19], s32 offen offset:2048 lds\n\t"
    "s_add_u32 s32, s32, 12288\n\t"
    "v_add_f32 v226, v226, v228\n\t"
    "v_add_f32 v227, v227, v229\n\t"
    "v_mfma_f32_32x32x16_bf16 v[96:111], a[96:99], v[204:207], v[96:111]\n\t"
    "ds_read_b128 a[96:99], v230 offset:12288\n\t"
    "v_mfma_f32_32x32x16_bf16 v[16:31], a[100:103], v[200:203], v[16:31]\n\t"
    "v_mfma_f32_32x32x16_bf16 v[112:127], a[100:103], v[204:207], v[112:127]\n\t"
    "ds_read_b128 a[100:103], v231 offset:12288\n\t"
    "ds_read_b128 v[220:223], v241 offset:4096\n\t"
    "v_fma_f32 v216, v128, s63, v224\n\t"
    "v_fma_f32 v217, v129, s63, v224\n\t"
    "v_mfma_f32_32x32x16_bf16 v[32:47], a[104:107], v[200:203], v[32:47]\n\t"
    "v_exp_f32 v216, v216\n\t"
    "v_exp_f32 v217, v217\n\t"
    "v_mfma_f32_32x32x16_bf16 a[144:159], a[104:107], v[204:207], a[144:159]\n\t"
    "ds_read_b128 a[104:107], v232 offset:12288\n\t"
    "v_fma_f32 v218, v144, s63, v225\n\t"
    "v_fma_f32 v219, v145, s63, v225\n\t"
    "v_exp_f32 v218, v218\n\t"
    "v_mfma_f32_32x32x16_bf16 v[48:63], a[108:111], v[200:203], v[48:63]\n\t"
    "v_exp_f32 v219, v219\n\t"
    "v_cvt_pk_bf16_f32 v192, v216, v217\n\t"
    "v_add_f32 v228, v216, v217\n\t"
    "v_mfma_f32_32x32x16_bf16 a[160:175], a[108:111], v[204:207], a[160:175]\n\t"
    "s_waitcnt lgkmcnt(1)\n\t"
    "buffer_store_dwordx4 v[220:223], v248, s[28:31], 0 offen offset:128 nt\n\t"
    "ds_read_b128 a[108:111], v233 offset:12288\n\t"
    "v_fma_f32 v216, v130, s63, v224\n\t"
    "v_fma_f32 v217, v131, s63, v224\n\t"
    "v_mfma_f32_32x32x16_bf16 v[64:79], a[112:115], v[200:203], v[64:79]\n\t"
    "v_exp_f32 v216, v216\n\t"
    "v_exp_f32 v217, v217\n\t"
    "v_mfma_f32_32x32x16_bf16 a[176:191], a[112:115], v[204:207], a[176:191]\n\t"
    "ds_read_b128 a[112:115], v230 offset:13312\n\t"
    "ds_read_b128 v[252:255], v241 offset:5120\n\t"
    "v_cvt_pk_bf16_f32 v196, v218, v219\n\t"
    "v_add_f32 v229, v218, v219\n\t"
    "v_fma_f32 v218, v146, s63, v225\n\t"
    "v_mfma_f32_32x32x16_bf16 v[80:95], a[116:119], v[200:203], v[80:95]\n\t"
    "v_fma_f32 v219, v147, s63, v225\n\t"
    "v_exp_f32 v218, v218\n\t"
    "v_mfma_f32_32x32x16_bf16 a[192:207], a[116:119], v[204:207], a[192:207]\n\t"
    "ds_read_b128 a[116:119], v231 offset:13312\n\t"
    "v_exp_f32 v219, v219\n\t"
    "v_cvt_pk_bf16_f32 v193, v216, v217\n\t"
    "v_add_f32 v228, v228, v216\n\t"
    "v_mfma_f32_32x32x16_bf16 v[0:15], a[120:123], v[208:211], v[0:15]\n\t"
    "v_add_f32 v228, v228, v217\n\t"
    "v_fma_f32 v216, v132, s63, v224\n\t"
    "v_fma_f32 v217, v133, s63, v224\n\t"
    "v_exp_f32 v216, v216\n\t"
    "v_mfma_f32_32x32x16_bf16 v[96:111], a[120:123], v[212:215], v[96:111]\n\t"
    "s_waitcnt lgkmcnt(1)\n\t"
    "buffer_store_dwordx4 v[252:255], v249, s[28:31], 0 offen offset:128 nt\n\t"
    "ds_read_b128 a[120:123], v232 offset:13312\n\t"
    "v_exp_f32 v217, v217\n\t"
    "v_mfma_f32_32x32x16_bf16 v[16:31], a[124:127], v[208:211], v[16:31]\n\t"
    "v_cvt_pk_bf16_f32 v197, v218, v219\n\t"
    "v_add_f32 v229, v229, v218\n\t"
    "v_add_f32 v229, v229, v219\n\t"
    "v_fma_f32 v218, v148, s63, v225\n\t"
    "v_mfma_f32_32x32x16_bf16 v[112:127], a[124:127], v[212:215], v[112:127]\n\t"
    "ds_read_b128 a[124:127], v233 offset:13312\n\t"
    "ds_read_b128 v[220:223], v241 offset:6144\n\t"
    "v_fma_f32 v219, v149, s63, v225\n\t"
    "v_exp_f32 v218, v218\n\t"
    "v_mfma_f32_32x32x16_bf16 v[32:47], a[128:131], v[208:211], v[32:47]\n\t"
    "v_exp_f32 v219, v219\n\t"
    "v_cvt_pk_bf16_f32 v194, v216, v217\n\t"
    "v_mfma_f32_32x32x16_bf16 a[144:159], a[128:131], v[212:215], a[144:159]\n\t"
    "ds_read_b128 a[128:131], v230 offset:14336\n\t"
    "v_add_f32 v228, v228, v216\n\t"
    "v_add_f32 v228, v228, v217\n\t"
    "v_fma_f32 v216, v134, s63, v224\n\t"
    "v_fma_f32 v217, v135, s63, v224\n\t"
    "v_mfma_f32_32x32x16_bf16 v[48:63], a[132:135], v[208:211], v[48:63]\n\t"
    "v_exp_f32 v216, v216\n\t"
    "v_exp_f32 v217, v217\n\t"
    "v_mfma_f32_32x32x16_bf16 a[160:175], a[132:135], v[212:215], a[160:175]\n\t"
    "s_waitcnt lgkmcnt(1)\n\t"
    "buffer_store_dwordx4 v[220:223], v248, s[28:31], s86 offen offset:128 nt\n\t"
    "ds_read_b128 a[132:135], v231 offset:14336\n\t"
    "v_cvt_pk_bf16_f32 v198, v218, v219\n\t"
    "v_add_f32 v229, v229, v218\n\t"
    "v_mfma_f32_32x32x16_bf16 v[64:79], a[136:139], v[208:211], v[64:79]\n\t"
    "v_add_f32 v229, v229, v219\n\t"
    "v_fma_f32 v218, v150, s63, v225\n\t"
    "v_fma_f32 v219, v151, s63, v225\n\t"
    "v_exp_f32 v218, v218\n\t"
    "v_mfma_f32_32x32x16_bf16 a[176:191], a[136:139], v[212:215], a[176:191]\n\t"
    "ds_read_b128 a[136:139], v232 offset:14336\n\t"
    "v_exp_f32 v219, v219\n\t"
    "v_cvt_pk_bf16_f32 v195, v216, v217\n\t"
    "v_mfma_f32_32x32x16_bf16 v[80:95], a[140:143], v[208:211], v[80:95]\n\t"
    "v_add_f32 v228, v228, v216\n\t"
    "v_add_f32 v228, v228, v217\n\t"
    "s_nop 0\n\t"
    "v_cvt_pk_bf16_f32 v199, v218, v219\n\t"
    "v_mfma_f32_32x32x16_bf16 a[192:207], a[140:143], v[212:215], a[192:207]\n\t"
    "ds_read_b128 a[140:143], v233 offset:14336\n\t"
    "v_add_f32 v229, v229, v218\n\t"
    "v_add_f32 v229, v229, v219\n\t"
    "v_mfma_f32_32x32x16_bf16 v[160:175], a[96:99], a[0:3], 0\n\t"
    "v_fma_f32 v216, v136, s63, v224\n\t"
    "v_fma_f32 v217, v137, s63, v224\n\t"
    "v_exp_f32 v216, v216\n\t"
    "v_mfma_f32_32x32x16_bf16 v[176:191], a[96:99], a[48:51], 0\n\t"
    "v_exp_f32 v217, v217\n\t"
    "v_fma_f32 v218, v152, s63, v225\n\t"
    "v_fma_f32 v219, v153, s63, v225\n\t"
    "v_exp_f32 v218, v218\n\t"
    "v_mfma_f32_32x32x16_bf16 v[160:175], a[100:103], a[4:7], v[160:175]\n\t"
    "v_exp_f32 v219, v219\n\t"
    "v_cvt_pk_bf16_f32 v208, v216, v217\n\t"
    "v_add_f32 v228, v228, v216\n\t"
    "v_mfma_f32_32x32x16_bf16 v[176:191], a[100:103], a[52:55], v[176:191]\n\t"
    "v_add_f32 v228, v228, v217\n\t"
    "s_waitcnt vmcnt(22) lgkmcnt(0)\n\t"
    "s_barrier\n\t"
    "v_mfma_f32_32x32x16_bf16 v[160:175], a[104:107], a[8:11], v[160:175]\n\t"
    "ds_read_b64_tr_b16 a[96:97], v234 offset:0\n\t"
    "ds_read_b64_tr_b16 a[98:99], v234 offset:3072\n\t"
    "s_add_u32 m0, s49, 36864\n\t"
    "s_nop 0\n\t"
    "buffer_load_dwordx4 v238, s[20:23], s34 offen lds\n\t"
    "v_mfma_f32_32x32x16_bf16 v[176:191], a[104:107], a[56:59], v[176:191]\n\t"
    "ds_read_b64_tr_b16 a[100:101], v234 offset:512\n\t"
    "ds_read_b64_tr_b16 a[102:103], v234 offset:3584\n\t"
    "v_fma_f32 v216, v138, s63, v224\n\t"
    "v_fma_f32 v217, v139, s63, v224\n\t"
    "v_mfma_f32_32x32x16_bf16 v[160:175], a[108:111], a[12:15], v[160:175]\n\t"
    "ds_read_b64_tr_b16 a[104:105], v234 offset:1024\n\t"
    "ds_read_b64_tr_b16 a[106:107], v234 offset:4096\n\t"
    "v_exp_f32 v216, v216\n\t"
    "v_exp_f32 v217, v217\n\t"
    "v_mfma_f32_32x32x16_bf16 v[176:191], a[108:111], a[60:63], v[176:191]\n\t"
    "ds_read_b64_tr_b16 a[108:109], v234 offset:1536\n\t"
    "ds_read_b64_tr_b16 a[110:111], v234 offset:4608\n\t"
    "v_cvt_pk_bf16_f32 v212, v218, v219\n\t"
    "v_add_f32 v229, v229, v218\n\t"
    "v_mfma_f32_32x32x16_bf16 v[160:175], a[112:115], a[16:19], v[160:175]\n\t"
    "buffer_load_dwordx4 v239, s[20:23], s34 offen offset:1024 lds\n\t"
    "v_add_f32 v229, v229, v219\n\t"
    "v_fma_f32 v218, v154, s63, v225\n\t"
    "v_fma_f32 v219, v155, s63, v225\n\t"
    "v_mfma_f32_32x32x16_bf16 v[176:191], a[112:115], a[64:67], v[176:191]\n\t"
    "ds_read_b64_tr_b16 a[112:113], v234 offset:2048\n\t"
    "ds_read_b64_tr_b16 a[114:115], v234 offset:5120\n\t"
    "v_exp_f32 v218, v218\n\t"
    "v_exp_f32 v219, v219\n\t"
    "v_mfma_f32_32x32x16_bf16 v[160:175], a[116:119], a[20:23], v[160:175]\n\t"
    "v_cvt_pk_bf16_f32 v209, v216, v217\n\t"
    "v_add_f32 v228, v228, v216\n\t"
    "v_add_f32 v228, v228, v217\n\t"
    "v_fma_f32 v216, v140, s63, v224\n\t"
    "v_mfma_f32_32x32x16_bf16 v[176:191], a[116:119], a[68:71], v[176:191]\n\t"
    "ds_read_b64_tr_b16 a[116:117], v234 offset:2560\n\t"
    "ds_read_b64_tr_b16 a[118:119], v234 offset:5632\n\t"
    "v_fma_f32 v217, v141, s63, v224\n\t"
    "v_exp_f32 v216, v216\n\t"
    "v_mfma_f32_32x32x16_bf16 v[160:175], a[120:123], a[24:27], v[160:175]\n\t"
    "buffer_load_dwordx4 v240, s[20:23], s34 offen offset:2048 lds\n\t"
    "s_add_u32 s34, s34, 12288\n\t"
    "v_exp_f32 v217, v217\n\t"
    "v_cvt_pk_bf16_f32 v213, v218, v219\n\t"
    "v_mfma_f32_32x32x16_bf16 v[176:191], a[120:123], a[72:75], v[176:191]\n\t"
    "ds_read_b64_tr_b16 a[120:121], v234 offset:6144\n\t"
    "ds_read_b64_tr_b16 a[122:123], v234 offset:9216\n\t"
    "v_add_f32 v229, v229, v218\n\t"
    "v_add_f32 v229, v229, v219\n\t"
    "v_mfma_f32_32x32x16_bf16 v[160:175], a[124:127], a[28:31], v[160:175]\n\t"
    "v_fma_f32 v218, v156, s63, v225\n\t"
    "v_fma_f32 v219, v157, s63, v225\n\t"
    "v_exp_f32 v218, v218\n\t"
    "v_exp_f32 v219, v219\n\t"
    "v_mfma_f32_32x32x16_bf16 v[176:191], a[124:127], a[76:79], v[176:191]\n\t"
    "ds_read_b64_tr_b16 a[124:125], v234 offset:6656\n\t"
    "ds_read_b64_tr_b16 a[126:127], v234 offset:9728\n\t"
    "v_cvt_pk_bf16_f32 v210, v216, v217\n\t"
    "v_add_f32 v228, v228, v216\n\t"
    "v_mfma_f32_32x32x16_bf16 v[160:175], a[128:131], a[32:35], v[160:175]\n\t"
    "s_add_u32 m0, s46, 12288\n\t"
    "s_nop 0\n\t"
    "buffer_load_dwordx4 v235, s[16:19], s32 offen lds\n\t"
    "v_add_f32 v228, v228, v217\n\t"
    "v_fma_f32 v216, v142, s63, v224\n\t"
    "v_mfma_f32_32x32x16_bf16 v[176:191], a[128:131], a[80:83], v[176:191]\n\t"
    "ds_read_b64_tr_b16 a[128:129], v234 offset:7168\n\t"
    "ds_read_b64_tr_b16 a[130:131], v234 offset:10240\n\t"
    "v_fma_f32 v217, v143, s63, v224\n\t"
    "v_exp_f32 v216, v216\n\t"
    "v_mfma_f32_32x32x16_bf16 v[160:175], a[132:135], a[36:39], v[160:175]\n\t"
    "v_exp_f32 v217, v217\n\t"
    "v_cvt_pk_bf16_f32 v214, v218, v219\n\t"
    "v_add_f32 v229, v229, v218\n\t"
    "v_mfma_f32_32x32x16_bf16 v[176:191], a[132:135], a[84:87], v[176:191]\n\t"
    "ds_read_b64_tr_b16 a[132:133], v234 offset:7680\n\t"
    "ds_read_b64_tr_b16 a[134:135], v234 offset:10752\n\t"
    "v_add_f32 v229, v229, v219\n\t"
    "v_fma_f32 v218, v158, s63, v225\n\t"
    "v_fma_f32 v219, v159, s63, v225\n\t"
    "v_mfma_f32_32x32x16_bf16 v[160:175], a[136:139], a[40:43], v[160:175]\n\t"
    "buffer_load_dwordx4 v236, s[16:19], s32 offen offset:1024 lds\n\t"
    "v_exp_f32 v218, v218\n\t"
    "v_exp_f32 v219, v219\n\t"
    "v_mfma_f32_32x32x16_bf16 v[176:191], a[136:139], a[88:91], v[176:191]\n\t"
    "ds_read_b64_tr_b16 a[136:137], v234 offset:8192\n\t"
    "ds_read_b64_tr_b16 a[138:139], v234 offset:11264\n\t"
    "v_cvt_pk_bf16_f32 v211, v216, v217\n\t"
    "v_add_f32 v228, v228, v216\n\t"
    "v_mfma_f32_32x32x16_bf16 v[160:175], a[140:143], a[44:47], v[160:175]\n\t"
    "v_add_f32 v228, v228, v217\n\t"
    "s_nop 0\n\t"
    "v_cvt_pk_bf16_f32 v215, v218, v219\n\t"
    "v_add_f32 v229, v229, v218\n\t"
    "v_add_f32 v229, v229, v219\n\t"
    "v_mfma_f32_32x32x16_bf16 v[176:191], a[140:143], a[92:95], v[176:191]\n\t"
    "ds_read_b64_tr_b16 a[140:141], v234 offset:8704\n\t"
    "ds_read_b64_tr_b16 a[142:143], v234 offset:11776\n\t"
    "v_add_f32 v251, v228, v229\n\t"
    "v_cmp_nge_f32 vcc, s64, v251\n\t"
    "s_cbranch_vccnz .Ld_stub3\n\t"
    ".Ld_back3:\n\t"
    "s_waitcnt lgkmcnt(12)\n\t"
    "v_mfma_f32_32x32x16_bf16 v[0:15], a[96:99], v[192:195], v[0:15]\n\t"
    "buffer_load_dwordx4 v237, s[16:19], s32 offen offset:2048 lds\n\t"
    "s_add_u32 s32, s32, 12288\n\t"
    "ds_read_b128 v[252:255], v241 offset:7168\n\t"
    "v_add_f32 v226, v226, v228\n\t"
    "v_add_f32 v227, v227, v229\n\t"
    "v_mfma_f32_32x32x16_bf16 v[96:111], a[96:99], v[196:199], v[96:111]\n\t"
    "ds_read_b128 a[96:99], v230 offset:24576\n\t"
    "s_waitcnt vmcnt(15)\n\t"
    "ds_read_b128 a[208:211], v244 offset:0\n\t"
    "ds_write_b128 v244, a[224:227] offset:4096\n\t"
    "v_mfma_f32_32x32x16_bf16 v[16:31], a[100:103], v[192:195], v[16:31]\n\t"
    "v_mfma_f32_32x32x16_bf16 v[112:127], a[100:103], v[196:199], v[112:127]\n\t"
    "ds_read_b128 a[100:103], v231 offset:24576\n\t"
    "ds_read_b128 a[212:215], v245 offset:0\n\t"
    "ds_write_b128 v245, a[228:231] offset:4096\n\t"
    "v_fma_f32 v216, v160, s63, v224\n\t"
    "v_fma_f32 v217, v161, s63, v224\n\t"
    "v_mfma_f32_32x32x16_bf16 v[32:47], a[104:107], v[192:195], v[32:47]\n\t"
    "s_waitcnt lgkmcnt(6)\n\t"
    "buffer_store_dwordx4 v[252:255], v249, s[28:31], s86 offen offset:128 nt\n\t"
    "v_exp_f32 v216, v216\n\t"
    "v_exp_f32 v217, v217\n\t"
    "v_mfma_f32_32x32x16_bf16 a[144:159], a[104:107], v[196:199], a[144:159]\n\t"
    "ds_read_b128 a[104:107], v232 offset:24576\n\t"
    "ds_read_b128 a[216:219], v246 offset:0\n\t"
    "ds_write_b128 v246, a[232:235] offset:4096\n\t"
    "v_fma_f32 v218, v176, s63, v225\n\t"
    "v_mfma_f32_32x32x16_bf16 v[48:63], a[108:111], v[192:195], v[48:63]\n\t"
    "v_fma_f32 v219, v177, s63, v225\n\t"
    "v_exp_f32 v218, v218\n\t"
    "v_exp_f32 v219, v219\n\t"
    "v_mfma_f32_32x32x16_bf16 a[160:175], a[108:111], v[196:199], a[160:175]\n\t"
    "ds_read_b128 a[108:111], v233 offset:24576\n\t"
    "ds_read_b128 a[220:223], v247 offset:0\n\t"
    "ds_write_b128 v247, a[236:239] offset:4096\n\t"
    "v_cvt_pk_bf16_f32 v200, v216, v217\n\t"
    "v_mfma_f32_32x32x16_bf16 v[64:79], a[112:115], v[192:195], v[64:79]\n\t"
    "v_add_f32 v228, v216, v217\n\t"
    "v_fma_f32 v216, v162, s63, v224\n\t"
    "v_fma_f32 v217, v163, s63, v224\n\t"
    "v_exp_f32 v216, v216\n\t"
    "v_mfma_f32_32x32x16_bf16 a[176:191], a[112:115], v[196:199], a[176:191]\n\t"
    "ds_read_b128 a[112:115], v230 offset:25600\n\t"
    "v_exp_f32 v217, v217\n\t"
    "v_cvt_pk_bf16_f32 v204, v218, v219\n\t"
    "v_add_f32 v229, v218, v219\n\t"
    "v_mfma_f32_32x32x16_bf16 v[80:95], a[116:119], v[192:195], v[80:95]\n\t"
    "s_waitcnt lgkmcnt(2)\n\t"
    "s_add_u32 m0, s61, 0\n\t"
    "s_nop 0\n\t"
    "buffer_load_dwordx4 v248, s[24:27], 0 offen nt lds\n\t"
    "v_fma_f32 v218, v178, s63, v225\n\t"
    "v_fma_f32 v219, v179, s63, v225\n\t"
    "v_mfma_f32_32x32x16_bf16 a[192:207], a[116:119], v[196:199], a[192:207]\n\t"
    "ds_read_b128 a[116:119], v231 offset:25600\n\t"
    "v_exp_f32 v218, v218\n\t"
    "v_exp_f32 v219, v219\n\t"
    "v_mfma_f32_32x32x16_bf16 v[0:15], a[120:123], v[208:211], v[0:15]\n\t"
    "v_cvt_pk_bf16_f32 v201, v216, v217\n\t"
    "v_add_f32 v228, v228, v216\n\t"
    "v_add_f32 v228, v228, v217\n\t"
    "v_fma_f32 v216, v164, s63, v224\n\t"
    "v_mfma_f32_32x32x16_bf16 v[96:111], a[120:123], v[212:215], v[96:111]\n\t"
    "ds_read_b128 a[120:123], v232 offset:25600\n\t"
    "v_fma_f32 v217, v165, s63, v224\n\t"
    "v_exp_f32 v216, v216\n\t"
    "v_mfma_f32_32x32x16_bf16 v[16:31], a[124:127], v[208:211], v[16:31]\n\t"
    "v_exp_f32 v217, v217\n\t"
    "v_cvt_pk_bf16_f32 v205, v218, v219\n\t"
    "v_add_f32 v229, v229, v218\n\t"
    "v_add_f32 v229, v229, v219\n\t"
    "v_mfma_f32_32x32x16_bf16 v[112:127], a[124:127], v[212:215], v[112:127]\n\t"
    "ds_read_b128 a[124:127], v233 offset:25600\n\t"
    "v_fma_f32 v218, v180, s63, v225\n\t"
    "v_fma_f32 v219, v181, s63, v225\n\t"
    "v_exp_f32 v218, v218\n\t"
    "v_mfma_f32_32x32x16_bf16 v[32:47], a[128:131], v[208:211], v[32:47]\n\t"
    "s_add_u32 m0, s61, 1024\n\t"
    "s_nop 0\n\t"
    "buffer_load_dwordx4 v249, s[24:27], 0 offen nt lds\n\t"
    "v_exp_f32 v219, v219\n\t"
    "v_mfma_f32_32x32x16_bf16 a[144:159], a[128:131], v[212:215], a[144:159]\n\t"
    "ds_read_b128 a[128:131], v230 offset:26624\n\t"
    "v_cvt_pk_bf16_f32 v202, v216, v217\n\t"
    "v_add_f32 v228, v228, v216\n\t"
    "v_add_f32 v228, v228, v217\n\t"
    "v_mfma_f32_32x32x16_bf16 v[48:63], a[132:135], v[208:211], v[48:63]\n\t"
    "v_fma_f32 v216, v166, s63, v224\n\t"
    "v_fma_f32 v217, v167, s63, v224\n\t"
    "v_exp_f32 v216, v216\n\t"
    "v_exp_f32 v217, v217\n\t"
    "v_mfma_f32_32x32x16_bf16 a[160:175], a[132:135], v[212:215], a[160:175]\n\t"
    "ds_read_b128 a[132:135], v231 offset:26624\n\t"
    "v_cvt_pk_bf16_f32 v206, v218, v219\n\t"
    "v_add_f32 v229, v229, v218\n\t"
    "v_add_f32 v229, v229, v219\n\t"
    "v_mfma_f32_32x32x16_bf16 v[64:79], a[136:139], v[208:211], v[64:79]\n\t"
    "v_fma_f32 v218, v182, s63, v225\n\t"
    "v_fma_f32 v219, v183, s63, v225\n\t"
    "v_exp_f32 v218, v218\n\t"
    "v_mfma_f32_32x32x16_bf16 a[176:191], a[136:139], v[212:215], a[176:191]\n\t"
    "ds_read_b128 a[136:139], v232 offset:26624\n\t"
    "v_exp_f32 v219, v219\n\t"
    "v_cvt_pk_bf16_f32 v203, v216, v217\n\t"
    "v_add_f32 v228, v228, v216\n\t"
    "v_mfma_f32_32x32x16_bf16 v[80:95], a[140:143], v[208:211], v[80:95]\n\t"
    "s_add_u32 m0, s61, 2048\n\t"
    "s_nop 0\n\t"
    "buffer_load_dwordx4 v248, s[24:27], s86 offen nt lds\n\t"
    "v_add_f32 v228, v228, v217\n\t"
    "s_nop 0\n\t"
    "v_mfma_f32_32x32x16_bf16 a[192:207], a[140:143], v[212:215], a[192:207]\n\t"
    "ds_read_b128 a[140:143], v233 offset:26624\n\t"
    "v_cvt_pk_bf16_f32 v207, v218, v219\n\t"
    "v_add_f32 v229, v229, v218\n\t"
    "v_add_f32 v229, v229, v219\n\t"
    "v_mfma_f32_32x32x16_bf16 v[128:143], a[96:99], a[0:3], 0\n\t"
    "v_fma_f32 v216, v168, s63, v224\n\t"
    "v_fma_f32 v217, v169, s63, v224\n\t"
    "v_exp_f32 v216, v216\n\t"
    "v_mfma_f32_32x32x16_bf16 v[144:159], a[96:99], a[48:51], 0\n\t"
    "v_exp_f32 v217, v217\n\t"
    "v_fma_f32 v218, v184, s63, v225\n\t"
    "v_fma_f32 v219, v185, s63, v225\n\t"
    "v_exp_f32 v218, v218\n\t"
    "v_mfma_f32_32x32x16_bf16 v[128:143], a[100:103], a[4:7], v[128:143]\n\t"
    "v_exp_f32 v219, v219\n\t"
    "v_cvt_pk_bf16_f32 v208, v216, v217\n\t"
    "v_add_f32 v228, v228, v216\n\t"
    "v_mfma_f32_32x32x16_bf16 v[144:159], a[100:103], a[52:55], v[144:159]\n\t"
    "v_add_f32 v228, v228, v217\n\t"
    "s_waitcnt vmcnt(23) lgkmcnt(0)\n\t"
    "s_barrier\n\t"
    "v_mfma_f32_32x32x16_bf16 v[128:143], a[104:107], a[8:11], v[128:143]\n\t"
    "ds_read_b64_tr_b16 a[96:97], v234 offset:12288\n\t"
    "ds_read_b64_tr_b16 a[98:99], v234 offset:15360\n\t"
    "s_mov_b32 m0, s49\n\t"
    "s_nop 0\n\t"
    "buffer_load_dwordx4 v238, s[20:23], s34 offen lds\n\t"
    "v_mfma_f32_32x32x16_bf16 v[144:159], a[104:107], a[56:59], v[144:159]\n\t"
    "ds_read_b64_tr_b16 a[100:101], v234 offset:12800\n\t"
    "ds_read_b64_tr_b16 a[102:103], v234 offset:15872\n\t"
    "v_fma_f32 v216, v170, s63, v224\n\t"
    "v_fma_f32 v217, v171, s63, v224\n\t"
    "v_mfma_f32_32x32x16_bf16 v[128:143], a[108:111], a[12:15], v[128:143]\n\t"
    "ds_read_b64_tr_b16 a[104:105], v234 offset:13312\n\t"
    "ds_read_b64_tr_b16 a[106:107], v234 offset:16384\n\t"
    "v_exp_f32 v216, v216\n\t"
    "v_exp_f32 v217, v217\n\t"
    "v_mfma_f32_32x32x16_bf16 v[144:159], a[108:111], a[60:63], v[144:159]\n\t"
    "ds_read_b64_tr_b16 a[108:109], v234 offset:13824\n\t"
    "ds_read_b64_tr_b16 a[110:111], v234 offset:16896\n\t"
    "v_cvt_pk_bf16_f32 v212, v218, v219\n\t"
    "v_add_f32 v229, v229, v218\n\t"
    "v_mfma_f32_32x32x16_bf16 v[128:143], a[112:115], a[16:19], v[128:143]\n\t"
    "buffer_load_dwordx4 v239, s[20:23], s34 offen offset:1024 lds\n\t"
    "v_add_f32 v229, v229, v219\n\t"
    "v_fma_f32 v218, v186, s63, v225\n\t"
    "v_fma_f32 v219, v187, s63, v225\n\t"
    "v_mfma_f32_32x32x16_bf16 v[144:159], a[112:115], a[64:67], v[144:159]\n\t"
    "ds_read_b64_tr_b16 a[112:113], v234 offset:14336\n\t"
    "ds_read_b64_tr_b16 a[114:115], v234 offset:17408\n\t"
    "v_exp_f32 v218, v218\n\t"
    "v_exp_f32 v219, v219\n\t"
    "v_mfma_f32_32x32x16_bf16 v[128:143], a[116:119], a[20:23], v[128:143]\n\t"
    "v_cvt_pk_bf16_f32 v209, v216, v217\n\t"
    "v_add_f32 v228, v228, v216\n\t"
    "v_add_f32 v228, v228, v217\n\t"
    "v_fma_f32 v216, v172, s63, v224\n\t"
    "v_mfma_f32_32x32x16_bf16 v[144:159], a[116:119], a[68:71], v[144:159]\n\t"
    "ds_read_b64_tr_b16 a[116:117], v234 offset:14848\n\t"
    "ds_read_b64_tr_b16 a[118:119], v234 offset:17920\n\t"
    "v_fma_f32 v217, v173, s63, v224\n\t"
    "v_exp_f32 v216, v216\n\t"
    "v_mfma_f32_32x32x16_bf16 v[128:143], a[120:123], a[24:27], v[128:143]\n\t"
    "buffer_load_dwordx4 v240, s[20:23], s34 offen offset:2048 lds\n\t"
    "s_add_u32 s34, s34, 12288\n\t"
    "v_exp_f32 v217, v217\n\t"
    "v_cvt_pk_bf16_f32 v213, v218, v219\n\t"
    "v_mfma_f32_32x32x16_bf16 v[144:159], a[120:123], a[72:75], v[144:159]\n\t"
    "ds_read_b64_tr_b16 a[120:121], v234 offset:18432\n\t"
    "ds_read_b64_tr_b16 a[122:123], v234 offset:21504\n\t"
    "v_add_f32 v229, v229, v218\n\t"
    "v_add_f32 v229, v229, v219\n\t"
    "v_mfma_f32_32x32x16_bf16 v[128:143], a[124:127], a[28:31], v[128:143]\n\t"
    "v_fma_f32 v218, v188, s63, v225\n\t"
    "v_fma_f32 v219, v189, s63, v225\n\t"
    "v_exp_f32 v218, v218\n\t"
    "v_exp_f32 v219, v219\n\t"
    "v_mfma_f32_32x32x16_bf16 v[144:159], a[124:127], a[76:79], v[144:159]\n\t"
    "ds_read_b64_tr_b16 a[124:125], v234 offset:18944\n\t"
    "ds_read_b64_tr_b16 a[126:127], v234 offset:22016\n\t"
    "v_cvt_pk_bf16_f32 v210, v216, v217\n\t"
    "v_add_f32 v228, v228, v216\n\t"
    "v_mfma_f32_32x32x16_bf16 v[128:143], a[128:131], a[32:35], v[128:143]\n\t"
    "s_add_u32 m0, s46, 24576\n\t"
    "s_nop 0\n\t"
    "buffer_load_dwordx4 v235, s[16:19], s32 offen lds\n\t"
    "v_add_f32 v228, v228, v217\n\t"
    "v_fma_f32 v216, v174, s63, v224\n\t"
    "v_mfma_f32_32x32x16_bf16 v[144:159], a[128:131], a[80:83], v[144:159]\n\t"
    "ds_read_b64_tr_b16 a[128:129], v234 offset:19456\n\t"
    "ds_read_b64_tr_b16 a[130:131], v234 offset:22528\n\t"
    "v_fma_f32 v217, v175, s63, v224\n\t"
    "v_exp_f32 v216, v216\n\t"
    "v_mfma_f32_32x32x16_bf16 v[128:143], a[132:135], a[36:39], v[128:143]\n\t"
    "v_exp_f32 v217, v217\n\t"
    "v_cvt_pk_bf16_f32 v214, v218, v219\n\t"
    "v_add_f32 v229, v229, v218\n\t"
    "v_mfma_f32_32x32x16_bf16 v[144:159], a[132:135], a[84:87], v[144:159]\n\t"
    "ds_read_b64_tr_b16 a[132:133], v234 offset:19968\n\t"
    "ds_read_b64_tr_b16 a[134:135], v234 offset:23040\n\t"
    "v_add_f32 v229, v229, v219\n\t"
    "v_fma_f32 v218, v190, s63, v225\n\t"
    "v_fma_f32 v219, v191, s63, v225\n\t"
    "v_mfma_f32_32x32x16_bf16 v[128:143], a[136:139], a[40:43], v[128:143]\n\t"
    "buffer_load_dwordx4 v236, s[16:19], s32 offen offset:1024 lds\n\t"
    "v_exp_f32 v218, v218\n\t"
    "v_exp_f32 v219, v219\n\t"
    "v_mfma_f32_32x32x16_bf16 v[144:159], a[136:139], a[88:91], v[144:159]\n\t"
    "ds_read_b64_tr_b16 a[136:137], v234 offset:20480\n\t"
    "ds_read_b64_tr_b16 a[138:139], v234 offset:23552\n\t"
    "v_cvt_pk_bf16_f32 v211, v216, v217\n\t"
    "v_add_f32 v228, v228, v216\n\t"
    "v_mfma_f32_32x32x16_bf16 v[128:143], a[140:143], a[44:47], v[128:143]\n\t"
    "v_add_f32 v228, v228, v217\n\t"
    "s_nop 0\n\t"
    "v_cvt_pk_bf16_f32 v215, v218, v219\n\t"
    "v_add_f32 v229, v229, v218\n\t"
    "v_add_f32 v229, v229, v219\n\t"
    "v_mfma_f32_32x32x16_bf16 v[144:159], a[140:143], a[92:95], v[144:159]\n\t"
    "ds_read_b64_tr_b16 a[140:141], v234 offset:20992\n\t"
    "ds_read_b64_tr_b16 a[142:143], v234 offset:24064\n\t"
    "v_add_f32 v251, v228, v229\n\t"
    "v_cmp_nge_f32 vcc, s64, v251\n\t"
    "s_cbranch_vccnz .Ld_stub4\n\t"
    ".Ld_back4:\n\t"
    "s_waitcnt lgkmcnt(12)\n\t"
    "v_mfma_f32_32x32x16_bf16 v[0:15], a[96:99], v[200:203], v[0:15]\n\t"
    "buffer_load_dwordx4 v237, s[16:19], s32 offen offset:2048 lds\n\t"
    "s_add_u32 s32, s32, 12288\n\t"
    "v_add_f32 v226, v226, v228\n\t"
    "v_add_f32 v227, v227, v229\n\t"
    "v_mfma_f32_32x32x16_bf16 v[96:111], a[96:99], v[204:207], v[96:111]\n\t"
    "ds_read_b128 a[96:99], v230 offset:36864\n\t"
    "v_mfma_f32_32x32x16_bf16 v[16:31], a[100:103], v[200:203], v[16:31]\n\t"
    "v_mfma_f32_32x32x16_bf16 v[112:127], a[100:103], v[204:207], v[112:127]\n\t"
    "ds_read_b128 a[100:103], v231 offset:36864\n\t"
    "v_fma_f32 v216, v128, s63, v224\n\t"
    "v_fma_f32 v217, v129, s63, v224\n\t"
    "v_exp_f32 v216, v216\n\t"
    "v_mfma_f32_32x32x16_bf16 v[32:47], a[104:107], v[200:203], v[32:47]\n\t"
    "v_exp_f32 v217, v217\n\t"
    "v_fma_f32 v218, v144, s63, v225\n\t"
    "v_fma_f32 v219, v145, s63, v225\n\t"
    "v_mfma_f32_32x32x16_bf16 a[144:159], a[104:107], v[204:207], a[144:159]\n\t"
    "ds_read_b128 a[104:107], v232 offset:36864\n\t"
    "v_exp_f32 v218, v218\n\t"
    "v_exp_f32 v219, v219\n\t"
    "v_mfma_f32_32x32x16_bf16 v[48:63], a[108:111], v[200:203], v[48:63]\n\t"
    "v_cvt_pk_bf16_f32 v192, v216, v217\n\t"
    "v_add_f32 v228, v216, v217\n\t"
    "v_fma_f32 v216, v130, s63, v224\n\t"
    "v_mfma_f32_32x32x16_bf16 a[160:175], a[108:111], v[204:207], a[160:175]\n\t"
    "ds_read_b128 a[108:111], v233 offset:36864\n\t"
    "s_add_u32 m0, s61, 3072\n\t"
    "s_nop 0\n\t"
    "buffer_load_dwordx4 v249, s[24:27], s86 offen nt lds\n\t"
    "v_fma_f32 v217, v131, s63, v224\n\t"
    "v_mfma_f32_32x32x16_bf16 v[64:79], a[112:115], v[200:203], v[64:79]\n\t"
    "v_exp_f32 v216, v216\n\t"
    "v_exp_f32 v217, v217\n\t"
    "v_mfma_f32_32x32x16_bf16 a[176:191], a[112:115], v[204:207], a[176:191]\n\t"
    "ds_read_b128 a[112:115], v230 offset:37888\n\t"
    "ds_read_b128 v[220:223], v241 offset:4096\n\t"
    "v_cvt_pk_bf16_f32 v196, v218, v219\n\t"
    "v_add_f32 v229, v218, v219\n\t"
    "v_mfma_f32_32x32x16_bf16 v[80:95], a[116:119], v[200:203], v[80:95]\n\t"
    "v_fma_f32 v218, v146, s63, v225\n\t"
    "v_fma_f32 v219, v147, s63, v225\n\t"
    "v_exp_f32 v218, v218\n\t"
    "v_mfma_f32_32x32x16_bf16 a[192:207], a[116:119], v[204:207], a[192:207]\n\t"
    "ds_read_b128 a[116:119], v231 offset:37888\n\t"
    "v_exp_f32 v219, v219\n\t"
    "v_cvt_pk_bf16_f32 v193, v216, v217\n\t"
    "v_add_f32 v228, v228, v216\n\t"
    "s_waitcnt lgkmcnt(11)\n\t"
    "v_mfma_f32_32x32x16_bf16 v[0:15], a[120:123], v[208:211], v[0:15]\n\t"
    "v_add_f32 v228, v228, v217\n\t"
    "v_fma_f32 v216, v132, s63, v224\n\t"
    "v_fma_f32 v217, v133, s63, v224\n\t"
    "v_exp_f32 v216, v216\n\t"
    "v_mfma_f32_32x32x16_bf16 v[96:111], a[120:123], v[212:215], v[96:111]\n\t"
    "s_waitcnt lgkmcnt(1)\n\t"
    "buffer_store_dwordx4 v[220:223], v248, s[28:31], s87 offen offset:128 nt\n\t"
    "ds_read_b128 a[120:123], v232 offset:37888\n\t"
    "v_exp_f32 v217, v217\n\t"
    "v_mfma_f32_32x32x16_bf16 v[16:31], a[124:127], v[208:211], v[16:31]\n\t"
    "v_cvt_pk_bf16_f32 v197, v218, v219\n\t"
    "v_add_f32 v229, v229, v218\n\t"
    "v_add_f32 v229, v229, v219\n\t"
    "v_fma_f32 v218, v148, s63, v225\n\t"
    "v_mfma_f32_32x32x16_bf16 v[112:127], a[124:127], v[212:215], v[112:127]\n\t"
    "ds_read_b128 a[124:127], v233 offset:37888\n\t"
    "ds_read_b128 v[252:255], v241 offset:5120\n\t"
    "v_fma_f32 v219, v149, s63, v225\n\t"
    "v_exp_f32 v218, v218\n\t"
    "v_mfma_f32_32x32x16_bf16 v[32:47], a[128:131], v[208:211], v[32:47]\n\t"
    "v_exp_f32 v219, v219\n\t"
    "v_cvt_pk_bf16_f32 v194, v216, v217\n\t"
    "v_add_f32 v228, v228, v216\n\t"
    "v_mfma_f32_32x32x16_bf16 a[144:159], a[128:131], v[212:215], a[144:159]\n\t"
    "ds_read_b128 a[128:131], v230 offset:38912\n\t"
    "v_add_f32 v228, v228, v217\n\t"
    "v_fma_f32 v216, v134, s63, v224\n\t"
    "v_fma_f32 v217, v135, s63, v224\n\t"
    "v_mfma_f32_32x32x16_bf16 v[48:63], a[132:135], v[208:211], v[48:63]\n\t"
    "v_exp_f32 v216, v216\n\t"
    "v_exp_f32 v217, v217\n\t"
    "v_cvt_pk_bf16_f32 v198, v218, v219\n\t"
    "v_mfma_f32_32x32x16_bf16 a[160:175], a[132:135], v[212:215], a[160:175]\n\t"
    "s_waitcnt lgkmcnt(1)\n\t"
    "buffer_store_dwordx4 v[252:255], v249, s[28:31], s87 offen offset:128 nt\n\t"
    "ds_read_b128 a[132:135], v231 offset:38912\n\t"
    "v_add_f32 v229, v229, v218\n\t"
    "v_add_f32 v229, v229, v219\n\t"
    "v_mfma_f32_32x32x16_bf16 v[64:79], a[136:139], v[208:211], v[64:79]\n\t"
    "v_fma_f32 v218, v150, s63, v225\n\t"
    "v_fma_f32 v219, v151, s63, v225\n\t"
    "v_exp_f32 v218, v218\n\t"
    "v_mfma_f32_32x32x16_bf16 a[176:191], a[136:139], v[212:215], a[176:191]\n\t"
    "ds_read_b128 a[136:139], v232 offset:38912\n\t"
    "ds_read_b128 v[220:223], v241 offset:6144\n\t"
    "v_exp_f32 v219, v219\n\t"
    "v_cvt_pk_bf16_f32 v195, v216, v217\n\t"
    "v_mfma_f32_32x32x16_bf16 v[80:95], a[140:143], v[208:211], v[80:95]\n\t"
    "v_add_f32 v228, v228, v216\n\t"
    "v_add_f32 v228, v228, v217\n\t"
    "s_nop 0\n\t"
    "v_cvt_pk_bf16_f32 v199, v218, v219\n\t"
    "v_mfma_f32_32x32x16_bf16 a[192:207], a[140:143], v[212:215], a[192:207]\n\t"
    "ds_read_b128 a[140:143], v233 offset:38912\n\t"
    "v_add_f32 v229, v229, v218\n\t"
    "v_add_f32 v229, v229, v219\n\t"
    "v_mfma_f32_32x32x16_bf16 v[160:175], a[96:99], a[0:3], 0\n\t"
    "v_fma_f32 v216, v136, s63, v224\n\t"
    "v_fma_f32 v217, v137, s63, v224\n\t"
    "v_exp_f32 v216, v216\n\t"
    "v_mfma_f32_32x32x16_bf16 v[176:191], a[96:99], a[48:51], 0\n\t"
    "s_waitcnt lgkmcnt(1)\n\t"
    "buffer_store_dwordx4 v[220:223], v248, s[28:31], s43 offen offset:128 nt\n\t"
    "v_exp_f32 v217, v217\n\t"
    "v_fma_f32 v218, v152, s63, v225\n\t"
    "v_fma_f32 v219, v153, s63, v225\n\t"
    "v_mfma_f32_32x32x16_bf16 v[160:175], a[100:103], a[4:7], v[160:175]\n\t"
    "v_exp_f32 v218, v218\n\t"
    "v_exp_f32 v219, v219\n\t"
    "v_cvt_pk_bf16_f32 v208, v216, v217\n\t"
    "v_mfma_f32_32x32x16_bf16 v[176:191], a[100:103], a[52:55], v[176:191]\n\t"
    "v_add_f32 v228, v228, v216\n\t"
    "s_waitcnt vmcnt(23) lgkmcnt(0)\n\t"
    "s_barrier\n\t"
    "v_mfma_f32_32x32x16_bf16 v[160:175], a[104:107], a[8:11], v[160:175]\n\t"
    "ds_read_b64_tr_b16 a[96:97], v234 offset:24576\n\t"
    "ds_read_b64_tr_b16 a[98:99], v234 offset:27648\n\t"
    "s_add_u32 m0, s49, 12288\n\t"
    "s_nop 0\n\t"
    "buffer_load_dwordx4 v238, s[20:23], s34 offen lds\n\t"
    "v_mfma_f32_32x32x16_bf16 v[176:191], a[104:107], a[56:59], v[176:191]\n\t"
    "ds_read_b64_tr_b16 a[100:101], v234 offset:25088\n\t"
    "ds_read_b64_tr_b16 a[102:103], v234 offset:28160\n\t"
    "v_add_f32 v228, v228, v217\n\t"
    "v_fma_f32 v216, v138, s63, v224\n\t"
    "v_fma_f32 v217, v139, s63, v224\n\t"
    "v_mfma_f32_32x32x16_bf16 v[160:175], a[108:111], a[12:15], v[160:175]\n\t"
    "ds_read_b64_tr_b16 a[104:105], v234 offset:25600\n\t"
    "ds_read_b64_tr_b16 a[106:107], v234 offset:28672\n\t"
    "v_exp_f32 v216, v216\n\t"
    "v_mfma_f32_32x32x16_bf16 v[176:191], a[108:111], a[60:63], v[176:191]\n\t"
    "ds_read_b64_tr_b16 a[108:109], v234 offset:26112\n\t"
    "ds_read_b64_tr_b16 a[110:111], v234 offset:29184\n\t"
    "v_exp_f32 v217, v217\n\t"
    "v_cvt_pk_bf16_f32 v212, v218, v219\n\t"
    "v_mfma_f32_32x32x16_bf16 v[160:175], a[112:115], a[16:19], v[160:175]\n\t"
    "buffer_load_dwordx4 v239, s[20:23], s34 offen offset:1024 lds\n\t"
    "v_add_f32 v229, v229, v218\n\t"
    "v_add_f32 v229, v229, v219\n\t"
    "v_fma_f32 v218, v154, s63, v225\n\t"
    "v_fma_f32 v219, v155, s63, v225\n\t"
    "v_mfma_f32_32x32x16_bf16 v[176:191], a[112:115], a[64:67], v[176:191]\n\t"
    "ds_read_b64_tr_b16 a[112:113], v234 offset:26624\n\t"
    "ds_read_b64_tr_b16 a[114:115], v234 offset:29696\n\t"
    "v_exp_f32 v218, v218\n\t"
    "v_mfma_f32_32x32x16_bf16 v[160:175], a[116:119], a[20:23], v[160:175]\n\t"
    "v_exp_f32 v219, v219\n\t"
    "v_cvt_pk_bf16_f32 v209, v216, v217\n\t"
    "v_add_f32 v228, v228, v216\n\t"
    "v_add_f32 v228, v228, v217\n\t"
    "v_fma_f32 v216, v140, s63, v224\n\t"
    "v_mfma_f32_32x32x16_bf16 v[176:191], a[116:119], a[68:71], v[176:191]\n\t"
    "ds_read_b64_tr_b16 a[116:117], v234 offset:27136\n\t"
    "ds_read_b64_tr_b16 a[118:119], v234 offset:30208\n\t"
    "v_fma_f32 v217, v141, s63, v224\n\t"
    "v_exp_f32 v216, v216\n\t"
    "v_mfma_f32_32x32x16_bf16 v[160:175], a[120:123], a[24:27], v[160:175]\n\t"
    "buffer_load_dwordx4 v240, s[20:23], s34 offen offset:2048 lds\n\t"
    "s_add_u32 s34, s34, 12288\n\t"
    "v_exp_f32 v217, v217\n\t"
    "v_mfma_f32_32x32x16_bf16 v[176:191], a[120:123], a[72:75], v[176:191]\n\t"
    "ds_read_b64_tr_b16 a[120:121], v234 offset:30720\n\t"
    "ds_read_b64_tr_b16 a[122:123], v234 offset:33792\n\t"
    "v_cvt_pk_bf16_f32 v213, v218, v219\n\t"
    "v_add_f32 v229, v229, v218\n\t"
    "v_add_f32 v229, v229, v219\n\t"
    "v_mfma_f32_32x32x16_bf16 v[160:175], a[124:127], a[28:31], v[160:175]\n\t"
    "v_fma_f32 v218, v156, s63, v225\n\t"
    "v_fma_f32 v219, v157, s63, v225\n\t"
    "v_exp_f32 v218, v218\n\t"
    "v_mfma_f32_32x32x16_bf16 v[176:191], a[124:127], a[76:79], v[176:191]\n\t"
    "ds_read_b64_tr_b16 a[124:125], v234 offset:31232\n\t"
    "ds_read_b64_tr_b16 a[126:127], v234 offset:34304\n\t"
    "v_exp_f32 v219, v219\n\t"
    "v_cvt_pk_bf16_f32 v210, v216, v217\n\t"
    "v_add_f32 v228, v228, v216\n\t"
    "v_mfma_f32_32x32x16_bf16 v[160:175], a[128:131], a[32:35], v[160:175]\n\t"
    "s_add_u32 m0, s46, 36864\n\t"
    "s_nop 0\n\t"
    "buffer_load_dwordx4 v235, s[16:19], s32 offen lds\n\t"
    "v_add_f32 v228, v228, v217\n\t"
    "v_fma_f32 v216, v142, s63, v224\n\t"
    "v_mfma_f32_32x32x16_bf16 v[176:191], a[128:131], a[80:83], v[176:191]\n\t"
    "ds_read_b64_tr_b16 a[128:129], v234 offset:31744\n\t"
    "ds_read_b64_tr_b16 a[130:131], v234 offset:34816\n\t"
    "v_fma_f32 v217, v143, s63, v224\n\t"
    "v_exp_f32 v216, v216\n\t"
    "v_mfma_f32_32x32x16_bf16 v[160:175], a[132:135], a[36:39], v[160:175]\n\t"
    "v_exp_f32 v217, v217\n\t"
    "v_cvt_pk_bf16_f32 v214, v218, v219\n\t"
    "v_add_f32 v229, v229, v218\n\t"
    "v_mfma_f32_32x32x16_bf16 v[176:191], a[132:135], a[84:87], v[176:191]\n\t"
    "ds_read_b64_tr_b16 a[132:133], v234 offset:32256\n\t"
    "ds_read_b64_tr_b16 a[134:135], v234 offset:35328\n\t"
    "v_add_f32 v229, v229, v219\n\t"
    "v_fma_f32 v218, v158, s63, v225\n\t"
    "v_fma_f32 v219, v159, s63, v225\n\t"
    "v_mfma_f32_32x32x16_bf16 v[160:175], a[136:139], a[40:43], v[160:175]\n\t"
    "buffer_load_dwordx4 v236, s[16:19], s32 offen offset:1024 lds\n\t"
    "v_exp_f32 v218, v218\n\t"
    "v_exp_f32 v219, v219\n\t"
    "v_mfma_f32_32x32x16_bf16 v[176:191], a[136:139], a[88:91], v[176:191]\n\t"
    "ds_read_b64_tr_b16 a[136:137], v234 offset:32768\n\t"
    "ds_read_b64_tr_b16 a[138:139], v234 offset:35840\n\t"
    "v_cvt_pk_bf16_f32 v211, v216, v217\n\t"
    "v_add_f32 v228, v228, v216\n\t"
    "v_add_f32 v228, v228, v217\n\t"
    "v_mfma_f32_32x32x16_bf16 v[160:175], a[140:143], a[44:47], v[160:175]\n\t"
    "s_nop 0\n\t"
    "v_cvt_pk_bf16_f32 v215, v218, v219\n\t"
    "v_add_f32 v229, v229, v218\n\t"
    "v_add_f32 v229, v229, v219\n\t"
    "v_mfma_f32_32x32x16_bf16 v[176:191], a[140:143], a[92:95], v[176:191]\n\t"
    "ds_read_b64_tr_b16 a[140:141], v234 offset:33280\n\t"
    "ds_read_b64_tr_b16 a[142:143], v234 offset:36352\n\t"
    "v_add_f32 v251, v228, v229\n\t"
    "v_cmp_nge_f32 vcc, s64, v251\n\t"
    "s_cbranch_vccnz .Ld_stub5\n\t"
    ".Ld_back5:\n\t"
    "s_waitcnt lgkmcnt(12)\n\t"
    "v_mfma_f32_32x32x16_bf16 v[0:15], a[96:99], v[192:195], v[0:15]\n\t"
    "buffer_load_dwordx4 v237, s[16:19], s32 offen offset:2048 lds\n\t"
    "s_add_u32 s32, s32, 12288\n\t"
    "v_add_f32 v226, v226, v228\n\t"
    "v_add_f32 v227, v227, v229\n\t"
    "v_mfma_f32_32x32x16_bf16 v[96:111], a[96:99], v[196:199], v[96:111]\n\t"
    "ds_read_b128 a[96:99], v230 offset:0\n\t"
    "v_mfma_f32_32x32x16_bf16 v[16:31], a[100:103], v[192:195], v[16:31]\n\t"
    "v_mfma_f32_32x32x16_bf16 v[112:127], a[100:103], v[196:199], v[112:127]\n\t"
    "ds_read_b128 a[100:103], v231 offset:0\n\t"
    "ds_read_b128 v[252:255], v241 offset:7168\n\t"
    "v_fma_f32 v216, v160, s63, v224\n\t"
    "v_fma_f32 v217, v161, s63, v224\n\t"
    "v_mfma_f32_32x32x16_bf16 v[32:47], a[104:107], v[192:195], v[32:47]\n\t"
    "v_exp_f32 v216, v216\n\t"
    "v_exp_f32 v217, v217\n\t"
    "v_fma_f32 v218, v176, s63, v225\n\t"
    "v_mfma_f32_32x32x16_bf16 a[144:159], a[104:107], v[196:199], a[144:159]\n\t"
    "ds_read_b128 a[104:107], v232 offset:0\n\t"
    "v_fma_f32 v219, v177, s63, v225\n\t"
    "v_exp_f32 v218, v218\n\t"
    "v_mfma_f32_32x32x16_bf16 v[48:63], a[108:111], v[192:195], v[48:63]\n\t"
    "v_exp_f32 v219, v219\n\t"
    "v_cvt_pk_bf16_f32 v200, v216, v217\n\t"
    "v_add_f32 v228, v216, v217\n\t"
    "v_mfma_f32_32x32x16_bf16 a[160:175], a[108:111], v[196:199], a[160:175]\n\t"
    "s_waitcnt lgkmcnt(1)\n\t"
    "buffer_store_dwordx4 v[252:255], v249, s[28:31], s43 offen offset:128 nt\n\t"
    "ds_read_b128 a[108:111], v233 offset:0\n\t"
    "v_fma_f32 v216, v162, s63, v224\n\t"
    "v_fma_f32 v217, v163, s63, v224\n\t"
    "v_mfma_f32_32x32x16_bf16 v[64:79], a[112:115], v[192:195], v[64:79]\n\t"
    "v_exp_f32 v216, v216\n\t"
    "v_exp_f32 v217, v217\n\t"
    "v_cvt_pk_bf16_f32 v204, v218, v219\n\t"
    "v_mfma_f32_32x32x16_bf16 a[176:191], a[112:115], v[196:199], a[176:191]\n\t"
    "ds_read_b128 a[112:115], v230 offset:1024\n\t"
    "v_add_f32 v229, v218, v219\n\t"
    "v_fma_f32 v218, v178, s63, v225\n\t"
    "v_fma_f32 v219, v179, s63, v225\n\t"
    "v_mfma_f32_32x32x16_bf16 v[80:95], a[116:119], v[192:195], v[80:95]\n\t"
    "v_exp_f32 v218, v218\n\t"
    "v_exp_f32 v219, v219\n\t"
    "v_cvt_pk_bf16_f32 v201, v216, v217\n\t"
    "v_mfma_f32_32x32x16_bf16 a[192:207], a[116:119], v[196:199], a[192:207]\n\t"
    "ds_read_b128 a[116:119], v231 offset:1024\n\t"
    "v_add_f32 v228, v228, v216\n\t"
    "v_add_f32 v228, v228, v217\n\t"
    "v_fma_f32 v216, v164, s63, v224\n\t"
    "v_mfma_f32_32x32x16_bf16 v[0:15], a[120:123], v[208:211], v[0:15]\n\t"
    "v_fma_f32 v217, v165, s63, v224\n\t"
    "v_exp_f32 v216, v216\n\t"
    "v_exp_f32 v217, v217\n\t"
    "v_mfma_f32_32x32x16_bf16 v[96:111], a[120:123], v[212:215], v[96:111]\n\t"
    "ds_read_b128 a[120:123], v232 offset:1024\n\t"
    "v_cvt_pk_bf16_f32 v205, v218, v219\n\t"
    "v_add_f32 v229, v229, v218\n\t"
    "v_add_f32 v229, v229, v219\n\t"
    "v_mfma_f32_32x32x16_bf16 v[16:31], a[124:127], v[208:211], v[16:31]\n\t"
    "v_fma_f32 v218, v180, s63, v225\n\t"
    "v_fma_f32 v219, v181, s63, v225\n\t"
    "v_exp_f32 v218, v218\n\t"
    "v_mfma_f32_32x32x16_bf16 v[112:127], a[124:127], v[212:215], v[112:127]\n\t"
    "ds_read_b128 a[124:127], v233 offset:1024\n\t"
    "v_exp_f32 v219, v219\n\t"
    "v_cvt_pk_bf16_f32 v202, v216, v217\n\t"
    "v_add_f32 v228, v228, v216\n\t"
    "v_mfma_f32_32x32x16_bf16 v[32:47], a[128:131], v[208:211], v[32:47]\n\t"
    "s_add_u32 m0, s61, 3968\n\t"
    "s_nop 0\n\t"
    "buffer_load_dwordx4 v248, s[24:27], s87 offen offset:128 nt lds\n\t"
    "v_add_f32 v228, v228, v217\n\t"
    "v_mfma_f32_32x32x16_bf16 a[144:159], a[128:131], v[212:215], a[144:159]\n\t"
    "ds_read_b128 a[128:131], v230 offset:2048\n\t"
    "v_fma_f32 v216, v166, s63, v224\n\t"
    "v_fma_f32 v217, v167, s63, v224\n\t"
    "v_exp_f32 v216, v216\n\t"
    "v_mfma_f32_32x32x16_bf16 v[48:63], a[132:135], v[208:211], v[48:63]\n\t"
    "v_exp_f32 v217, v217\n\t"
    "v_cvt_pk_bf16_f32 v206, v218, v219\n\t"
    "v_add_f32 v229, v229, v218\n\t"
    "v_mfma_f32_32x32x16_bf16 a[160:175], a[132:135], v[212:215], a[160:175]\n\t"
    "ds_read_b128 a[132:135], v231 offset:2048\n\t"
    "v_add_f32 v229, v229, v219\n\t"
    "v_fma_f32 v218, v182, s63, v225\n\t"
    "v_fma_f32 v219, v183, s63, v225\n\t"
    "v_mfma_f32_32x32x16_bf16 v[64:79], a[136:139], v[208:211], v[64:79]\n\t"
    "v_exp_f32 v218, v218\n\t"
    "v_exp_f32 v219, v219\n\t"
    "v_mfma_f32_32x32x16_bf16 a[176:191], a[136:139], v[212:215], a[176:191]\n\t"
    "ds_read_b128 a[136:139], v232 offset:2048\n\t"
    "v_cvt_pk_bf16_f32 v203, v216, v217\n\t"
    "v_add_f32 v228, v228, v216\n\t"
    "v_add_f32 v228, v228, v217\n\t"
    "s_nop 0\n\t"
    "v_mfma_f32_32x32x16_bf16 v[80:95], a[140:143], v[208:211], v[80:95]\n\t"
    "s_add_u32 m0, s61, 4992\n\t"
    "s_nop 0\n\t"
    "buffer_load_dwordx4 v249, s[24:27], s87 offen offset:128 nt lds\n\t"
    "v_cvt_pk_bf16_f32 v207, v218, v219\n\t"
    "v_mfma_f32_32x32x16_bf16 a[192:207], a[140:143], v[212:215], a[192:207]\n\t"
    "ds_read_b128 a[140:143], v233 offset:2048\n\t"
    "v_add_f32 v229, v229, v218\n\t"
    "v_add_f32 v229, v229, v219\n\t"
    "v_mfma_f32_32x32x16_bf16 v[128:143], a[96:99], a[0:3], 0\n\t"
    "v_fma_f32 v216, v168, s63, v224\n\t"
    "v_fma_f32 v217, v169, s63, v224\n\t"
    "v_exp_f32 v216, v216\n\t"
    "v_mfma_f32_32x32x16_bf16 v[144:159], a[96:99], a[48:51], 0\n\t"
    "v_exp_f32 v217, v217\n\t"
    "v_fma_f32 v218, v184, s63, v225\n\t"
    "v_fma_f32 v219, v185, s63, v225\n\t"
    "v_exp_f32 v218, v218\n\t"
    "s_waitcnt lgkmcnt(9)\n\t"
    "v_mfma_f32_32x32x16_bf16 v[128:143], a[100:103], a[4:7], v[128:143]\n\t"
    "v_exp_f32 v219, v219\n\t"
    "v_cvt_pk_bf16_f32 v208, v216, v217\n\t"
    "v_add_f32 v228, v228, v216\n\t"
    "v_mfma_f32_32x32x16_bf16 v[144:159], a[100:103], a[52:55], v[144:159]\n\t"
    "v_add_f32 v228, v228, v217\n\t"
    "s_waitcnt vmcnt(23) lgkmcnt(0)\n\t"
    "s_barrier\n\t"
    "v_mfma_f32_32x32x16_bf16 v[128:143], a[104:107], a[8:11], v[128:143]\n\t"
    "ds_read_b64_tr_b16 a[96:97], v234 offset:36864\n\t"
    "ds_read_b64_tr_b16 a[98:99], v234 offset:39936\n\t"
    "s_add_u32 m0, s49, 24576\n\t"
    "s_nop 0\n\t"
    "buffer_load_dwordx4 v238, s[20:23], s34 offen lds\n\t"
    "v_mfma_f32_32x32x16_bf16 v[144:159], a[104:107], a[56:59], v[144:159]\n\t"
    "ds_read_b64_tr_b16 a[100:101], v234 offset:37376\n\t"
    "ds_read_b64_tr_b16 a[102:103], v234 offset:40448\n\t"
    "v_fma_f32 v216, v170, s63, v224\n\t"
    "v_fma_f32 v217, v171, s63, v224\n\t"
    "v_mfma_f32_32x32x16_bf16 v[128:143], a[108:111], a[12:15], v[128:143]\n\t"
    "ds_read_b64_tr_b16 a[104:105], v234 offset:37888\n\t"
    "ds_read_b64_tr_b16 a[106:107], v234 offset:40960\n\t"
    "v_exp_f32 v216, v216\n\t"
    "v_exp_f32 v217, v217\n\t"
    "v_mfma_f32_32x32x16_bf16 v[144:159], a[108:111], a[60:63], v[144:159]\n\t"
    "ds_read_b64_tr_b16 a[108:109], v234 offset:38400\n\t"
    "ds_read_b64_tr_b16 a[110:111], v234 offset:41472\n\t"
    "v_cvt_pk_bf16_f32 v212, v218, v219\n\t"
    "v_add_f32 v229, v229, v218\n\t"
    "v_mfma_f32_32x32x16_bf16 v[128:143], a[112:115], a[16:19], v[128:143]\n\t"
    "buffer_load_dwordx4 v239, s[20:23], s34 offen offset:1024 lds\n\t"
    "v_add_f32 v229, v229, v219\n\t"
    "v_fma_f32 v218, v186, s63, v225\n\t"
    "v_fma_f32 v219, v187, s63, v225\n\t"
    "v_mfma_f32_32x32x16_bf16 v[144:159], a[112:115], a[64:67], v[144:159]\n\t"
    "ds_read_b64_tr_b16 a[112:113], v234 offset:38912\n\t"
    "ds_read_b64_tr_b16 a[114:115], v234 offset:41984\n\t"
    "v_exp_f32 v218, v218\n\t"
    "v_exp_f32 v219, v219\n\t"
    "v_mfma_f32_32x32x16_bf16 v[128:143], a[116:119], a[20:23], v[128:143]\n\t"
    "v_cvt_pk_bf16_f32 v209, v216, v217\n\t"
    "v_add_f32 v228, v228, v216\n\t"
    "v_add_f32 v228, v228, v217\n\t"
    "v_fma_f32 v216, v172, s63, v224\n\t"
    "v_mfma_f32_32x32x16_bf16 v[144:159], a[116:119], a[68:71], v[144:159]\n\t"
    "ds_read_b64_tr_b16 a[116:117], v234 offset:39424\n\t"
    "ds_read_b64_tr_b16 a[118:119], v234 offset:42496\n\t"
    "v_fma_f32 v217, v173, s63, v224\n\t"
    "v_exp_f32 v216, v216\n\t"
    "v_mfma_f32_32x32x16_bf16 v[128:143], a[120:123], a[24:27], v[128:143]\n\t"
    "buffer_load_dwordx4 v240, s[20:23], s34 offen offset:2048 lds\n\t"
    "s_add_u32 s34, s34, 12288\n\t"
    "v_exp_f32 v217, v217\n\t"
    "v_cvt_pk_bf16_f32 v213, v218, v219\n\t"
    "v_mfma_f32_32x32x16_bf16 v[144:159], a[120:123], a[72:75], v[144:159]\n\t"
    "ds_read_b64_tr_b16 a[120:121], v234 offset:43008\n\t"
    "ds_read_b64_tr_b16 a[122:123], v234 offset:46080\n\t"
    "v_add_f32 v229, v229, v218\n\t"
    "v_add_f32 v229, v229, v219\n\t"
    "v_mfma_f32_32x32x16_bf16 v[128:143], a[124:127], a[28:31], v[128:143]\n\t"
    "v_fma_f32 v218, v188, s63, v225\n\t"
    "v_fma_f32 v219, v189, s63, v225\n\t"
    "v_exp_f32 v218, v218\n\t"
    "v_exp_f32 v219, v219\n\t"
    "v_mfma_f32_32x32x16_bf16 v[144:159], a[124:127], a[76:79], v[144:159]\n\t"
    "ds_read_b64_tr_b16 a[124:125], v234 offset:43520\n\t"
    "ds_read_b64_tr_b16 a[126:127], v234 offset:46592\n\t"
    "v_cvt_pk_bf16_f32 v210, v216, v217\n\t"
    "v_add_f32 v228, v228, v216\n\t"
    "v_mfma_f32_32x32x16_bf16 v[128:143], a[128:131], a[32:35], v[128:143]\n\t"
    "s_mov_b32 m0, s46\n\t"
    "s_nop 0\n\t"
    "buffer_load_dwordx4 v235, s[16:19], s32 offen lds\n\t"
    "v_add_f32 v228, v228, v217\n\t"
    "v_fma_f32 v216, v174, s63, v224\n\t"
    "v_mfma_f32_32x32x16_bf16 v[144:159], a[128:131], a[80:83], v[144:159]\n\t"
    "ds_read_b64_tr_b16 a[128:129], v234 offset:44032\n\t"
    "ds_read_b64_tr_b16 a[130:131], v234 offset:47104\n\t"
    "v_fma_f32 v217, v175, s63, v224\n\t"
    "v_exp_f32 v216, v216\n\t"
    "v_mfma_f32_32x32x16_bf16 v[128:143], a[132:135], a[36:39], v[128:143]\n\t"
    "v_exp_f32 v217, v217\n\t"
    "v_cvt_pk_bf16_f32 v214, v218, v219\n\t"
    "v_add_f32 v229, v229, v218\n\t"
    "v_mfma_f32_32x32x16_bf16 v[144:159], a[132:135], a[84:87], v[144:159]\n\t"
    "ds_read_b64_tr_b16 a[132:133], v234 offset:44544\n\t"
    "ds_read_b64_tr_b16 a[134:135], v234 offset:47616\n\t"
    "v_add_f32 v229, v229, v219\n\t"
    "v_fma_f32 v218, v190, s63, v225\n\t"
    "v_fma_f32 v219, v191, s63, v225\n\t"
    "v_mfma_f32_32x32x16_bf16 v[128:143], a[136:139], a[40:43], v[128:143]\n\t"
    "buffer_load_dwordx4 v236, s[16:19], s32 offen offset:1024 lds\n\t"
    "v_exp_f32 v218, v218\n\t"
    "v_exp_f32 v219, v219\n\t"
    "v_mfma_f32_32x32x16_bf16 v[144:159], a[136:139], a[88:91], v[144:159]\n\t"
    "ds_read_b64_tr_b16 a[136:137], v234 offset:45056\n\t"
    "ds_read_b64_tr_b16 a[138:139], v234 offset:48128\n\t"
    "v_cvt_pk_bf16_f32 v211, v216, v217\n\t"
    "v_add_f32 v228, v228, v216\n\t"
    "v_mfma_f32_32x32x16_bf16 v[128:143], a[140:143], a[44:47], v[128:143]\n\t"
    "v_add_f32 v228, v228, v217\n\t"
    "s_nop 0\n\t"
    "v_cvt_pk_bf16_f32 v215, v218, v219\n\t"
    "v_add_f32 v229, v229, v218\n\t"
    "v_add_f32 v229, v229, v219\n\t"
    "v_mfma_f32_32x32x16_bf16 v[144:159], a[140:143], a[92:95], v[144:159]\n\t"
    "ds_read_b64_tr_b16 a[140:141], v234 offset:45568\n\t"
    "ds_read_b64_tr_b16 a[142:143], v234 offset:48640\n\t"
    "v_add_f32 v251, v228, v229\n\t"
    "v_cmp_nge_f32 vcc, s64, v251\n\t"
    "s_cbranch_vccnz .Ld_stub6\n\t"
    ".Ld_back6:\n\t"
    "s_waitcnt lgkmcnt(12)\n\t"
    "v_mfma_f32_32x32x16_bf16 v[0:15], a[96:99], v[200:203], v[0:15]\n\t"
    "buffer_load_dwordx4 v237, s[16:19], s32 offen offset:2048 lds\n\t"
    "s_add_u32 s32, s32, 12288\n\t"
    "v_add_f32 v226, v226, v228\n\t"
    "v_add_f32 v227, v227, v229\n\t"
    "v_mfma_f32_32x32x16_bf16 v[96:111], a[96:99], v[204:207], v[96:111]\n\t"
    "ds_read_b128 a[96:99], v230 offset:12288\n\t"
    "v_mfma_f32_32x32x16_bf16 v[16:31], a[100:103], v[200:203], v[16:31]\n\t"
    "v_mfma_f32_32x32x16_bf16 v[112:127], a[100:103], v[204:207], v[112:127]\n\t"
    "ds_read_b128 a[100:103], v231 offset:12288\n\t"
    "v_fma_f32 v216, v128, s63, v224\n\t"
    "v_fma_f32 v217, v129, s63, v224\n\t"
    "v_exp_f32 v216, v216\n\t"
    "v_mfma_f32_32x32x16_bf16 v[32:47], a[104:107], v[200:203], v[32:47]\n\t"
    "s_add_u32 m0, s61, 6016\n\t"
    "s_nop 0\n\t"
    "buffer_load_dwordx4 v248, s[24:27], s43 offen offset:128 nt lds\n\t"
    "v_mfma_f32_32x32x16_bf16 a[144:159], a[104:107], v[204:207], a[144:159]\n\t"
    "ds_read_b128 a[104:107], v232 offset:12288\n\t"
    "v_exp_f32 v217, v217\n\t"
    "v_fma_f32 v218, v144, s63, v225\n\t"
    "v_fma_f32 v219, v145, s63, v225\n\t"
    "v_mfma_f32_32x32x16_bf16 v[48:63], a[108:111], v[200:203], v[48:63]\n\t"
    "v_exp_f32 v218, v218\n\t"
    "v_exp_f32 v219, v219\n\t"
    "v_cvt_pk_bf16_f32 v192, v216, v217\n\t"
    "v_mfma_f32_32x32x16_bf16 a[160:175], a[108:111], v[204:207], a[160:175]\n\t"
    "ds_read_b128 a[108:111], v233 offset:12288\n\t"
    "v_add_f32 v228, v216, v217\n\t"
    "v_fma_f32 v216, v130, s63, v224\n\t"
    "v_fma_f32 v217, v131, s63, v224\n\t"
    "v_mfma_f32_32x32x16_bf16 v[64:79], a[112:115], v[200:203], v[64:79]\n\t"
    "v_exp_f32 v216, v216\n\t"
    "v_exp_f32 v217, v217\n\t"
    "v_mfma_f32_32x32x16_bf16 a[176:191], a[112:115], v[204:207], a[176:191]\n\t"
    "ds_read_b128 a[112:115], v230 offset:13312\n\t"
    "v_cvt_pk_bf16_f32 v196, v218, v219\n\t"
    "v_add_f32 v229, v218, v219\n\t"
    "v_fma_f32 v218, v146, s63, v225\n\t"
    "v_fma_f32 v219, v147, s63, v225\n\t"
    "v_mfma_f32_32x32x16_bf16 v[80:95], a[116:119], v[200:203], v[80:95]\n\t"
    "s_add_u32 m0, s61, 7040\n\t"
    "s_nop 0\n\t"
    "buffer_load_dwordx4 v249, s[24:27], s43 offen offset:128 nt lds\n\t"
    "v_exp_f32 v218, v218\n\t"
    "v_mfma_f32_32x32x16_bf16 a[192:207], a[116:119], v[204:207], a[192:207]\n\t"
    "ds_read_b128 a[116:119], v231 offset:13312\n\t"
    "v_exp_f32 v219, v219\n\t"
    "v_cvt_pk_bf16_f32 v193, v216, v217\n\t"
    "s_waitcnt lgkmcnt(10)\n\t"
    "v_mfma_f32_32x32x16_bf16 v[0:15], a[120:123], v[208:211], v[0:15]\n\t"
    "ds_read_b128 v[220:223], v241 offset:8192\n\t"
    "v_add_f32 v228, v228, v216\n\t"
    "v_add_f32 v228, v228, v217\n\t"
    "v_fma_f32 v216, v132, s63, v224\n\t"
    "v_mfma_f32_32x32x16_bf16 v[96:111], a[120:123], v[212:215], v[96:111]\n\t"
    "ds_read_b128 a[120:123], v232 offset:13312\n\t"
    "v_fma_f32 v217, v133, s63, v224\n\t"
    "v_exp_f32 v216, v216\n\t"
    "v_mfma_f32_32x32x16_bf16 v[16:31], a[124:127], v[208:211], v[16:31]\n\t"
    "v_exp_f32 v217, v217\n\t"
    "v_cvt_pk_bf16_f32 v197, v218, v219\n\t"
    "v_add_f32 v229, v229, v218\n\t"
    "v_add_f32 v229, v229, v219\n\t"
    "v_mfma_f32_32x32x16_bf16 v[112:127], a[124:127], v[212:215], v[112:127]\n\t"
    "ds_read_b128 a[124:127], v233 offset:13312\n\t"
    "v_fma_f32 v218, v148, s63, v225\n\t"
    "v_fma_f32 v219, v149, s63, v225\n\t"
    "v_exp_f32 v218, v218\n\t"
    "v_mfma_f32_32x32x16_bf16 v[32:47], a[128:131], v[208:211], v[32:47]\n\t"
    "s_waitcnt lgkmcnt(2)\n\t"
    "buffer_store_dwordx4 v[220:223], v248, s[28:31], 0 offen offset:256 nt\n\t"
    "v_exp_f32 v219, v219\n\t"
    "v_cvt_pk_bf16_f32 v194, v216, v217\n\t"
    "v_mfma_f32_32x32x16_bf16 a[144:159], a[128:131], v[212:215], a[144:159]\n\t"
    "ds_read_b128 a[128:131], v230 offset:14336\n\t"
    "v_add_f32 v228, v228, v216\n\t"
    "v_add_f32 v228, v228, v217\n\t"
    "v_fma_f32 v216, v134, s63, v224\n\t"
    "v_mfma_f32_32x32x16_bf16 v[48:63], a[132:135], v[208:211], v[48:63]\n\t"
    "v_fma_f32 v217, v135, s63, v224\n\t"
    "v_exp_f32 v216, v216\n\t"
    "v_exp_f32 v217, v217\n\t"
    "v_mfma_f32_32x32x16_bf16 a[160:175], a[132:135], v[212:215], a[160:175]\n\t"
    "ds_read_b128 a[132:135], v231 offset:14336\n\t"
    "v_cvt_pk_bf16_f32 v198, v218, v219\n\t"
    "v_add_f32 v229, v229, v218\n\t"
    "v_add_f32 v229, v229, v219\n\t"
    "v_mfma_f32_32x32x16_bf16 v[64:79], a[136:139], v[208:211], v[64:79]\n\t"
    "v_fma_f32 v218, v150, s63, v225\n\t"
    "v_fma_f32 v219, v151, s63, v225\n\t"
    "v_exp_f32 v218, v218\n\t"
    "v_mfma_f32_32x32x16_bf16 a[176:191], a[136:139], v[212:215], a[176:191]\n\t"
    "ds_read_b128 a[136:139], v232 offset:14336\n\t"
    "ds_read_b128 v[252:255], v241 offset:9216\n\t"
    "v_exp_f32 v219, v219\n\t"
    "v_cvt_pk_bf16_f32 v195, v216, v217\n\t"
    "v_mfma_f32_32x32x16_bf16 v[80:95], a[140:143], v[208:211], v[80:95]\n\t"
    "v_add_f32 v228, v228, v216\n\t"
    "v_add_f32 v228, v228, v217\n\t"
    "s_nop 0\n\t"
    "v_cvt_pk_bf16_f32 v199, v218, v219\n\t"
    "v_mfma_f32_32x32x16_bf16 a[192:207], a[140:143], v[212:215], a[192:207]\n\t"
    "ds_read_b128 a[140:143], v233 offset:14336\n\t"
    "v_add_f32 v229, v229, v218\n\t"
    "v_add_f32 v229, v229, v219\n\t"
    "v_mfma_f32_32x32x16_bf16 v[160:175], a[96:99], a[0:3], 0\n\t"
    "v_fma_f32 v216, v136, s63, v224\n\t"
    "v_fma_f32 v217, v137, s63, v224\n\t"
    "v_exp_f32 v216, v216\n\t"
    "v_mfma_f32_32x32x16_bf16 v[176:191], a[96:99], a[48:51], 0\n\t"
    "s_waitcnt lgkmcnt(1)\n\t"
    "buffer_store_dwordx4 v[252:255], v249, s[28:31], 0 offen offset:256 nt\n\t"
    "v_exp_f32 v217, v217\n\t"
    "v_fma_f32 v218, v152, s63, v225\n\t"
    "v_fma_f32 v219, v153, s63, v225\n\t"
    "v_mfma_f32_32x32x16_bf16 v[160:175], a[100:103], a[4:7], v[160:175]\n\t"
    "v_exp_f32 v218, v218\n\t"
    "v_exp_f32 v219, v219\n\t"
    "v_cvt_pk_bf16_f32 v208, v216, v217\n\t"
    "v_mfma_f32_32x32x16_bf16 v[176:191], a[100:103], a[52:55], v[176:191]\n\t"
    "v_add_f32 v228, v228, v216\n\t"
    "s_waitcnt vmcnt(23) lgkmcnt(0)\n\t"
    "s_barrier\n\t"
    "v_mfma_f32_32x32x16_bf16 v[160:175], a[104:107], a[8:11], v[160:175]\n\t"
    "ds_read_b64_tr_b16 a[96:97], v234 offset:0\n\t"
    "ds_read_b64_tr_b16 a[98:99], v234 offset:3072\n\t"
    "s_add_u32 m0, s49, 36864\n\t"
    "s_nop 0\n\t"
    "buffer_load_dwordx4 v238, s[20:23], s34 offen lds\n\t"
    "v_mfma_f32_32x32x16_bf16 v[176:191], a[104:107], a[56:59], v[176:191]\n\t"
    "ds_read_b64_tr_b16 a[100:101], v234 offset:512\n\t"
    "ds_read_b64_tr_b16 a[102:103], v234 offset:3584\n\t"
    "v_add_f32 v228, v228, v217\n\t"
    "v_fma_f32 v216, v138, s63, v224\n\t"
    "v_fma_f32 v217, v139, s63, v224\n\t"
    "v_mfma_f32_32x32x16_bf16 v[160:175], a[108:111], a[12:15], v[160:175]\n\t"
    "ds_read_b64_tr_b16 a[104:105], v234 offset:1024\n\t"
    "ds_read_b64_tr_b16 a[106:107], v234 offset:4096\n\t"
    "v_exp_f32 v216, v216\n\t"
    "v_mfma_f32_32x32x16_bf16 v[176:191], a[108:111], a[60:63], v[176:191]\n\t"
    "ds_read_b64_tr_b16 a[108:109], v234 offset:1536\n\t"
    "ds_read_b64_tr_b16 a[110:111], v234 offset:4608\n\t"
    "v_exp_f32 v217, v217\n\t"
    "v_cvt_pk_bf16_f32 v212, v218, v219\n\t"
    "v_mfma_f32_32x32x16_bf16 v[160:175], a[112:115], a[16:19], v[160:175]\n\t"
    "buffer_load_dwordx4 v239, s[20:23], s34 offen offset:1024 lds\n\t"
    "v_add_f32 v229, v229, v218\n\t"
    "v_add_f32 v229, v229, v219\n\t"
    "v_fma_f32 v218, v154, s63, v225\n\t"
    "v_fma_f32 v219, v155, s63, v225\n\t"
    "v_mfma_f32_32x32x16_bf16 v[176:191], a[112:115], a[64:67], v[176:191]\n\t"
    "ds_read_b64_tr_b16 a[112:113], v234 offset:2048\n\t"
    "ds_read_b64_tr_b16 a[114:115], v234 offset:5120\n\t"
    "v_exp_f32 v218, v218\n\t"
    "v_mfma_f32_32x32x16_bf16 v[160:175], a[116:119], a[20:23], v[160:175]\n\t"
    "v_exp_f32 v219, v219\n\t"
    "v_cvt_pk_bf16_f32 v209, v216, v217\n\t"
    "v_add_f32 v228, v228, v216\n\t"
    "v_add_f32 v228, v228, v217\n\t"
    "v_fma_f32 v216, v140, s63, v224\n\t"
    "v_mfma_f32_32x32x16_bf16 v[176:191], a[116:119], a[68:71], v[176:191]\n\t"
    "ds_read_b64_tr_b16 a[116:117], v234 offset:2560\n\t"
    "ds_read_b64_tr_b16 a[118:119], v234 offset:5632\n\t"
    "v_fma_f32 v217, v141, s63, v224\n\t"
    "v_exp_f32 v216, v216\n\t"
    "v_mfma_f32_32x32x16_bf16 v[160:175], a[120:123], a[24:27], v[160:175]\n\t"
    "buffer_load_dwordx4 v240, s[20:23], s34 offen offset:2048 lds\n\t"
    "s_add_u32 s34, s34, 12288\n\t"
    "v_exp_f32 v217, v217\n\t"
    "v_mfma_f32_32x32x16_bf16 v[176:191], a[120:123], a[72:75], v[176:191]\n\t"
    "ds_read_b64_tr_b16 a[120:121], v234 offset:6144\n\t"
    "ds_read_b64_tr_b16 a[122:123], v234 offset:9216\n\t"
    "v_cvt_pk_bf16_f32 v213, v218, v219\n\t"
    "v_add_f32 v229, v229, v218\n\t"
    "v_add_f32 v229, v229, v219\n\t"
    "v_mfma_f32_32x32x16_bf16 v[160:175], a[124:127], a[28:31], v[160:175]\n\t"
    "v_fma_f32 v218, v156, s63, v225\n\t"
    "v_fma_f32 v219, v157, s63, v225\n\t"
    "v_exp_f32 v218, v218\n\t"
    "v_mfma_f32_32x32x16_bf16 v[176:191], a[124:127], a[76:79], v[176:191]\n\t"
    "ds_read_b64_tr_b16 a[124:125], v234 offset:6656\n\t"
    "ds_read_b64_tr_b16 a[126:127], v234 offset:9728\n\t"
    "v_exp_f32 v219, v219\n\t"
    "v_cvt_pk_bf16_f32 v210, v216, v217\n\t"
    "v_add_f32 v228, v228, v216\n\t"
    "v_mfma_f32_32x32x16_bf16 v[160:175], a[128:131], a[32:35], v[160:175]\n\t"
    "s_add_u32 m0, s46, 12288\n\t"
    "s_nop 0\n\t"
    "buffer_load_dwordx4 v235, s[16:19], s32 offen lds\n\t"
    "v_add_f32 v228, v228, v217\n\t"
    "v_fma_f32 v216, v142, s63, v224\n\t"
    "v_mfma_f32_32x32x16_bf16 v[176:191], a[128:131], a[80:83], v[176:191]\n\t"
    "ds_read_b64_tr_b16 a[128:129], v234 offset:7168\n\t"
    "ds_read_b64_tr_b16 a[130:131], v234 offset:10240\n\t"
    "v_fma_f32 v217, v143, s63, v224\n\t"
    "v_exp_f32 v216, v216\n\t"
    "v_mfma_f32_32x32x16_bf16 v[160:175], a[132:135], a[36:39], v[160:175]\n\t"
    "v_exp_f32 v217, v217\n\t"
    "v_cvt_pk_bf16_f32 v214, v218, v219\n\t"
    "v_add_f32 v229, v229, v218\n\t"
    "v_mfma_f32_32x32x16_bf16 v[176:191], a[132:135], a[84:87], v[176:191]\n\t"
    "ds_read_b64_tr_b16 a[132:133], v234 offset:7680\n\t"
    "ds_read_b64_tr_b16 a[134:135], v234 offset:10752\n\t"
    "v_add_f32 v229, v229, v219\n\t"
    "v_fma_f32 v218, v158, s63, v225\n\t"
    "v_fma_f32 v219, v159, s63, v225\n\t"
    "v_mfma_f32_32x32x16_bf16 v[160:175], a[136:139], a[40:43], v[160:175]\n\t"
    "buffer_load_dwordx4 v236, s[16:19], s32 offen offset:1024 lds\n\t"
    "v_exp_f32 v218, v218\n\t"
    "v_exp_f32 v219, v219\n\t"
    "v_mfma_f32_32x32x16_bf16 v[176:191], a[136:139], a[88:91], v[176:191]\n\t"
    "ds_read_b64_tr_b16 a[136:137], v234 offset:8192\n\t"
    "ds_read_b64_tr_b16 a[138:139], v234 offset:11264\n\t"
    "v_cvt_pk_bf16_f32 v211, v216, v217\n\t"
    "v_add_f32 v228, v228, v216\n\t"
    "v_add_f32 v228, v228, v217\n\t"
    "v_mfma_f32_32x32x16_bf16 v[160:175], a[140:143], a[44:47], v[160:175]\n\t"
    "s_nop 0\n\t"
    "v_cvt_pk_bf16_f32 v215, v218, v219\n\t"
    "v_add_f32 v229, v229, v218\n\t"
    "v_add_f32 v229, v229, v219\n\t"
    "v_mfma_f32_32x32x16_bf16 v[176:191], a[140:143], a[92:95], v[176:191]\n\t"
    "ds_read_b64_tr_b16 a[140:141], v234 offset:8704\n\t"
    "ds_read_b64_tr_b16 a[142:143], v234 offset:11776\n\t"
    "v_add_f32 v251, v228, v229\n\t"
    "v_cmp_nge_f32 vcc, s64, v251\n\t"
    "s_cbranch_vccnz .Ld_stub7\n\t"
    ".Ld_back7:\n\t"
    "s_waitcnt lgkmcnt(12)\n\t"
    "v_mfma_f32_32x32x16_bf16 v[0:15], a[96:99], v[192:195], v[0:15]\n\t"
    "buffer_load_dwordx4 v237, s[16:19], s32 offen offset:2048 lds\n\t"
    "s_add_u32 s32, s32, 12288\n\t"
    "v_add_f32 v226, v226, v228\n\t"
    "v_add_f32 v227, v227, v229\n\t"
    "v_mfma_f32_32x32x16_bf16 v[96:111], a[96:99], v[196:199], v[96:111]\n\t"
    "ds_read_b128 a[96:99], v230 offset:24576\n\t"
    "v_mfma_f32_32x32x16_bf16 v[16:31], a[100:103], v[192:195], v[16:31]\n\t"
    "v_mfma_f32_32x32x16_bf16 v[112:127], a[100:103], v[196:199], v[112:127]\n\t"
    "ds_read_b128 a[100:103], v231 offset:24576\n\t"
    "ds_read_b128 v[220:223], v241 offset:10240\n\t"
    "v_fma_f32 v216, v160, s63, v224\n\t"
    "v_fma_f32 v217, v161, s63, v224\n\t"
    "v_mfma_f32_32x32x16_bf16 v[32:47], a[104:107], v[192:195], v[32:47]\n\t"
    "v_exp_f32 v216, v216\n\t"
    "v_exp_f32 v217, v217\n\t"
    "v_fma_f32 v218, v176, s63, v225\n\t"
    "v_mfma_f32_32x32x16_bf16 a[144:159], a[104:107], v[196:199], a[144:159]\n\t"
    "ds_read_b128 a[104:107], v232 offset:24576\n\t"
    "v_fma_f32 v219, v177, s63, v225\n\t"
    "v_exp_f32 v218, v218\n\t"
    "v_exp_f32 v219, v219\n\t"
    "v_mfma_f32_32x32x16_bf16 v[48:63], a[108:111], v[192:195], v[48:63]\n\t"
    "v_cvt_pk_bf16_f32 v200, v216, v217\n\t"
    "v_add_f32 v228, v216, v217\n\t"
    "v_fma_f32 v216, v162, s63, v224\n\t"
    "v_fma_f32 v217, v163, s63, v224\n\t"
    "v_mfma_f32_32x32x16_bf16 a[160:175], a[108:111], v[196:199], a[160:175]\n\t"
    "s_waitcnt lgkmcnt(1)\n\t"
    "buffer_store_dwordx4 v[220:223], v248, s[28:31], s86 offen offset:256 nt\n\t"
    "ds_read_b128 a[108:111], v233 offset:24576\n\t"
    "v_exp_f32 v216, v216\n\t"
    "v_mfma_f32_32x32x16_bf16 v[64:79], a[112:115], v[192:195], v[64:79]\n\t"
    "v_exp_f32 v217, v217\n\t"
    "v_cvt_pk_bf16_f32 v204, v218, v219\n\t"
    "v_add_f32 v229, v218, v219\n\t"
    "v_fma_f32 v218, v178, s63, v225\n\t"
    "v_mfma_f32_32x32x16_bf16 a[176:191], a[112:115], v[196:199], a[176:191]\n\t"
    "ds_read_b128 a[112:115], v230 offset:25600\n\t"
    "ds_read_b128 v[252:255], v241 offset:11264\n\t"
    "v_fma_f32 v219, v179, s63, v225\n\t"
    "v_exp_f32 v218, v218\n\t"
    "v_mfma_f32_32x32x16_bf16 v[80:95], a[116:119], v[192:195], v[80:95]\n\t"
    "s_waitcnt vmcnt(9)\n\t"
    "ds_read_b128 a[224:227], v244 offset:4096\n\t"
    "ds_write_b128 v244, a[240:243] offset:8192\n\t"
    "v_exp_f32 v219, v219\n\t"
    "v_mfma_f32_32x32x16_bf16 a[192:207], a[116:119], v[196:199], a[192:207]\n\t"
    "ds_read_b128 a[116:119], v231 offset:25600\n\t"
    "v_cvt_pk_bf16_f32 v201, v216, v217\n\t"
    "v_add_f32 v228, v228, v216\n\t"
    "v_add_f32 v228, v228, v217\n\t"
    "v_mfma_f32_32x32x16_bf16 v[0:15], a[120:123], v[208:211], v[0:15]\n\t"
    "ds_read_b128 a[228:231], v245 offset:4096\n\t"
    "ds_write_b128 v245, a[244:247] offset:8192\n\t"
    "v_fma_f32 v216, v164, s63, v224\n\t"
    "v_fma_f32 v217, v165, s63, v224\n\t"
    "v_mfma_f32_32x32x16_bf16 v[96:111], a[120:123], v[212:215], v[96:111]\n\t"
    "s_waitcnt lgkmcnt(5)\n\t"
    "buffer_store_dwordx4 v[252:255], v249, s[28:31], s86 offen offset:256 nt\n\t"
    "ds_read_b128 a[120:123], v232 offset:25600\n\t"
    "v_exp_f32 v216, v216\n\t"
    "v_exp_f32 v217, v217\n\t"
    "v_mfma_f32_32x32x16_bf16 v[16:31], a[124:127], v[208:211], v[16:31]\n\t"
    "ds_read_b128 a[232:235], v246 offset:4096\n\t"
    "ds_write_b128 v246, a[248:251] offset:8192\n\t"
    "v_cvt_pk_bf16_f32 v205, v218, v219\n\t"
    "v_add_f32 v229, v229, v218\n\t"
    "v_mfma_f32_32x32x16_bf16 v[112:127], a[124:127], v[212:215], v[112:127]\n\t"
    "ds_read_b128 a[124:127], v233 offset:25600\n\t"
    "v_add_f32 v229, v229, v219\n\t"
    "v_fma_f32 v218, v180, s63, v225\n\t"
    "v_fma_f32 v219, v181, s63, v225\n\t"
    "v_exp_f32 v218, v218\n\t"
    "v_mfma_f32_32x32x16_bf16 v[32:47], a[128:131], v[208:211], v[32:47]\n\t"
    "ds_read_b128 a[236:239], v247 offset:4096\n\t"
    "ds_write_b128 v247, a[252:255] offset:8192\n\t"
    "v_exp_f32 v219, v219\n\t"
    "v_mfma_f32_32x32x16_bf16 a[144:159], a[128:131], v[212:215], a[144:159]\n\t"
    "ds_read_b128 a[128:131], v230 offset:26624\n\t"
    "v_cvt_pk_bf16_f32 v202, v216, v217\n\t"
    "v_add_f32 v228, v228, v216\n\t"
    "v_add_f32 v228, v228, v217\n\t"
    "v_mfma_f32_32x32x16_bf16 v[48:63], a[132:135], v[208:211], v[48:63]\n\t"
    "v_fma_f32 v216, v166, s63, v224\n\t"
    "v_fma_f32 v217, v167, s63, v224\n\t"
    "v_exp_f32 v216, v216\n\t"
    "v_exp_f32 v217, v217\n\t"
    "v_mfma_f32_32x32x16_bf16 a[160:175], a[132:135], v[212:215], a[160:175]\n\t"
    "ds_read_b128 a[132:135], v231 offset:26624\n\t"
    "s_waitcnt lgkmcnt(3)\n\t"
    "s_add_u32 m0, s61, 3968\n\t"
    "s_nop 0\n\t"
    "buffer_load_dwordx4 v248, s[24:27], 0 offen offset:128 nt lds\n\t"
    "v_mfma_f32_32x32x16_bf16 v[64:79], a[136:139], v[208:211], v[64:79]\n\t"
    "v_cvt_pk_bf16_f32 v206, v218, v219\n\t"
    "v_add_f32 v229, v229, v218\n\t"
    "v_add_f32 v229, v229, v219\n\t"
    "v_fma_f32 v218, v182, s63, v225\n\t"
    "v_fma_f32 v219, v183, s63, v225\n\t"
    "v_mfma_f32_32x32x16_bf16 a[176:191], a[136:139], v[212:215], a[176:191]\n\t"
    "ds_read_b128 a[136:139], v232 offset:26624\n\t"
    "v_exp_f32 v218, v218\n\t"
    "v_exp_f32 v219, v219\n\t"
    "v_mfma_f32_32x32x16_bf16 v[80:95], a[140:143], v[208:211], v[80:95]\n\t"
    "v_cvt_pk_bf16_f32 v203, v216, v217\n\t"
    "v_add_f32 v228, v228, v216\n\t"
    "v_add_f32 v228, v228, v217\n\t"
    "s_nop 0\n\t"
    "v_mfma_f32_32x32x16_bf16 a[192:207], a[140:143], v[212:215], a[192:207]\n\t"
    "ds_read_b128 a[140:143], v233 offset:26624\n\t"
    "v_cvt_pk_bf16_f32 v207, v218, v219\n\t"
    "v_add_f32 v229, v229, v218\n\t"
    "v_add_f32 v229, v229, v219\n\t"
    "v_mfma_f32_32x32x16_bf16 v[128:143], a[96:99], a[0:3], 0\n\t"
    "v_fma_f32 v216, v168, s63, v224\n\t"
    "v_fma_f32 v217, v169, s63, v224\n\t"
    "v_exp_f32 v216, v216\n\t"
    "v_mfma_f32_32x32x16_bf16 v[144:159], a[96:99], a[48:51], 0\n\t"
    "s_add_u32 m0, s61, 4992\n\t"
    "s_nop 0\n\t"
    "buffer_load_dwordx4 v249, s[24:27], 0 offen offset:128 nt lds\n\t"
    "v_exp_f32 v217, v217\n\t"
    "v_fma_f32 v218, v184, s63, v225\n\t"
    "v_mfma_f32_32x32x16_bf16 v[128:143], a[100:103], a[4:7], v[128:143]\n\t"
    "v_fma_f32 v219, v185, s63, v225\n\t"
    "v_exp_f32 v218, v218\n\t"
    "v_exp_f32 v219, v219\n\t"
    "v_mfma_f32_32x32x16_bf16 v[144:159], a[100:103], a[52:55], v[144:159]\n\t"
    "v_cvt_pk_bf16_f32 v208, v216, v217\n\t"
    "s_waitcnt vmcnt(23) lgkmcnt(0)\n\t"
    "s_barrier\n\t"
    "v_mfma_f32_32x32x16_bf16 v[128:143], a[104:107], a[8:11], v[128:143]\n\t"
    "ds_read_b64_tr_b16 a[96:97], v234 offset:12288\n\t"
    "ds_read_b64_tr_b16 a[98:99], v234 offset:15360\n\t"
    "s_mov_b32 m0, s49\n\t"
    "s_nop 0\n\t"
    "buffer_load_dwordx4 v238, s[20:23], s34 offen lds\n\t"
    "v_mfma_f32_32x32x16_bf16 v[144:159], a[104:107], a[56:59], v[144:159]\n\t"
    "ds_read_b64_tr_b16 a[100:101], v234 offset:12800\n\t"
    "ds_read_b64_tr_b16 a[102:103], v234 offset:15872\n\t"
    "v_add_f32 v228, v228, v216\n\t"
    "v_add_f32 v228, v228, v217\n\t"
    "v_mfma_f32_32x32x16_bf16 v[128:143], a[108:111], a[12:15], v[128:143]\n\t"
    "ds_read_b64_tr_b16 a[104:105], v234 offset:13312\n\t"
    "ds_read_b64_tr_b16 a[106:107], v234 offset:16384\n\t"
    "v_fma_f32 v216, v170, s63, v224\n\t"
    "v_fma_f32 v217, v171, s63, v224\n\t"
    "v_exp_f32 v216, v216\n\t"
    "v_mfma_f32_32x32x16_bf16 v[144:159], a[108:111], a[60:63], v[144:159]\n\t"
    "ds_read_b64_tr_b16 a[108:109], v234 offset:13824\n\t"
    "ds_read_b64_tr_b16 a[110:111], v234 offset:16896\n\t"
    "v_exp_f32 v217, v217\n\t"
    "v_mfma_f32_32x32x16_bf16 v[128:143], a[112:115], a[16:19], v[128:143]\n\t"
    "buffer_load_dwordx4 v239, s[20:23], s34 offen offset:1024 lds\n\t"
    "v_cvt_pk_bf16_f32 v212, v218, v219\n\t"
    "v_add_f32 v229, v229, v218\n\t"
    "v_add_f32 v229, v229, v219\n\t"
    "v_fma_f32 v218, v186, s63, v225\n\t"
    "v_mfma_f32_32x32x16_bf16 v[144:159], a[112:115], a[64:67], v[144:159]\n\t"
    "ds_read_b64_tr_b16 a[112:113], v234 offset:14336\n\t"
    "ds_read_b64_tr_b16 a[114:115], v234 offset:17408\n\t"
    "v_fma_f32 v219, v187, s63, v225\n\t"
    "v_exp_f32 v218, v218\n\t"
    "v_mfma_f32_32x32x16_bf16 v[128:143], a[116:119], a[20:23], v[128:143]\n\t"
    "v_exp_f32 v219, v219\n\t"
    "v_cvt_pk_bf16_f32 v209, v216, v217\n\t"
    "v_add_f32 v228, v228, v216\n\t"
    "v_add_f32 v228, v228, v217\n\t"
    "v_mfma_f32_32x32x16_bf16 v[144:159], a[116:119], a[68:71], v[144:159]\n\t"
    "ds_read_b64_tr_b16 a[116:117], v234 offset:14848\n\t"
    "ds_read_b64_tr_b16 a[118:119], v234 offset:17920\n\t"
    "v_fma_f32 v216, v172, s63, v224\n\t"
    "v_fma_f32 v217, v173, s63, v224\n\t"
    "v_mfma_f32_32x32x16_bf16 v[128:143], a[120:123], a[24:27], v[128:143]\n\t"
    "buffer_load_dwordx4 v240, s[20:23], s34 offen offset:2048 lds\n\t"
    "s_add_u32 s34, s34, 12288\n\t"
    "v_exp_f32 v216, v216\n\t"
    "v_exp_f32 v217, v217\n\t"
    "v_mfma_f32_32x32x16_bf16 v[144:159], a[120:123], a[72:75], v[144:159]\n\t"
    "ds_read_b64_tr_b16 a[120:121], v234 offset:18432\n\t"
    "ds_read_b64_tr_b16 a[122:123], v234 offset:21504\n\t"
    "v_cvt_pk_bf16_f32 v213, v218, v219\n\t"
    "v_add_f32 v229, v229, v218\n\t"
    "v_add_f32 v229, v229, v219\n\t"
    "v_mfma_f32_32x32x16_bf16 v[128:143], a[124:127], a[28:31], v[128:143]\n\t"
    "v_fma_f32 v218, v188, s63, v225\n\t"
    "v_fma_f32 v219, v189, s63, v225\n\t"
    "v_exp_f32 v218, v218\n\t"
    "v_mfma_f32_32x32x16_bf16 v[144:159], a[124:127], a[76:79], v[144:159]\n\t"
    "ds_read_b64_tr_b16 a[124:125], v234 offset:18944\n\t"
    "ds_read_b64_tr_b16 a[126:127], v234 offset:22016\n\t"
    "v_exp_f32 v219, v219\n\t"
    "v_cvt_pk_bf16_f32 v210, v216, v217\n\t"
    "v_mfma_f32_32x32x16_bf16 v[128:143], a[128:131], a[32:35], v[128:143]\n\t"
    "s_add_u32 m0, s46, 24576\n\t"
    "s_nop 0\n\t"
    "buffer_load_dwordx4 v235, s[16:19], s32 offen lds\n\t"
    "v_add_f32 v228, v228, v216\n\t"
    "v_add_f32 v228, v228, v217\n\t"
    "v_mfma_f32_32x32x16_bf16 v[144:159], a[128:131], a[80:83], v[144:159]\n\t"
    "ds_read_b64_tr_b16 a[128:129], v234 offset:19456\n\t"
    "ds_read_b64_tr_b16 a[130:131], v234 offset:22528\n\t"
    "v_fma_f32 v216, v174, s63, v224\n\t"
    "v_fma_f32 v217, v175, s63, v224\n\t"
    "v_exp_f32 v216, v216\n\t"
    "v_mfma_f32_32x32x16_bf16 v[128:143], a[132:135], a[36:39], v[128:143]\n\t"
    "v_exp_f32 v217, v217\n\t"
    "v_cvt_pk_bf16_f32 v214, v218, v219\n\t"
    "v_add_f32 v229, v229, v218\n\t"
    "v_mfma_f32_32x32x16_bf16 v[144:159], a[132:135], a[84:87], v[144:159]\n\t"
    "ds_read_b64_tr_b16 a[132:133], v234 offset:19968\n\t"
    "ds_read_b64_tr_b16 a[134:135], v234 offset:23040\n\t"
    "v_add_f32 v229, v229, v219\n\t"
    "v_fma_f32 v218, v190, s63, v225\n\t"
    "v_fma_f32 v219, v191, s63, v225\n\t"
    "v_mfma_f32_32x32x16_bf16 v[128:143], a[136:139], a[40:43], v[128:143]\n\t"
    "buffer_load_dwordx4 v236, s[16:19], s32 offen offset:1024 lds\n\t"
    "v_exp_f32 v218, v218\n\t"
    "v_exp_f32 v219, v219\n\t"
    "v_mfma_f32_32x32x16_bf16 v[144:159], a[136:139], a[88:91], v[144:159]\n\t"
    "ds_read_b64_tr_b16 a[136:137], v234 offset:20480\n\t"
    "ds_read_b64_tr_b16 a[138:139], v234 offset:23552\n\t"
    "v_cvt_pk_bf16_f32 v211, v216, v217\n\t"
    "v_add_f32 v228, v228, v216\n\t"
    "v_add_f32 v228, v228, v217\n\t"
    "v_mfma_f32_32x32x16_bf16 v[128:143], a[140:143], a[44:47], v[128:143]\n\t"
    "s_nop 0\n\t"
    "v_cvt_pk_bf16_f32 v215, v218, v219\n\t"
    "v_add_f32 v229, v229, v218\n\t"
    "v_add_f32 v229, v229, v219\n\t"
    "v_add_f32 v251, v228, v229\n\t"
    "v_mfma_f32_32x32x16_bf16 v[144:159], a[140:143], a[92:95], v[144:159]\n\t"
    "ds_read_b64_tr_b16 a[140:141], v234 offset:20992\n\t"
    "ds_read_b64_tr_b16 a[142:143], v234 offset:24064\n\t"
    "v_cmp_nge_f32 vcc, s64, v251\n\t"
    "s_cbranch_vccnz .Ld_stub8\n\t"
    ".Ld_back8:\n\t"
    "s_waitcnt lgkmcnt(12)\n\t"
    "v_mfma_f32_32x32x16_bf16 v[0:15], a[96:99], v[200:203], v[0:15]\n\t"
    "buffer_load_dwordx4 v237, s[16:19], s32 offen offset:2048 lds\n\t"
    "s_add_u32 s32, s32, 12288\n\t"
    "v_add_f32 v226, v226, v228\n\t"
    "v_add_f32 v227, v227, v229\n\t"
    "v_mfma_f32_32x32x16_bf16 v[96:111], a[96:99], v[204:207], v[96:111]\n\t"
    "ds_read_b128 a[96:99], v230 offset:36864\n\t"
    "v_mfma_f32_32x32x16_bf16 v[16:31], a[100:103], v[200:203], v[16:31]\n\t"
    "v_mfma_f32_32x32x16_bf16 v[112:127], a[100:103], v[204:207], v[112:127]\n\t"
    "ds_read_b128 a[100:103], v231 offset:36864\n\t"
    "v_fma_f32 v216, v128, s63, v224\n\t"
    "v_fma_f32 v217, v129, s63, v224\n\t"
    "v_exp_f32 v216, v216\n\t"
    "v_mfma_f32_32x32x16_bf16 v[32:47], a[104:107], v[200:203], v[32:47]\n\t"
    "v_exp_f32 v217, v217\n\t"
    "v_fma_f32 v218, v144, s63, v225\n\t"
    "v_fma_f32 v219, v145, s63, v225\n\t"
    "v_mfma_f32_32x32x16_bf16 a[144:159], a[104:107], v[204:207], a[144:159]\n\t"
    "ds_read_b128 a[104:107], v232 offset:36864\n\t"
    "v_exp_f32 v218, v218\n\t"
    "v_exp_f32 v219, v219\n\t"
    "v_mfma_f32_32x32x16_bf16 v[48:63], a[108:111], v[200:203], v[48:63]\n\t"
    "v_cvt_pk_bf16_f32 v192, v216, v217\n\t"
    "v_add_f32 v228, v216, v217\n\t"
    "v_fma_f32 v216, v130, s63, v224\n\t"
    "v_mfma_f32_32x32x16_bf16 a[160:175], a[108:111], v[204:207], a[160:175]\n\t"
    "ds_read_b128 a[108:111], v233 offset:36864\n\t"
    "v_fma_f32 v217, v131, s63, v224\n\t"
    "v_exp_f32 v216, v216\n\t"
    "v_mfma_f32_32x32x16_bf16 v[64:79], a[112:115], v[200:203], v[64:79]\n\t"
    "v_exp_f32 v217, v217\n\t"
    "v_cvt_pk_bf16_f32 v196, v218, v219\n\t"
    "v_add_f32 v229, v218, v219\n\t"
    "v_fma_f32 v218, v146, s63, v225\n\t"
    "v_mfma_f32_32x32x16_bf16 a[176:191], a[112:115], v[204:207], a[176:191]\n\t"
    "ds_read_b128 a[112:115], v230 offset:37888\n\t"
    "v_fma_f32 v219, v147, s63, v225\n\t"
    "v_exp_f32 v218, v218\n\t"
    "v_mfma_f32_32x32x16_bf16 v[80:95], a[116:119], v[200:203], v[80:95]\n\t"
    "s_add_u32 m0, s61, 6016\n\t"
    "s_nop 0\n\t"
    "buffer_load_dwordx4 v248, s[24:27], s86 offen offset:128 nt lds\n\t"
    "v_exp_f32 v219, v219\n\t"
    "v_mfma_f32_32x32x16_bf16 a[192:207], a[116:119], v[204:207], a[192:207]\n\t"
    "ds_read_b128 a[116:119], v231 offset:37888\n\t"
    "v_cvt_pk_bf16_f32 v193, v216, v217\n\t"
    "v_add_f32 v228, v228, v216\n\t"
    "v_add_f32 v228, v228, v217\n\t"
    "s_waitcnt lgkmcnt(10)\n\t"
    "v_mfma_f32_32x32x16_bf16 v[0:15], a[120:123], v[208:211], v[0:15]\n\t"
    "v_fma_f32 v216, v132, s63, v224\n\t"
    "v_fma_f32 v217, v133, s63, v224\n\t"
    "v_exp_f32 v216, v216\n\t"
    "v_mfma_f32_32x32x16_bf16 v[96:111], a[120:123], v[212:215], v[96:111]\n\t"
    "ds_read_b128 a[120:123], v232 offset:37888\n\t"
    "v_exp_f32 v217, v217\n\t"
    "v_cvt_pk_bf16_f32 v197, v218, v219\n\t"
    "v_add_f32 v229, v229, v218\n\t"
    "v_mfma_f32_32x32x16_bf16 v[16:31], a[124:127], v[208:211], v[16:31]\n\t"
    "v_add_f32 v229, v229, v219\n\t"
    "v_fma_f32 v218, v148, s63, v225\n\t"
    "v_fma_f32 v219, v149, s63, v225\n\t"
    "v_exp_f32 v218, v218\n\t"
    "v_mfma_f32_32x32x16_bf16 v[112:127], a[124:127], v[212:215], v[112:127]\n\t"
    "ds_read_b128 a[124:127], v233 offset:37888\n\t"
    "v_exp_f32 v219, v219\n\t"
    "v_cvt_pk_bf16_f32 v194, v216, v217\n\t"
    "v_mfma_f32_32x32x16_bf16 v[32:47], a[128:131], v[208:211], v[32:47]\n\t"
    "s_add_u32 m0, s61, 7040\n\t"
    "s_nop 0\n\t"
    "buffer_load_dwordx4 v249, s[24:27], s86 offen offset:128 nt lds\n\t"
    "v_add_f32 v228, v228, v216\n\t"
    "v_mfma_f32_32x32x16_bf16 a[144:159], a[128:131], v[212:215], a[144:159]\n\t"
    "ds_read_b128 a[128:131], v230 offset:38912\n\t"
    "v_add_f32 v228, v228, v217\n\t"
    "v_fma_f32 v216, v134, s63, v224\n\t"
    "v_fma_f32 v217, v135, s63, v224\n\t"
    "v_mfma_f32_32x32x16_bf16 v[48:63], a[132:135], v[208:211], v[48:63]\n\t"
    "ds_read_b128 v[220:223], v241 offset:8192\n\t"
    "v_exp_f32 v216, v216\n\t"
    "v_exp_f32 v217, v217\n\t"
    "v_mfma_f32_32x32x16_bf16 a[160:175], a[132:135], v[212:215], a[160:175]\n\t"
    "ds_read_b128 a[132:135], v231 offset:38912\n\t"
    "v_cvt_pk_bf16_f32 v198, v218, v219\n\t"
    "v_add_f32 v229, v229, v218\n\t"
    "v_add_f32 v229, v229, v219\n\t"
    "s_waitcnt lgkmcnt(11)\n\t"
    "v_mfma_f32_32x32x16_bf16 v[64:79], a[136:139], v[208:211], v[64:79]\n\t"
    "v_fma_f32 v218, v150, s63, v225\n\t"
    "v_fma_f32 v219, v151, s63, v225\n\t"
    "v_exp_f32 v218, v218\n\t"
    "v_mfma_f32_32x32x16_bf16 a[176:191], a[136:139], v[212:215], a[176:191]\n\t"
    "ds_read_b128 a[136:139], v232 offset:38912\n\t"
    "v_exp_f32 v219, v219\n\t"
    "v_cvt_pk_bf16_f32 v195, v216, v217\n\t"
    "v_add_f32 v228, v228, v216\n\t"
    "v_mfma_f32_32x32x16_bf16 v[80:95], a[140:143], v[208:211], v[80:95]\n\t"
    "s_waitcnt lgkmcnt(2)\n\t"
    "buffer_store_dwordx4 v[220:223], v248, s[28:31], s87 offen offset:256 nt\n\t"
    "v_add_f32 v228, v228, v217\n\t"
    "s_nop 0\n\t"
    "v_cvt_pk_bf16_f32 v199, v218, v219\n\t"
    "v_mfma_f32_32x32x16_bf16 a[192:207], a[140:143], v[212:215], a[192:207]\n\t"
    "ds_read_b128 a[140:143], v233 offset:38912\n\t"
    "v_add_f32 v229, v229, v218\n\t"
    "v_add_f32 v229, v229, v219\n\t"
    "v_mfma_f32_32x32x16_bf16 v[160:175], a[96:99], a[0:3], 0\n\t"
    "v_fma_f32 v216, v136, s63, v224\n\t"
    "v_fma_f32 v217, v137, s63, v224\n\t"
    "v_exp_f32 v216, v216\n\t"
    "v_mfma_f32_32x32x16_bf16 v[176:191], a[96:99], a[48:51], 0\n\t"
    "v_exp_f32 v217, v217\n\t"
    "v_fma_f32 v218, v152, s63, v225\n\t"
    "v_fma_f32 v219, v153, s63, v225\n\t"
    "v_exp_f32 v218, v218\n\t"
    "v_mfma_f32_32x32x16_bf16 v[160:175], a[100:103], a[4:7], v[160:175]\n\t"
    "v_exp_f32 v219, v219\n\t"
    "v_cvt_pk_bf16_f32 v208, v216, v217\n\t"
    "v_add_f32 v228, v228, v216\n\t"
    "v_mfma_f32_32x32x16_bf16 v[176:191], a[100:103], a[52:55], v[176:191]\n\t"
    "v_add_f32 v228, v228, v217\n\t"
    "s_waitcnt vmcnt(23) lgkmcnt(0)\n\t"
    "s_barrier\n\t"
    "v_mfma_f32_32x32x16_bf16 v[160:175], a[104:107], a[8:11], v[160:175]\n\t"
    "ds_read_b64_tr_b16 a[96:97], v234 offset:24576\n\t"
    "ds_read_b64_tr_b16 a[98:99], v234 offset:27648\n\t"
    "s_add_u32 m0, s49, 12288\n\t"
    "s_nop 0\n\t"
    "buffer_load_dwordx4 v238, s[20:23], s34 offen lds\n\t"
    "v_mfma_f32_32x32x16_bf16 v[176:191], a[104:107], a[56:59], v[176:191]\n\t"
    "ds_read_b64_tr_b16 a[100:101], v234 offset:25088\n\t"
    "ds_read_b64_tr_b16 a[102:103], v234 offset:28160\n\t"
    "v_fma_f32 v216, v138, s63, v224\n\t"
    "v_fma_f32 v217, v139, s63, v224\n\t"
    "v_mfma_f32_32x32x16_bf16 v[160:175], a[108:111], a[12:15], v[160:175]\n\t"
    "ds_read_b64_tr_b16 a[104:105], v234 offset:25600\n\t"
    "ds_read_b64_tr_b16 a[106:107], v234 offset:28672\n\t"
    "v_exp_f32 v216, v216\n\t"
    "v_exp_f32 v217, v217\n\t"
    "v_mfma_f32_32x32x16_bf16 v[176:191], a[108:111], a[60:63], v[176:191]\n\t"
    "ds_read_b64_tr_b16 a[108:109], v234 offset:26112\n\t"
    "ds_read_b64_tr_b16 a[110:111], v234 offset:29184\n\t"
    "v_cvt_pk_bf16_f32 v212, v218, v219\n\t"
    "v_add_f32 v229, v229, v218\n\t"
    "v_mfma_f32_32x32x16_bf16 v[160:175], a[112:115], a[16:19], v[160:175]\n\t"
    "buffer_load_dwordx4 v239, s[20:23], s34 offen offset:1024 lds\n\t"
    "v_add_f32 v229, v229, v219\n\t"
    "v_fma_f32 v218, v154, s63, v225\n\t"
    "v_fma_f32 v219, v155, s63, v225\n\t"
    "v_mfma_f32_32x32x16_bf16 v[176:191], a[112:115], a[64:67], v[176:191]\n\t"
    "ds_read_b64_tr_b16 a[112:113], v234 offset:26624\n\t"
    "ds_read_b64_tr_b16 a[114:115], v234 offset:29696\n\t"
    "v_exp_f32 v218, v218\n\t"
    "v_exp_f32 v219, v219\n\t"
    "v_mfma_f32_32x32x16_bf16 v[160:175], a[116:119], a[20:23], v[160:175]\n\t"
    "v_cvt_pk_bf16_f32 v209, v216, v217\n\t"
    "v_add_f32 v228, v228, v216\n\t"
    "v_add_f32 v228, v228, v217\n\t"
    "v_fma_f32 v216, v140, s63, v224\n\t"
    "v_mfma_f32_32x32x16_bf16 v[176:191], a[116:119], a[68:71], v[176:191]\n\t"
    "ds_read_b64_tr_b16 a[116:117], v234 offset:27136\n\t"
    "ds_read_b64_tr_b16 a[118:119], v234 offset:30208\n\t"
    "v_fma_f32 v217, v141, s63, v224\n\t"
    "v_exp_f32 v216, v216\n\t"
    "v_mfma_f32_32x32x16_bf16 v[160:175], a[120:123], a[24:27], v[160:175]\n\t"
    "buffer_load_dwordx4 v240, s[20:23], s34 offen offset:2048 lds\n\t"
    "s_add_u32 s34, s34, 12288\n\t"
    "v_exp_f32 v217, v217\n\t"
    "v_cvt_pk_bf16_f32 v213, v218, v219\n\t"
    "v_mfma_f32_32x32x16_bf16 v[176:191], a[120:123], a[72:75], v[176:191]\n\t"
    "ds_read_b64_tr_b16 a[120:121], v234 offset:30720\n\t"
    "ds_read_b64_tr_b16 a[122:123], v234 offset:33792\n\t"
    "v_add_f32 v229, v229, v218\n\t"
    "v_add_f32 v229, v229, v219\n\t"
    "v_mfma_f32_32x32x16_bf16 v[160:175], a[124:127], a[28:31], v[160:175]\n\t"
    "v_fma_f32 v218, v156, s63, v225\n\t"
    "v_fma_f32 v219, v157, s63, v225\n\t"
    "v_exp_f32 v218, v218\n\t"
    "v_exp_f32 v219, v219\n\t"
    "v_mfma_f32_32x32x16_bf16 v[176:191], a[124:127], a[76:79], v[176:191]\n\t"
    "ds_read_b64_tr_b16 a[124:125], v234 offset:31232\n\t"
    "ds_read_b64_tr_b16 a[126:127], v234 offset:34304\n\t"
    "v_cvt_pk_bf16_f32 v210, v216, v217\n\t"
    "v_add_f32 v228, v228, v216\n\t"
    "v_mfma_f32_32x32x16_bf16 v[160:175], a[128:131], a[32:35], v[160:175]\n\t"
    "s_add_u32 m0, s46, 36864\n\t"
    "s_nop 0\n\t"
    "buffer_load_dwordx4 v235, s[16:19], s32 offen lds\n\t"
    "v_add_f32 v228, v228, v217\n\t"
    "v_fma_f32 v216, v142, s63, v224\n\t"
    "v_mfma_f32_32x32x16_bf16 v[176:191], a[128:131], a[80:83], v[176:191]\n\t"
    "ds_read_b64_tr_b16 a[128:129], v234 offset:31744\n\t"
    "ds_read_b64_tr_b16 a[130:131], v234 offset:34816\n\t"
    "v_fma_f32 v217, v143, s63, v224\n\t"
    "v_exp_f32 v216, v216\n\t"
    "v_mfma_f32_32x32x16_bf16 v[160:175], a[132:135], a[36:39], v[160:175]\n\t"
    "v_exp_f32 v217, v217\n\t"
    "v_cvt_pk_bf16_f32 v214, v218, v219\n\t"
    "v_add_f32 v229, v229, v218\n\t"
    "v_mfma_f32_32x32x16_bf16 v[176:191], a[132:135], a[84:87], v[176:191]\n\t"
    "ds_read_b64_tr_b16 a[132:133], v234 offset:32256\n\t"
    "ds_read_b64_tr_b16 a[134:135], v234 offset:35328\n\t"
    "v_add_f32 v229, v229, v219\n\t"
    "v_fma_f32 v218, v158, s63, v225\n\t"
    "v_fma_f32 v219, v159, s63, v225\n\t"
    "v_mfma_f32_32x32x16_bf16 v[160:175], a[136:139], a[40:43], v[160:175]\n\t"
    "buffer_load_dwordx4 v236, s[16:19], s32 offen offset:1024 lds\n\t"
    "v_exp_f32 v218, v218\n\t"
    "v_exp_f32 v219, v219\n\t"
    "v_mfma_f32_32x32x16_bf16 v[176:191], a[136:139], a[88:91], v[176:191]\n\t"
    "ds_read_b64_tr_b16 a[136:137], v234 offset:32768\n\t"
    "ds_read_b64_tr_b16 a[138:139], v234 offset:35840\n\t"
    "v_cvt_pk_bf16_f32 v211, v216, v217\n\t"
    "v_add_f32 v228, v228, v216\n\t"
    "v_mfma_f32_32x32x16_bf16 v[160:175], a[140:143], a[44:47], v[160:175]\n\t"
    "v_add_f32 v228, v228, v217\n\t"
    "s_nop 0\n\t"
    "v_cvt_pk_bf16_f32 v215, v218, v219\n\t"
    "v_add_f32 v229, v229, v218\n\t"
    "v_add_f32 v229, v229, v219\n\t"
    "v_mfma_f32_32x32x16_bf16 v[176:191], a[140:143], a[92:95], v[176:191]\n\t"
    "ds_read_b64_tr_b16 a[140:141], v234 offset:33280\n\t"
    "ds_read_b64_tr_b16 a[142:143], v234 offset:36352\n\t"
    "v_add_f32 v251, v228, v229\n\t"
    "v_cmp_nge_f32 vcc, s64, v251\n\t"
    "s_cbranch_vccnz .Ld_stub9\n\t"
    ".Ld_back9:\n\t"
    "s_waitcnt lgkmcnt(12)\n\t"
    "v_mfma_f32_32x32x16_bf16 v[0:15], a[96:99], v[192:195], v[0:15]\n\t"
    "buffer_load_dwordx4 v237, s[16:19], s32 offen offset:2048 lds\n\t"
    "ds_read_b128 v[252:255], v241 offset:9216\n\t"
    "v_add_f32 v226, v226, v228\n\t"
    "v_add_f32 v227, v227, v229\n\t"
    "v_mfma_f32_32x32x16_bf16 v[96:111], a[96:99], v[196:199], v[96:111]\n\t"
    "ds_read_b128 a[96:99], v230 offset:0\n\t"
    "v_mfma_f32_32x32x16_bf16 v[16:31], a[100:103], v[192:195], v[16:31]\n\t"
    "v_mfma_f32_32x32x16_bf16 v[112:127], a[100:103], v[196:199], v[112:127]\n\t"
    "ds_read_b128 a[100:103], v231 offset:0\n\t"
    "v_fma_f32 v216, v160, s63, v224\n\t"
    "v_fma_f32 v217, v161, s63, v224\n\t"
    "v_exp_f32 v216, v216\n\t"
    "v_mfma_f32_32x32x16_bf16 v[32:47], a[104:107], v[192:195], v[32:47]\n\t"
    "s_waitcnt lgkmcnt(2)\n\t"
    "buffer_store_dwordx4 v[252:255], v249, s[28:31], s87 offen offset:256 nt\n\t"
    "v_exp_f32 v217, v217\n\t"
    "v_mfma_f32_32x32x16_bf16 a[144:159], a[104:107], v[196:199], a[144:159]\n\t"
    "ds_read_b128 a[104:107], v232 offset:0\n\t"
    "v_fma_f32 v218, v176, s63, v225\n\t"
    "v_fma_f32 v219, v177, s63, v225\n\t"
    "v_exp_f32 v218, v218\n\t"
    "v_mfma_f32_32x32x16_bf16 v[48:63], a[108:111], v[192:195], v[48:63]\n\t"
    "v_exp_f32 v219, v219\n\t"
    "v_cvt_pk_bf16_f32 v200, v216, v217\n\t"
    "v_add_f32 v228, v216, v217\n\t"
    "v_mfma_f32_32x32x16_bf16 a[160:175], a[108:111], v[196:199], a[160:175]\n\t"
    "ds_read_b128 a[108:111], v233 offset:0\n\t"
    "v_fma_f32 v216, v162, s63, v224\n\t"
    "v_fma_f32 v217, v163, s63, v224\n\t"
    "v_mfma_f32_32x32x16_bf16 v[64:79], a[112:115], v[192:195], v[64:79]\n\t"
    "v_exp_f32 v216, v216\n\t"
    "v_exp_f32 v217, v217\n\t"
    "v_cvt_pk_bf16_f32 v204, v218, v219\n\t"
    "v_mfma_f32_32x32x16_bf16 a[176:191], a[112:115], v[196:199], a[176:191]\n\t"
    "ds_read_b128 a[112:115], v230 offset:1024\n\t"
    "ds_read_b128 v[220:223], v241 offset:10240\n\t"
    "v_add_f32 v229, v218, v219\n\t"
    "v_fma_f32 v218, v178, s63, v225\n\t"
    "v_mfma_f32_32x32x16_bf16 v[80:95], a[116:119], v[192:195], v[80:95]\n\t"
    "v_fma_f32 v219, v179, s63, v225\n\t"
    "v_exp_f32 v218, v218\n\t"
    "v_exp_f32 v219, v219\n\t"
    "v_mfma_f32_32x32x16_bf16 a[192:207], a[116:119], v[196:199], a[192:207]\n\t"
    "ds_read_b128 a[116:119], v231 offset:1024\n\t"
    "v_cvt_pk_bf16_f32 v201, v216, v217\n\t"
    "v_add_f32 v228, v228, v216\n\t"
    "v_add_f32 v228, v228, v217\n\t"
    "v_mfma_f32_32x32x16_bf16 v[0:15], a[120:123], v[208:211], v[0:15]\n\t"
    "v_fma_f32 v216, v164, s63, v224\n\t"
    "v_fma_f32 v217, v165, s63, v224\n\t"
    "v_exp_f32 v216, v216\n\t"
    "v_mfma_f32_32x32x16_bf16 v[96:111], a[120:123], v[212:215], v[96:111]\n\t"
    "s_waitcnt lgkmcnt(1)\n\t"
    "buffer_store_dwordx4 v[220:223], v248, s[28:31], s43 offen offset:256 nt\n\t"
    "ds_read_b128 a[120:123], v232 offset:1024\n\t"
    "v_exp_f32 v217, v217\n\t"
    "v_mfma_f32_32x32x16_bf16 v[16:31], a[124:127], v[208:211], v[16:31]\n\t"
    "v_cvt_pk_bf16_f32 v205, v218, v219\n\t"
    "v_add_f32 v229, v229, v218\n\t"
    "v_add_f32 v229, v229, v219\n\t"
    "v_fma_f32 v218, v180, s63, v225\n\t"
    "v_mfma_f32_32x32x16_bf16 v[112:127], a[124:127], v[212:215], v[112:127]\n\t"
    "ds_read_b128 a[124:127], v233 offset:1024\n\t"
    "ds_read_b128 v[252:255], v241 offset:11264\n\t"
    "v_fma_f32 v219, v181, s63, v225\n\t"
    "v_exp_f32 v218, v218\n\t"
    "v_mfma_f32_32x32x16_bf16 v[32:47], a[128:131], v[208:211], v[32:47]\n\t"
    "v_exp_f32 v219, v219\n\t"
    "v_cvt_pk_bf16_f32 v202, v216, v217\n\t"
    "v_add_f32 v228, v228, v216\n\t"
    "v_mfma_f32_32x32x16_bf16 a[144:159], a[128:131], v[212:215], a[144:159]\n\t"
    "ds_read_b128 a[128:131], v230 offset:2048\n\t"
    "v_add_f32 v228, v228, v217\n\t"
    "v_fma_f32 v216, v166, s63, v224\n\t"
    "v_fma_f32 v217, v167, s63, v224\n\t"
    "v_mfma_f32_32x32x16_bf16 v[48:63], a[132:135], v[208:211], v[48:63]\n\t"
    "v_exp_f32 v216, v216\n\t"
    "v_exp_f32 v217, v217\n\t"
    "v_mfma_f32_32x32x16_bf16 a[160:175], a[132:135], v[212:215], a[160:175]\n\t"
    "s_waitcnt lgkmcnt(1)\n\t"
    "buffer_store_dwordx4 v[252:255], v249, s[28:31], s43 offen offset:256 nt\n\t"
    "ds_read_b128 a[132:135], v231 offset:2048\n\t"
    "v_cvt_pk_bf16_f32 v206, v218, v219\n\t"
    "v_add_f32 v229, v229, v218\n\t"
    "v_mfma_f32_32x32x16_bf16 v[64:79], a[136:139], v[208:211], v[64:79]\n\t"
    "v_add_f32 v229, v229, v219\n\t"
    "v_fma_f32 v218, v182, s63, v225\n\t"
    "v_fma_f32 v219, v183, s63, v225\n\t"
    "v_exp_f32 v218, v218\n\t"
    "v_mfma_f32_32x32x16_bf16 a[176:191], a[136:139], v[212:215], a[176:191]\n\t"
    "ds_read_b128 a[136:139], v232 offset:2048\n\t"
    "v_exp_f32 v219, v219\n\t"
    "v_cvt_pk_bf16_f32 v203, v216, v217\n\t"
    "v_mfma_f32_32x32x16_bf16 v[80:95], a[140:143], v[208:211], v[80:95]\n\t"
    "v_add_f32 v228, v228, v216\n\t"
    "v_add_f32 v228, v228, v217\n\t"
    "s_nop 0\n\t"
    "v_cvt_pk_bf16_f32 v207, v218, v219\n\t"
    "v_mfma_f32_32x32x16_bf16 a[192:207], a[140:143], v[212:215], a[192:207]\n\t"
    "ds_read_b128 a[140:143], v233 offset:2048\n\t"
    "v_add_f32 v229, v229, v218\n\t"
    "v_add_f32 v229, v229, v219\n\t"
    "v_mfma_f32_32x32x16_bf16 v[128:143], a[96:99], a[0:3], 0\n\t"
    "v_fma_f32 v216, v168, s63, v224\n\t"
    "v_fma_f32 v217, v169, s63, v224\n\t"
    "v_exp_f32 v216, v216\n\t"
    "v_mfma_f32_32x32x16_bf16 v[144:159], a[96:99], a[48:51], 0\n\t"
    "s_add_u32 m0, s61, 7936\n\t"
    "s_nop 0\n\t"
    "buffer_load_dwordx4 v248, s[24:27], s87 offen offset:256 nt lds\n\t"
    "v_exp_f32 v217, v217\n\t"
    "v_fma_f32 v218, v184, s63, v225\n\t"
    "v_mfma_f32_32x32x16_bf16 v[128:143], a[100:103], a[4:7], v[128:143]\n\t"
    "v_fma_f32 v219, v185, s63, v225\n\t"
    "v_exp_f32 v218, v218\n\t"
    "v_exp_f32 v219, v219\n\t"
    "v_mfma_f32_32x32x16_bf16 v[144:159], a[100:103], a[52:55], v[144:159]\n\t"
    "v_cvt_pk_bf16_f32 v208, v216, v217\n\t"
    "s_waitcnt vmcnt(23) lgkmcnt(0)\n\t"
    "s_barrier\n\t"
    "v_mfma_f32_32x32x16_bf16 v[128:143], a[104:107], a[8:11], v[128:143]\n\t"
    "ds_read_b64_tr_b16 a[96:97], v234 offset:36864\n\t"
    "ds_read_b64_tr_b16 a[98:99], v234 offset:39936\n\t"
    "s_add_u32 m0, s49, 24576\n\t"
    "s_nop 0\n\t"
    "buffer_load_dwordx4 v238, s[20:23], s34 offen lds\n\t"
    "v_mfma_f32_32x32x16_bf16 v[144:159], a[104:107], a[56:59], v[144:159]\n\t"
    "ds_read_b64_tr_b16 a[100:101], v234 offset:37376\n\t"
    "ds_read_b64_tr_b16 a[102:103], v234 offset:40448\n\t"
    "v_add_f32 v228, v228, v216\n\t"
    "v_add_f32 v228, v228, v217\n\t"
    "v_mfma_f32_32x32x16_bf16 v[128:143], a[108:111], a[12:15], v[128:143]\n\t"
    "ds_read_b64_tr_b16 a[104:105], v234 offset:37888\n\t"
    "ds_read_b64_tr_b16 a[106:107], v234 offset:40960\n\t"
    "v_fma_f32 v216, v170, s63, v224\n\t"
    "v_fma_f32 v217, v171, s63, v224\n\t"
    "v_exp_f32 v216, v216\n\t"
    "v_mfma_f32_32x32x16_bf16 v[144:159], a[108:111], a[60:63], v[144:159]\n\t"
    "ds_read_b64_tr_b16 a[108:109], v234 offset:38400\n\t"
    "ds_read_b64_tr_b16 a[110:111], v234 offset:41472\n\t"
    "v_exp_f32 v217, v217\n\t"
    "v_mfma_f32_32x32x16_bf16 v[128:143], a[112:115], a[16:19], v[128:143]\n\t"
    "buffer_load_dwordx4 v239, s[20:23], s34 offen offset:1024 lds\n\t"
    "v_cvt_pk_bf16_f32 v212, v218, v219\n\t"
    "v_add_f32 v229, v229, v218\n\t"
    "v_add_f32 v229, v229, v219\n\t"
    "v_fma_f32 v218, v186, s63, v225\n\t"
    "v_mfma_f32_32x32x16_bf16 v[144:159], a[112:115], a[64:67], v[144:159]\n\t"
    "ds_read_b64_tr_b16 a[112:113], v234 offset:38912\n\t"
    "ds_read_b64_tr_b16 a[114:115], v234 offset:41984\n\t"
    "v_fma_f32 v219, v187, s63, v225\n\t"
    "v_exp_f32 v218, v218\n\t"
    "v_mfma_f32_32x32x16_bf16 v[128:143], a[116:119], a[20:23], v[128:143]\n\t"
    "v_exp_f32 v219, v219\n\t"
    "v_cvt_pk_bf16_f32 v209, v216, v217\n\t"
    "v_add_f32 v228, v228, v216\n\t"
    "v_add_f32 v228, v228, v217\n\t"
    "v_mfma_f32_32x32x16_bf16 v[144:159], a[116:119], a[68:71], v[144:159]\n\t"
    "ds_read_b64_tr_b16 a[116:117], v234 offset:39424\n\t"
    "ds_read_b64_tr_b16 a[118:119], v234 offset:42496\n\t"
    "v_fma_f32 v216, v172, s63, v224\n\t"
    "v_fma_f32 v217, v173, s63, v224\n\t"
    "v_mfma_f32_32x32x16_bf16 v[128:143], a[120:123], a[24:27], v[128:143]\n\t"
    "buffer_load_dwordx4 v240, s[20:23], s34 offen offset:2048 lds\n\t"
    "s_add_u32 s34, s34, 12288\n\t"
    "v_exp_f32 v216, v216\n\t"
    "v_exp_f32 v217, v217\n\t"
    "v_mfma_f32_32x32x16_bf16 v[144:159], a[120:123], a[72:75], v[144:159]\n\t"
    "ds_read_b64_tr_b16 a[120:121], v234 offset:43008\n\t"
    "ds_read_b64_tr_b16 a[122:123], v234 offset:46080\n\t"
    "v_cvt_pk_bf16_f32 v213, v218, v219\n\t"
    "v_add_f32 v229, v229, v218\n\t"
    "v_add_f32 v229, v229, v219\n\t"
    "v_mfma_f32_32x32x16_bf16 v[128:143], a[124:127], a[28:31], v[128:143]\n\t"
    "v_fma_f32 v218, v188, s63, v225\n\t"
    "v_fma_f32 v219, v189, s63, v225\n\t"
    "v_exp_f32 v218, v218\n\t"
    "v_mfma_f32_32x32x16_bf16 v[144:159], a[124:127], a[76:79], v[144:159]\n\t"
    "ds_read_b64_tr_b16 a[124:125], v234 offset:43520\n\t"
    "ds_read_b64_tr_b16 a[126:127], v234 offset:46592\n\t"
    "v_exp_f32 v219, v219\n\t"
    "v_cvt_pk_bf16_f32 v210, v216, v217\n\t"
    "v_mfma_f32_32x32x16_bf16 v[128:143], a[128:131], a[32:35], v[128:143]\n\t"
    "s_mov_b32 m0, s46\n\t"
    "s_mov_b32 s18, s42\n\t"
    "buffer_load_dwordx4 v235, s[16:19], s36 offen lds\n\t"
    "v_add_f32 v228, v228, v216\n\t"
    "v_add_f32 v228, v228, v217\n\t"
    "v_mfma_f32_32x32x16_bf16 v[144:159], a[128:131], a[80:83], v[144:159]\n\t"
    "ds_read_b64_tr_b16 a[128:129], v234 offset:44032\n\t"
    "ds_read_b64_tr_b16 a[130:131], v234 offset:47104\n\t"
    "v_fma_f32 v216, v174, s63, v224\n\t"
    "v_fma_f32 v217, v175, s63, v224\n\t"
    "v_exp_f32 v216, v216\n\t"
    "v_mfma_f32_32x32x16_bf16 v[128:143], a[132:135], a[36:39], v[128:143]\n\t"
    "v_exp_f32 v217, v217\n\t"
    "v_cvt_pk_bf16_f32 v214, v218, v219\n\t"
    "v_add_f32 v229, v229, v218\n\t"
    "v_mfma_f32_32x32x16_bf16 v[144:159], a[132:135], a[84:87], v[144:159]\n\t"
    "ds_read_b64_tr_b16 a[132:133], v234 offset:44544\n\t"
    "ds_read_b64_tr_b16 a[134:135], v234 offset:47616\n\t"
    "v_add_f32 v229, v229, v219\n\t"
    "v_fma_f32 v218, v190, s63, v225\n\t"
    "v_fma_f32 v219, v191, s63, v225\n\t"
    "v_mfma_f32_32x32x16_bf16 v[128:143], a[136:139], a[40:43], v[128:143]\n\t"
    "buffer_load_dwordx4 v236, s[16:19], s36 offen offset:1024 lds\n\t"
    "v_exp_f32 v218, v218\n\t"
    "v_exp_f32 v219, v219\n\t"
    "v_mfma_f32_32x32x16_bf16 v[144:159], a[136:139], a[88:91], v[144:159]\n\t"
    "ds_read_b64_tr_b16 a[136:137], v234 offset:45056\n\t"
    "ds_read_b64_tr_b16 a[138:139], v234 offset:48128\n\t"
    "v_cvt_pk_bf16_f32 v211, v216, v217\n\t"
    "v_add_f32 v228, v228, v216\n\t"
    "v_add_f32 v228, v228, v217\n\t"
    "v_mfma_f32_32x32x16_bf16 v[128:143], a[140:143], a[44:47], v[128:143]\n\t"
    "s_nop 0\n\t"
    "v_cvt_pk_bf16_f32 v215, v218, v219\n\t"
    "v_add_f32 v229, v229, v218\n\t"
    "v_add_f32 v229, v229, v219\n\t"
    "v_add_f32 v251, v228, v229\n\t"
    "v_mfma_f32_32x32x16_bf16 v[144:159], a[140:143], a[92:95], v[144:159]\n\t"
    "ds_read_b64_tr_b16 a[140:141], v234 offset:45568\n\t"
    "ds_read_b64_tr_b16 a[142:143], v234 offset:48640\n\t"
    "v_cmp_nge_f32 vcc, s64, v251\n\t"
    "s_cbranch_vccnz .Ld_stub10\n\t"
    ".Ld_back10:\n\t"
    "s_waitcnt lgkmcnt(12)\n\t"
    "v_mfma_f32_32x32x16_bf16 v[0:15], a[96:99], v[200:203], v[0:15]\n\t"
    "buffer_load_dwordx4 v237, s[16:19], s36 offen offset:2048 lds\n\t"
    "v_add_f32 v226, v226, v228\n\t"
    "v_add_f32 v227, v227, v229\n\t"
    "v_mfma_f32_32x32x16_bf16 v[96:111], a[96:99], v[204:207], v[96:111]\n\t"
    "ds_read_b128 a[96:99], v230 offset:12288\n\t"
    "v_mfma_f32_32x32x16_bf16 v[16:31], a[100:103], v[200:203], v[16:31]\n\t"
    "v_mfma_f32_32x32x16_bf16 v[112:127], a[100:103], v[204:207], v[112:127]\n\t"
    "ds_read_b128 a[100:103], v231 offset:12288\n\t"
    "v_fma_f32 v216, v128, s63, v224\n\t"
    "v_fma_f32 v217, v129, s63, v224\n\t"
    "v_exp_f32 v216, v216\n\t"
    "v_mfma_f32_32x32x16_bf16 v[32:47], a[104:107], v[200:203], v[32:47]\n\t"
    "v_exp_f32 v217, v217\n\t"
    "v_fma_f32 v218, v144, s63, v225\n\t"
    "v_fma_f32 v219, v145, s63, v225\n\t"
    "v_mfma_f32_32x32x16_bf16 a[144:159], a[104:107], v[204:207], a[144:159]\n\t"
    "ds_read_b128 a[104:107], v232 offset:12288\n\t"
    "v_exp_f32 v218, v218\n\t"
    "v_exp_f32 v219, v219\n\t"
    "v_mfma_f32_32x32x16_bf16 v[48:63], a[108:111], v[200:203], v[48:63]\n\t"
    "v_cvt_pk_bf16_f32 v192, v216, v217\n\t"
    "v_add_f32 v228, v216, v217\n\t"
    "v_fma_f32 v216, v130, s63, v224\n\t"
    "v_fma_f32 v217, v131, s63, v224\n\t"
    "v_mfma_f32_32x32x16_bf16 a[160:175], a[108:111], v[204:207], a[160:175]\n\t"
    "ds_read_b128 a[108:111], v233 offset:12288\n\t"
    "s_add_u32 m0, s61, 8960\n\t"
    "s_nop 0\n\t"
    "buffer_load_dwordx4 v249, s[24:27], s87 offen offset:256 nt lds\n\t"
    "v_mfma_f32_32x32x16_bf16 v[64:79], a[112:115], v[200:203], v[64:79]\n\t"
    "v_exp_f32 v216, v216\n\t"
    "v_exp_f32 v217, v217\n\t"
    "v_mfma_f32_32x32x16_bf16 a[176:191], a[112:115], v[204:207], a[176:191]\n\t"
    "ds_read_b128 a[112:115], v230 offset:13312\n\t"
    "v_cvt_pk_bf16_f32 v196, v218, v219\n\t"
    "v_add_f32 v229, v218, v219\n\t"
    "v_fma_f32 v218, v146, s63, v225\n\t"
    "v_fma_f32 v219, v147, s63, v225\n\t"
    "v_mfma_f32_32x32x16_bf16 v[80:95], a[116:119], v[200:203], v[80:95]\n\t"
    "v_exp_f32 v218, v218\n\t"
    "v_exp_f32 v219, v219\n\t"
    "v_mfma_f32_32x32x16_bf16 a[192:207], a[116:119], v[204:207], a[192:207]\n\t"
    "ds_read_b128 a[116:119], v231 offset:13312\n\t"
    "v_cvt_pk_bf16_f32 v193, v216, v217\n\t"
    "v_add_f32 v228, v228, v216\n\t"
    "v_add_f32 v228, v228, v217\n\t"
    "v_fma_f32 v216, v132, s63, v224\n\t"
    "s_waitcnt lgkmcnt(10)\n\t"
    "v_mfma_f32_32x32x16_bf16 v[0:15], a[120:123], v[208:211], v[0:15]\n\t"
    "v_fma_f32 v217, v133, s63, v224\n\t"
    "v_exp_f32 v216, v216\n\t"
    "v_mfma_f32_32x32x16_bf16 v[96:111], a[120:123], v[212:215], v[96:111]\n\t"
    "ds_read_b128 a[120:123], v232 offset:13312\n\t"
    "s_add_u32 m0, s61, 9984\n\t"
    "s_nop 0\n\t"
    "buffer_load_dwordx4 v248, s[24:27], s43 offen offset:256 nt lds\n\t"
    "v_exp_f32 v217, v217\n\t"
    "v_mfma_f32_32x32x16_bf16 v[16:31], a[124:127], v[208:211], v[16:31]\n\t"
    "v_cvt_pk_bf16_f32 v197, v218, v219\n\t"
    "v_add_f32 v229, v229, v218\n\t"
    "v_add_f32 v229, v229, v219\n\t"
    "v_fma_f32 v218, v148, s63, v225\n\t"
    "v_mfma_f32_32x32x16_bf16 v[112:127], a[124:127], v[212:215], v[112:127]\n\t"
    "ds_read_b128 a[124:127], v233 offset:13312\n\t"
    "v_fma_f32 v219, v149, s63, v225\n\t"
    "v_exp_f32 v218, v218\n\t"
    "v_mfma_f32_32x32x16_bf16 v[32:47], a[128:131], v[208:211], v[32:47]\n\t"
    "v_exp_f32 v219, v219\n\t"
    "v_cvt_pk_bf16_f32 v194, v216, v217\n\t"
    "v_add_f32 v228, v228, v216\n\t"
    "v_add_f32 v228, v228, v217\n\t"
    "v_mfma_f32_32x32x16_bf16 a[144:159], a[128:131], v[212:215], a[144:159]\n\t"
    "ds_read_b128 a[128:131], v230 offset:14336\n\t"
    "v_fma_f32 v216, v134, s63, v224\n\t"
    "v_fma_f32 v217, v135, s63, v224\n\t"
    "v_mfma_f32_32x32x16_bf16 v[48:63], a[132:135], v[208:211], v[48:63]\n\t"
    "v_exp_f32 v216, v216\n\t"
    "v_exp_f32 v217, v217\n\t"
    "v_cvt_pk_bf16_f32 v198, v218, v219\n\t"
    "v_mfma_f32_32x32x16_bf16 a[160:175], a[132:135], v[212:215], a[160:175]\n\t"
    "ds_read_b128 a[132:135], v231 offset:14336\n\t"
    "v_add_f32 v229, v229, v218\n\t"
    "v_add_f32 v229, v229, v219\n\t"
    "v_fma_f32 v218, v150, s63, v225\n\t"
    "v_fma_f32 v219, v151, s63, v225\n\t"
    "s_waitcnt lgkmcnt(10)\n\t"
    "v_mfma_f32_32x32x16_bf16 v[64:79], a[136:139], v[208:211], v[64:79]\n\t"
    "v_exp_f32 v218, v218\n\t"
    "v_exp_f32 v219, v219\n\t"
    "v_mfma_f32_32x32x16_bf16 a[176:191], a[136:139], v[212:215], a[176:191]\n\t"
    "ds_read_b128 a[136:139], v232 offset:14336\n\t"
    "v_cvt_pk_bf16_f32 v195, v216, v217\n\t"
    "v_add_f32 v228, v228, v216\n\t"
    "v_add_f32 v228, v228, v217\n\t"
    "s_nop 0\n\t"
    "v_mfma_f32_32x32x16_bf16 v[80:95], a[140:143], v[208:211], v[80:95]\n\t"
    "s_add_u32 m0, s61, 11008\n\t"
    "s_nop 0\n\t"
    "buffer_load_dwordx4 v249, s[24:27], s43 offen offset:256 nt lds\n\t"
    "v_cvt_pk_bf16_f32 v199, v218, v219\n\t"
    "v_mfma_f32_32x32x16_bf16 a[192:207], a[140:143], v[212:215], a[192:207]\n\t"
    "ds_read_b128 a[140:143], v233 offset:14336\n\t"
    "v_add_f32 v229, v229, v218\n\t"
    "v_add_f32 v229, v229, v219\n\t"
    "s_waitcnt lgkmcnt(10)\n\t"
    "v_mfma_f32_32x32x16_bf16 v[160:175], a[96:99], a[0:3], 0\n\t"
    "v_fma_f32 v216, v136, s63, v224\n\t"
    "v_fma_f32 v217, v137, s63, v224\n\t"
    "v_exp_f32 v216, v216\n\t"
    "v_mfma_f32_32x32x16_bf16 v[176:191], a[96:99], a[48:51], 0\n\t"
    "v_exp_f32 v217, v217\n\t"
    "v_fma_f32 v218, v152, s63, v225\n\t"
    "v_fma_f32 v219, v153, s63, v225\n\t"
    "v_exp_f32 v218, v218\n\t"
    "s_waitcnt lgkmcnt(9)\n\t"
    "v_mfma_f32_32x32x16_bf16 v[160:175], a[100:103], a[4:7], v[160:175]\n\t"
    "v_exp_f32 v219, v219\n\t"
    "v_cvt_pk_bf16_f32 v208, v216, v217\n\t"
    "v_add_f32 v228, v228, v216\n\t"
    "v_mfma_f32_32x32x16_bf16 v[176:191], a[100:103], a[52:55], v[176:191]\n\t"
    "v_add_f32 v228, v228, v217\n\t"
    "s_waitcnt vmcnt(22) lgkmcnt(0)\n\t"
    "s_barrier\n\t"
    "v_mfma_f32_32x32x16_bf16 v[160:175], a[104:107], a[8:11], v[160:175]\n\t"
    "ds_read_b64_tr_b16 a[96:97], v234 offset:0\n\t"
    "ds_read_b64_tr_b16 a[98:99], v234 offset:3072\n\t"
    "s_add_u32 m0, s49, 36864\n\t"
    "s_nop 0\n\t"
    "buffer_load_dwordx4 v238, s[20:23], s34 offen lds\n\t"
    "v_mfma_f32_32x32x16_bf16 v[176:191], a[104:107], a[56:59], v[176:191]\n\t"
    "ds_read_b64_tr_b16 a[100:101], v234 offset:512\n\t"
    "ds_read_b64_tr_b16 a[102:103], v234 offset:3584\n\t"
    "v_fma_f32 v216, v138, s63, v224\n\t"
    "v_fma_f32 v217, v139, s63, v224\n\t"
    "v_mfma_f32_32x32x16_bf16 v[160:175], a[108:111], a[12:15], v[160:175]\n\t"
    "ds_read_b64_tr_b16 a[104:105], v234 offset:1024\n\t"
    "ds_read_b64_tr_b16 a[106:107], v234 offset:4096\n\t"
    "v_exp_f32 v216, v216\n\t"
    "v_mfma_f32_32x32x16_bf16 v[176:191], a[108:111], a[60:63], v[176:191]\n\t"
    "ds_read_b64_tr_b16 a[108:109], v234 offset:1536\n\t"
    "ds_read_b64_tr_b16 a[110:111], v234 offset:4608\n\t"
    "v_exp_f32 v217, v217\n\t"
    "v_cvt_pk_bf16_f32 v212, v218, v219\n\t"
    "v_add_f32 v229, v229, v218\n\t"
    "v_mfma_f32_32x32x16_bf16 v[160:175], a[112:115], a[16:19], v[160:175]\n\t"
    "buffer_load_dwordx4 v239, s[20:23], s34 offen offset:1024 lds\n\t"
    "v_add_f32 v229, v229, v219\n\t"
    "v_fma_f32 v218, v154, s63, v225\n\t"
    "v_fma_f32 v219, v155, s63, v225\n\t"
    "v_mfma_f32_32x32x16_bf16 v[176:191], a[112:115], a[64:67], v[176:191]\n\t"
    "ds_read_b64_tr_b16 a[112:113], v234 offset:2048\n\t"
    "ds_read_b64_tr_b16 a[114:115], v234 offset:5120\n\t"
    "v_exp_f32 v218, v218\n\t"
    "v_mfma_f32_32x32x16_bf16 v[160:175], a[116:119], a[20:23], v[160:175]\n\t"
    "v_exp_f32 v219, v219\n\t"
    "v_cvt_pk_bf16_f32 v209, v216, v217\n\t"
    "v_add_f32 v228, v228, v216\n\t"
    "v_add_f32 v228, v228, v217\n\t"
    "v_fma_f32 v216, v140, s63, v224\n\t"
    "v_mfma_f32_32x32x16_bf16 v[176:191], a[116:119], a[68:71], v[176:191]\n\t"
    "ds_read_b64_tr_b16 a[116:117], v234 offset:2560\n\t"
    "ds_read_b64_tr_b16 a[118:119], v234 offset:5632\n\t"
    "v_fma_f32 v217, v141, s63, v224\n\t"
    "v_exp_f32 v216, v216\n\t"
    "v_mfma_f32_32x32x16_bf16 v[160:175], a[120:123], a[24:27], v[160:175]\n\t"
    "buffer_load_dwordx4 v240, s[20:23], s34 offen offset:2048 lds\n\t"
    "v_exp_f32 v217, v217\n\t"
    "v_cvt_pk_bf16_f32 v213, v218, v219\n\t"
    "v_mfma_f32_32x32x16_bf16 v[176:191], a[120:123], a[72:75], v[176:191]\n\t"
    "ds_read_b64_tr_b16 a[120:121], v234 offset:6144\n\t"
    "ds_read_b64_tr_b16 a[122:123], v234 offset:9216\n\t"
    "v_add_f32 v229, v229, v218\n\t"
    "v_add_f32 v229, v229, v219\n\t"
    "v_fma_f32 v218, v156, s63, v225\n\t"
    "v_mfma_f32_32x32x16_bf16 v[160:175], a[124:127], a[28:31], v[160:175]\n\t"
    "v_fma_f32 v219, v157, s63, v225\n\t"
    "v_exp_f32 v218, v218\n\t"
    "v_exp_f32 v219, v219\n\t"
    "v_mfma_f32_32x32x16_bf16 v[176:191], a[124:127], a[76:79], v[176:191]\n\t"
    "ds_read_b64_tr_b16 a[124:125], v234 offset:6656\n\t"
    "ds_read_b64_tr_b16 a[126:127], v234 offset:9728\n\t"
    "v_cvt_pk_bf16_f32 v210, v216, v217\n\t"
    "v_add_f32 v228, v228, v216\n\t"
    "v_mfma_f32_32x32x16_bf16 v[160:175], a[128:131], a[32:35], v[160:175]\n\t"
    "s_add_u32 m0, s46, 12288\n\t"
    "s_nop 0\n\t"
    "buffer_load_dwordx4 v235, s[16:19], s37 offen lds\n\t"
    "v_add_f32 v228, v228, v217\n\t"
    "v_fma_f32 v216, v142, s63, v224\n\t"
    "v_mfma_f32_32x32x16_bf16 v[176:191], a[128:131], a[80:83], v[176:191]\n\t"
    "ds_read_b64_tr_b16 a[128:129], v234 offset:7168\n\t"
    "ds_read_b64_tr_b16 a[130:131], v234 offset:10240\n\t"
    "v_fma_f32 v217, v143, s63, v224\n\t"
    "v_exp_f32 v216, v216\n\t"
    "v_mfma_f32_32x32x16_bf16 v[160:175], a[132:135], a[36:39], v[160:175]\n\t"
    "v_exp_f32 v217, v217\n\t"
    "v_cvt_pk_bf16_f32 v214, v218, v219\n\t"
    "v_add_f32 v229, v229, v218\n\t"
    "v_mfma_f32_32x32x16_bf16 v[176:191], a[132:135], a[84:87], v[176:191]\n\t"
    "ds_read_b64_tr_b16 a[132:133], v234 offset:7680\n\t"
    "ds_read_b64_tr_b16 a[134:135], v234 offset:10752\n\t"
    "v_add_f32 v229, v229, v219\n\t"
    "v_fma_f32 v218, v158, s63, v225\n\t"
    "v_fma_f32 v219, v159, s63, v225\n\t"
    "v_mfma_f32_32x32x16_bf16 v[160:175], a[136:139], a[40:43], v[160:175]\n\t"
    "buffer_load_dwordx4 v236, s[16:19], s37 offen offset:1024 lds\n\t"
    "v_exp_f32 v218, v218\n\t"
    "v_exp_f32 v219, v219\n\t"
    "v_mfma_f32_32x32x16_bf16 v[176:191], a[136:139], a[88:91], v[176:191]\n\t"
    "ds_read_b64_tr_b16 a[136:137], v234 offset:8192\n\t"
    "ds_read_b64_tr_b16 a[138:139], v234 offset:11264\n\t"
    "v_cvt_pk_bf16_f32 v211, v216, v217\n\t"
    "v_add_f32 v228, v228, v216\n\t"
    "v_mfma_f32_32x32x16_bf16 v[160:175], a[140:143], a[44:47], v[160:175]\n\t"
    "v_add_f32 v228, v228, v217\n\t"
    "s_nop 0\n\t"
    "v_cvt_pk_bf16_f32 v215, v218, v219\n\t"
    "v_add_f32 v229, v229, v218\n\t"
    "v_add_f32 v229, v229, v219\n\t"
    "v_mfma_f32_32x32x16_bf16 v[176:191], a[140:143], a[92:95], v[176:191]\n\t"
    "ds_read_b64_tr_b16 a[140:141], v234 offset:8704\n\t"
    "ds_read_b64_tr_b16 a[142:143], v234 offset:11776\n\t"
    "v_add_f32 v251, v228, v229\n\t"
    "v_cmp_nge_f32 vcc, s64, v251\n\t"
    "s_cbranch_vccnz .Ld_stub11\n\t"
    ".Ld_back11:\n\t"
    "s_waitcnt lgkmcnt(12)\n\t"
    "v_mfma_f32_32x32x16_bf16 v[0:15], a[96:99], v[192:195], v[0:15]\n\t"
    "buffer_load_dwordx4 v237, s[16:19], s37 offen offset:2048 lds\n\t"
    "v_add_f32 v226, v226, v228\n\t"
    "v_add_f32 v227, v227, v229\n\t"
    "v_mfma_f32_32x32x16_bf16 v[96:111], a[96:99], v[196:199], v[96:111]\n\t"
    "ds_read_b128 a[96:99], v230 offset:24576\n\t"
    "v_mfma_f32_32x32x16_bf16 v[16:31], a[100:103], v[192:195], v[16:31]\n\t"
    "v_mfma_f32_32x32x16_bf16 v[112:127], a[100:103], v[196:199], v[112:127]\n\t"
    "ds_read_b128 a[100:103], v231 offset:24576\n\t"
    "v_fma_f32 v216, v160, s63, v224\n\t"
    "v_fma_f32 v217, v161, s63, v224\n\t"
    "v_mfma_f32_32x32x16_bf16 v[32:47], a[104:107], v[192:195], v[32:47]\n\t"
    "v_exp_f32 v216, v216\n\t"
    "v_exp_f32 v217, v217\n\t"
    "v_fma_f32 v218, v176, s63, v225\n\t"
    "v_mfma_f32_32x32x16_bf16 a[144:159], a[104:107], v[196:199], a[144:159]\n\t"
    "ds_read_b128 a[104:107], v232 offset:24576\n\t"
    "v_fma_f32 v219, v177, s63, v225\n\t"
    "v_exp_f32 v218, v218\n\t"
    "v_mfma_f32_32x32x16_bf16 v[48:63], a[108:111], v[192:195], v[48:63]\n\t"
    "v_exp_f32 v219, v219\n\t"
    "v_cvt_pk_bf16_f32 v200, v216, v217\n\t"
    "v_add_f32 v228, v216, v217\n\t"
    "v_mfma_f32_32x32x16_bf16 a[160:175], a[108:111], v[196:199], a[160:175]\n\t"
    "ds_read_b128 a[108:111], v233 offset:24576\n\t"
    "v_fma_f32 v216, v162, s63, v224\n\t"
    "v_fma_f32 v217, v163, s63, v224\n\t"
    "v_mfma_f32_32x32x16_bf16 v[64:79], a[112:115], v[192:195], v[64:79]\n\t"
    "v_exp_f32 v216, v216\n\t"
    "v_exp_f32 v217, v217\n\t"
    "v_cvt_pk_bf16_f32 v204, v218, v219\n\t"
    "v_mfma_f32_32x32x16_bf16 a[176:191], a[112:115], v[196:199], a[176:191]\n\t"
    "ds_read_b128 a[112:115], v230 offset:25600\n\t"
    "v_add_f32 v229, v218, v219\n\t"
    "v_fma_f32 v218, v178, s63, v225\n\t"
    "v_fma_f32 v219, v179, s63, v225\n\t"
    "v_mfma_f32_32x32x16_bf16 v[80:95], a[116:119], v[192:195], v[80:95]\n\t"
    "v_exp_f32 v218, v218\n\t"
    "v_exp_f32 v219, v219\n\t"
    "v_mfma_f32_32x32x16_bf16 a[192:207], a[116:119], v[196:199], a[192:207]\n\t"
    "ds_read_b128 a[116:119], v231 offset:25600\n\t"
    "v_cvt_pk_bf16_f32 v201, v216, v217\n\t"
    "v_add_f32 v228, v228, v216\n\t"
    "v_add_f32 v228, v228, v217\n\t"
    "s_waitcnt lgkmcnt(10)\n\t"
    "v_mfma_f32_32x32x16_bf16 v[0:15], a[120:123], v[208:211], v[0:15]\n\t"
    "v_fma_f32 v216, v164, s63, v224\n\t"
    "v_fma_f32 v217, v165, s63, v224\n\t"
    "v_exp_f32 v216, v216\n\t"
    "v_mfma_f32_32x32x16_bf16 v[96:111], a[120:123], v[212:215], v[96:111]\n\t"
    "ds_read_b128 a[120:123], v232 offset:25600\n\t"
    "v_exp_f32 v217, v217\n\t"
    "v_mfma_f32_32x32x16_bf16 v[16:31], a[124:127], v[208:211], v[16:31]\n\t"
    "v_cvt_pk_bf16_f32 v205, v218, v219\n\t"
    "v_add_f32 v229, v229, v218\n\t"
    "v_add_f32 v229, v229, v219\n\t"
    "v_fma_f32 v218, v180, s63, v225\n\t"
    "v_mfma_f32_32x32x16_bf16 v[112:127], a[124:127], v[212:215], v[112:127]\n\t"
    "ds_read_b128 a[124:127], v233 offset:25600\n\t"
    "v_fma_f32 v219, v181, s63, v225\n\t"
    "v_exp_f32 v218, v218\n\t"
    "v_mfma_f32_32x32x16_bf16 v[32:47], a[128:131], v[208:211], v[32:47]\n\t"
    "v_exp_f32 v219, v219\n\t"
    "v_cvt_pk_bf16_f32 v202, v216, v217\n\t"
    "v_add_f32 v228, v228, v216\n\t"
    "v_mfma_f32_32x32x16_bf16 a[144:159], a[128:131], v[212:215], a[144:159]\n\t"
    "ds_read_b128 a[128:131], v230 offset:26624\n\t"
    "v_add_f32 v228, v228, v217\n\t"
    "v_fma_f32 v216, v166, s63, v224\n\t"
    "v_fma_f32 v217, v167, s63, v224\n\t"
    "v_mfma_f32_32x32x16_bf16 v[48:63], a[132:135], v[208:211], v[48:63]\n\t"
    "v_exp_f32 v216, v216\n\t"
    "v_exp_f32 v217, v217\n\t"
    "v_mfma_f32_32x32x16_bf16 a[160:175], a[132:135], v[212:215], a[160:175]\n\t"
    "ds_read_b128 a[132:135], v231 offset:26624\n\t"
    "v_cvt_pk_bf16_f32 v206, v218, v219\n\t"
    "v_add_f32 v229, v229, v218\n\t"
    "v_add_f32 v229, v229, v219\n\t"
    "s_waitcnt lgkmcnt(10)\n\t"
    "v_mfma_f32_32x32x16_bf16 v[64:79], a[136:139], v[208:211], v[64:79]\n\t"
    "v_fma_f32 v218, v182, s63, v225\n\t"
    "v_fma_f32 v219, v183, s63, v225\n\t"
    "v_exp_f32 v218, v218\n\t"
    "v_mfma_f32_32x32x16_bf16 a[176:191], a[136:139], v[212:215], a[176:191]\n\t"
    "ds_read_b128 a[136:139], v232 offset:26624\n\t"
    "v_exp_f32 v219, v219\n\t"
    "v_cvt_pk_bf16_f32 v203, v216, v217\n\t"
    "v_mfma_f32_32x32x16_bf16 v[80:95], a[140:143], v[208:211], v[80:95]\n\t"
    "v_add_f32 v228, v228, v216\n\t"
    "v_add_f32 v228, v228, v217\n\t"
    "s_nop 0\n\t"
    "v_cvt_pk_bf16_f32 v207, v218, v219\n\t"
    "v_mfma_f32_32x32x16_bf16 a[192:207], a[140:143], v[212:215], a[192:207]\n\t"
    "ds_read_b128 a[140:143], v233 offset:26624\n\t"
    "v_add_f32 v229, v229, v218\n\t"
    "v_add_f32 v229, v229, v219\n\t"
    "s_waitcnt lgkmcnt(10)\n\t"
    "v_mfma_f32_32x32x16_bf16 v[128:143], a[96:99], a[0:3], 0\n\t"
    "v_fma_f32 v216, v168, s63, v224\n\t"
    "v_fma_f32 v217, v169, s63, v224\n\t"
    "v_exp_f32 v216, v216\n\t"
    "v_mfma_f32_32x32x16_bf16 v[144:159], a[96:99], a[48:51], 0\n\t"
    "v_exp_f32 v217, v217\n\t"
    "v_fma_f32 v218, v184, s63, v225\n\t"
    "v_fma_f32 v219, v185, s63, v225\n\t"
    "v_exp_f32 v218, v218\n\t"
    "s_waitcnt lgkmcnt(9)\n\t"
    "v_mfma_f32_32x32x16_bf16 v[128:143], a[100:103], a[4:7], v[128:143]\n\t"
    "v_exp_f32 v219, v219\n\t"
    "v_cvt_pk_bf16_f32 v208, v216, v217\n\t"
    "v_add_f32 v228, v228, v216\n\t"
    "v_mfma_f32_32x32x16_bf16 v[144:159], a[100:103], a[52:55], v[144:159]\n\t"
    "v_add_f32 v228, v228, v217\n\t"
    "s_waitcnt vmcnt(19) lgkmcnt(0)\n\t"
    "s_barrier\n\t"
    "v_mfma_f32_32x32x16_bf16 v[128:143], a[104:107], a[8:11], v[128:143]\n\t"
    "ds_read_b64_tr_b16 a[96:97], v234 offset:12288\n\t"
    "ds_read_b64_tr_b16 a[98:99], v234 offset:15360\n\t"
    "s_mov_b32 m0, s49\n\t"
    "s_mov_b32 s22, s42\n\t"
    "buffer_load_dwordx4 v238, s[20:23], s36 offen lds\n\t"
    "v_mfma_f32_32x32x16_bf16 v[144:159], a[104:107], a[56:59], v[144:159]\n\t"
    "ds_read_b64_tr_b16 a[100:101], v234 offset:12800\n\t"
    "ds_read_b64_tr_b16 a[102:103], v234 offset:15872\n\t"
    "v_fma_f32 v216, v170, s63, v224\n\t"
    "v_fma_f32 v217, v171, s63, v224\n\t"
    "v_mfma_f32_32x32x16_bf16 v[128:143], a[108:111], a[12:15], v[128:143]\n\t"
    "ds_read_b64_tr_b16 a[104:105], v234 offset:13312\n\t"
    "ds_read_b64_tr_b16 a[106:107], v234 offset:16384\n\t"
    "v_exp_f32 v216, v216\n\t"
    "v_exp_f32 v217, v217\n\t"
    "v_mfma_f32_32x32x16_bf16 v[144:159], a[108:111], a[60:63], v[144:159]\n\t"
    "ds_read_b64_tr_b16 a[108:109], v234 offset:13824\n\t"
    "ds_read_b64_tr_b16 a[110:111], v234 offset:16896\n\t"
    "v_cvt_pk_bf16_f32 v212, v218, v219\n\t"
    "v_add_f32 v229, v229, v218\n\t"
    "v_mfma_f32_32x32x16_bf16 v[128:143], a[112:115], a[16:19], v[128:143]\n\t"
    "buffer_load_dwordx4 v239, s[20:23], s36 offen offset:1024 lds\n\t"
    "v_add_f32 v229, v229, v219\n\t"
    "v_fma_f32 v218, v186, s63, v225\n\t"
    "v_fma_f32 v219, v187, s63, v225\n\t"
    "v_exp_f32 v218, v218\n\t"
    "v_mfma_f32_32x32x16_bf16 v[144:159], a[112:115], a[64:67], v[144:159]\n\t"
    "ds_read_b64_tr_b16 a[112:113], v234 offset:14336\n\t"
    "ds_read_b64_tr_b16 a[114:115], v234 offset:17408\n\t"
    "v_exp_f32 v219, v219\n\t"
    "v_mfma_f32_32x32x16_bf16 v[128:143], a[116:119], a[20:23], v[128:143]\n\t"
    "v_cvt_pk_bf16_f32 v209, v216, v217\n\t"
    "v_add_f32 v228, v228, v216\n\t"
    "v_add_f32 v228, v228, v217\n\t"
    "v_fma_f32 v216, v172, s63, v224\n\t"
    "v_fma_f32 v217, v173, s63, v224\n\t"
    "v_mfma_f32_32x32x16_bf16 v[144:159], a[116:119], a[68:71], v[144:159]\n\t"
    "ds_read_b64_tr_b16 a[116:117], v234 offset:14848\n\t"
    "ds_read_b64_tr_b16 a[118:119], v234 offset:17920\n\t"
    "v_exp_f32 v216, v216\n\t"
    "v_mfma_f32_32x32x16_bf16 v[128:143], a[120:123], a[24:27], v[128:143]\n\t"
    "buffer_load_dwordx4 v240, s[20:23], s36 offen offset:2048 lds\n\t"
    "v_exp_f32 v217, v217\n\t"
    "v_cvt_pk_bf16_f32 v213, v218, v219\n\t"
    "v_add_f32 v229, v229, v218\n\t"
    "v_mfma_f32_32x32x16_bf16 v[144:159], a[120:123], a[72:75], v[144:159]\n\t"
    "ds_read_b64_tr_b16 a[120:121], v234 offset:18432\n\t"
    "ds_read_b64_tr_b16 a[122:123], v234 offset:21504\n\t"
    "v_add_f32 v229, v229, v219\n\t"
    "v_fma_f32 v218, v188, s63, v225\n\t"
    "v_fma_f32 v219, v189, s63, v225\n\t"
    "v_mfma_f32_32x32x16_bf16 v[128:143], a[124:127], a[28:31], v[128:143]\n\t"
    "v_exp_f32 v218, v218\n\t"
    "v_exp_f32 v219, v219\n\t"
    "v_cvt_pk_bf16_f32 v210, v216, v217\n\t"
    "v_mfma_f32_32x32x16_bf16 v[144:159], a[124:127], a[76:79], v[144:159]\n\t"
    "ds_read_b64_tr_b16 a[124:125], v234 offset:18944\n\t"
    "ds_read_b64_tr_b16 a[126:127], v234 offset:22016\n\t"
    "v_add_f32 v228, v228, v216\n\t"
    "v_add_f32 v228, v228, v217\n\t"
    "v_fma_f32 v216, v174, s63, v224\n\t"
    "v_mfma_f32_32x32x16_bf16 v[128:143], a[128:131], a[32:35], v[128:143]\n\t"
    "s_add_u32 m0, s46, 24576\n\t"
    "s_nop 0\n\t"
    "buffer_load_dwordx4 v235, s[16:19], s38 offen lds\n\t"
    "v_fma_f32 v217, v175, s63, v224\n\t"
    "v_mfma_f32_32x32x16_bf16 v[144:159], a[128:131], a[80:83], v[144:159]\n\t"
    "ds_read_b64_tr_b16 a[128:129], v234 offset:19456\n\t"
    "ds_read_b64_tr_b16 a[130:131], v234 offset:22528\n\t"
    "v_exp_f32 v216, v216\n\t"
    "v_exp_f32 v217, v217\n\t"
    "v_mfma_f32_32x32x16_bf16 v[128:143], a[132:135], a[36:39], v[128:143]\n\t"
    "v_cvt_pk_bf16_f32 v214, v218, v219\n\t"
    "v_add_f32 v229, v229, v218\n\t"
    "v_add_f32 v229, v229, v219\n\t"
    "v_fma_f32 v218, v190, s63, v225\n\t"
    "v_mfma_f32_32x32x16_bf16 v[144:159], a[132:135], a[84:87], v[144:159]\n\t"
    "ds_read_b64_tr_b16 a[132:133], v234 offset:19968\n\t"
    "ds_read_b64_tr_b16 a[134:135], v234 offset:23040\n\t"
    "v_fma_f32 v219, v191, s63, v225\n\t"
    "v_exp_f32 v218, v218\n\t"
    "v_mfma_f32_32x32x16_bf16 v[128:143], a[136:139], a[40:43], v[128:143]\n\t"
    "buffer_load_dwordx4 v236, s[16:19], s38 offen offset:1024 lds\n\t"
    "v_exp_f32 v219, v219\n\t"
    "v_cvt_pk_bf16_f32 v211, v216, v217\n\t"
    "v_add_f32 v228, v228, v216\n\t"
    "v_mfma_f32_32x32x16_bf16 v[144:159], a[136:139], a[88:91], v[144:159]\n\t"
    "ds_read_b64_tr_b16 a[136:137], v234 offset:20480\n\t"
    "ds_read_b64_tr_b16 a[138:139], v234 offset:23552\n\t"
    "v_add_f32 v228, v228, v217\n\t"
    "s_nop 0\n\t"
    "v_cvt_pk_bf16_f32 v215, v218, v219\n\t"
    "v_mfma_f32_32x32x16_bf16 v[128:143], a[140:143], a[44:47], v[128:143]\n\t"
    "s_waitcnt vmcnt(11)\n\t"
    "ds_read_b128 a[240:243], v244 offset:8192\n\t"
    "v_add_f32 v229, v229, v218\n\t"
    "v_add_f32 v229, v229, v219\n\t"
    "v_mfma_f32_32x32x16_bf16 v[144:159], a[140:143], a[92:95], v[144:159]\n\t"
    "ds_read_b64_tr_b16 a[140:141], v234 offset:20992\n\t"
    "ds_read_b64_tr_b16 a[142:143], v234 offset:24064\n\t"
    "v_add_f32 v251, v228, v229\n\t"
    "v_cmp_nge_f32 vcc, s64, v251\n\t"
    "s_cbranch_vccnz .Ld_stub12\n\t"
    ".Ld_back12:\n\t"
    "s_waitcnt lgkmcnt(13)\n\t"
    "v_mfma_f32_32x32x16_bf16 v[0:15], a[96:99], v[200:203], v[0:15]\n\t"
    "buffer_load_dwordx4 v237, s[16:19], s38 offen offset:2048 lds\n\t"
    "ds_read_b128 a[244:247], v245 offset:8192\n\t"
    "v_add_f32 v226, v226, v228\n\t"
    "v_add_f32 v227, v227, v229\n\t"
    "v_mfma_f32_32x32x16_bf16 v[96:111], a[96:99], v[204:207], v[96:111]\n\t"
    "ds_read_b128 a[96:99], v230 offset:36864\n\t"
    "v_mfma_f32_32x32x16_bf16 v[16:31], a[100:103], v[200:203], v[16:31]\n\t"
    "ds_read_b128 a[248:251], v246 offset:8192\n\t"
    "v_mfma_f32_32x32x16_bf16 v[112:127], a[100:103], v[204:207], v[112:127]\n\t"
    "ds_read_b128 a[100:103], v231 offset:36864\n\t"
    "v_fma_f32 v216, v128, s63, v224\n\t"
    "v_fma_f32 v217, v129, s63, v224\n\t"
    "v_exp_f32 v216, v216\n\t"
    "v_mfma_f32_32x32x16_bf16 v[32:47], a[104:107], v[200:203], v[32:47]\n\t"
    "ds_read_b128 a[252:255], v247 offset:8192\n\t"
    "v_exp_f32 v217, v217\n\t"
    "v_fma_f32 v218, v144, s63, v225\n\t"
    "v_mfma_f32_32x32x16_bf16 a[144:159], a[104:107], v[204:207], a[144:159]\n\t"
    "ds_read_b128 a[104:107], v232 offset:36864\n\t"
    "v_fma_f32 v219, v145, s63, v225\n\t"
    "v_exp_f32 v218, v218\n\t"
    "v_mfma_f32_32x32x16_bf16 v[48:63], a[108:111], v[200:203], v[48:63]\n\t"
    "v_exp_f32 v219, v219\n\t"
    "v_cvt_pk_bf16_f32 v192, v216, v217\n\t"
    "v_add_f32 v228, v216, v217\n\t"
    "v_fma_f32 v216, v130, s63, v224\n\t"
    "v_mfma_f32_32x32x16_bf16 a[160:175], a[108:111], v[204:207], a[160:175]\n\t"
    "ds_read_b128 a[108:111], v233 offset:36864\n\t"
    "s_waitcnt lgkmcnt(2)\n\t"
    "s_add_u32 m0, s61, 7936\n\t"
    "s_nop 0\n\t"
    "buffer_load_dwordx4 v248, s[24:27], 0 offen offset:256 nt lds\n\t"
    "v_mfma_f32_32x32x16_bf16 v[64:79], a[112:115], v[200:203], v[64:79]\n\t"
    "v_fma_f32 v217, v131, s63, v224\n\t"
    "v_exp_f32 v216, v216\n\t"
    "v_exp_f32 v217, v217\n\t"
    "v_mfma_f32_32x32x16_bf16 a[176:191], a[112:115], v[204:207], a[176:191]\n\t"
    "ds_read_b128 a[112:115], v230 offset:37888\n\t"
    "v_cvt_pk_bf16_f32 v196, v218, v219\n\t"
    "v_add_f32 v229, v218, v219\n\t"
    "v_fma_f32 v218, v146, s63, v225\n\t"
    "v_mfma_f32_32x32x16_bf16 v[80:95], a[116:119], v[200:203], v[80:95]\n\t"
    "v_fma_f32 v219, v147, s63, v225\n\t"
    "v_exp_f32 v218, v218\n\t"
    "v_exp_f32 v219, v219\n\t"
    "v_mfma_f32_32x32x16_bf16 a[192:207], a[116:119], v[204:207], a[192:207]\n\t"
    "ds_read_b128 a[116:119], v231 offset:37888\n\t"
    "v_cvt_pk_bf16_f32 v193, v216, v217\n\t"
    "v_add_f32 v228, v228, v216\n\t"
    "v_add_f32 v228, v228, v217\n\t"
    "v_mfma_f32_32x32x16_bf16 v[0:15], a[120:123], v[208:211], v[0:15]\n\t"
    "v_fma_f32 v216, v132, s63, v224\n\t"
    "v_fma_f32 v217, v133, s63, v224\n\t"
    "v_exp_f32 v216, v216\n\t"
    "v_mfma_f32_32x32x16_bf16 v[96:111], a[120:123], v[212:215], v[96:111]\n\t"
    "ds_read_b128 a[120:123], v232 offset:37888\n\t"
    "s_add_u32 m0, s61, 8960\n\t"
    "s_nop 0\n\t"
    "buffer_load_dwordx4 v249, s[24:27], 0 offen offset:256 nt lds\n\t"
    "v_mfma_f32_32x32x16_bf16 v[16:31], a[124:127], v[208:211], v[16:31]\n\t"
    "v_exp_f32 v217, v217\n\t"
    "v_cvt_pk_bf16_f32 v197, v218, v219\n\t"
    "v_add_f32 v229, v229, v218\n\t"
    "v_add_f32 v229, v229, v219\n\t"
    "v_mfma_f32_32x32x16_bf16 v[112:127], a[124:127], v[212:215], v[112:127]\n\t"
    "ds_read_b128 a[124:127], v233 offset:37888\n\t"
    "v_fma_f32 v218, v148, s63, v225\n\t"
    "v_fma_f32 v219, v149, s63, v225\n\t"
    "v_exp_f32 v218, v218\n\t"
    "v_mfma_f32_32x32x16_bf16 v[32:47], a[128:131], v[208:211], v[32:47]\n\t"
    "v_exp_f32 v219, v219\n\t"
    "v_cvt_pk_bf16_f32 v194, v216, v217\n\t"
    "v_add_f32 v228, v228, v216\n\t"
    "v_mfma_f32_32x32x16_bf16 a[144:159], a[128:131], v[212:215], a[144:159]\n\t"
    "ds_read_b128 a[128:131], v230 offset:38912\n\t"
    "v_add_f32 v228, v228, v217\n\t"
    "v_fma_f32 v216, v134, s63, v224\n\t"
    "v_fma_f32 v217, v135, s63, v224\n\t"
    "v_mfma_f32_32x32x16_bf16 v[48:63], a[132:135], v[208:211], v[48:63]\n\t"
    "v_exp_f32 v216, v216\n\t"
    "v_exp_f32 v217, v217\n\t"
    "v_cvt_pk_bf16_f32 v198, v218, v219\n\t"
    "v_mfma_f32_32x32x16_bf16 a[160:175], a[132:135], v[212:215], a[160:175]\n\t"
    "ds_read_b128 a[132:135], v231 offset:38912\n\t"
    "s_add_u32 m0, s61, 9984\n\t"
    "s_nop 0\n\t"
    "buffer_load_dwordx4 v248, s[24:27], s86 offen offset:256 nt lds\n\t"
    "v_add_f32 v229, v229, v218\n\t"
    "v_mfma_f32_32x32x16_bf16 v[64:79], a[136:139], v[208:211], v[64:79]\n\t"
    "v_add_f32 v229, v229, v219\n\t"
    "v_fma_f32 v218, v150, s63, v225\n\t"
    "v_fma_f32 v219, v151, s63, v225\n\t"
    "v_exp_f32 v218, v218\n\t"
    "v_mfma_f32_32x32x16_bf16 a[176:191], a[136:139], v[212:215], a[176:191]\n\t"
    "ds_read_b128 a[136:139], v232 offset:38912\n\t"
    "v_exp_f32 v219, v219\n\t"
    "v_cvt_pk_bf16_f32 v195, v216, v217\n\t"
    "v_mfma_f32_32x32x16_bf16 v[80:95], a[140:143], v[208:211], v[80:95]\n\t"
    "v_add_f32 v228, v228, v216\n\t"
    "v_add_f32 v228, v228, v217\n\t"
    "s_nop 0\n\t"
    "v_cvt_pk_bf16_f32 v199, v218, v219\n\t"
    "v_mfma_f32_32x32x16_bf16 a[192:207], a[140:143], v[212:215], a[192:207]\n\t"
    "ds_read_b128 a[140:143], v233 offset:38912\n\t"
    "v_add_f32 v229, v229, v218\n\t"
    "v_add_f32 v229, v229, v219\n\t"
    "v_mfma_f32_32x32x16_bf16 v[160:175], a[96:99], a[0:3], 0\n\t"
    "v_fma_f32 v216, v136, s63, v224\n\t"
    "v_fma_f32 v217, v137, s63, v224\n\t"
    "v_exp_f32 v216, v216\n\t"
    "v_mfma_f32_32x32x16_bf16 v[176:191], a[96:99], a[48:51], 0\n\t"
    "s_add_u32 m0, s61, 11008\n\t"
    "s_nop 0\n\t"
    "buffer_load_dwordx4 v249, s[24:27], s86 offen offset:256 nt lds\n\t"
    "v_exp_f32 v217, v217\n\t"
    "v_fma_f32 v218, v152, s63, v225\n\t"
    "s_waitcnt lgkmcnt(9)\n\t"
    "v_mfma_f32_32x32x16_bf16 v[160:175], a[100:103], a[4:7], v[160:175]\n\t"
    "v_fma_f32 v219, v153, s63, v225\n\t"
    "v_exp_f32 v218, v218\n\t"
    "v_exp_f32 v219, v219\n\t"
    "v_mfma_f32_32x32x16_bf16 v[176:191], a[100:103], a[52:55], v[176:191]\n\t"
    "v_cvt_pk_bf16_f32 v208, v216, v217\n\t"
    "s_waitcnt vmcnt(22) lgkmcnt(0)\n\t"
    "s_barrier\n\t"
    "v_mfma_f32_32x32x16_bf16 v[160:175], a[104:107], a[8:11], v[160:175]\n\t"
    "ds_read_b64_tr_b16 a[96:97], v234 offset:24576\n\t"
    "ds_read_b64_tr_b16 a[98:99], v234 offset:27648\n\t"
    "s_add_u32 m0, s49, 12288\n\t"
    "s_nop 0\n\t"
    "buffer_load_dwordx4 v238, s[20:23], s37 offen lds\n\t"
    "v_mfma_f32_32x32x16_bf16 v[176:191], a[104:107], a[56:59], v[176:191]\n\t"
    "ds_read_b64_tr_b16 a[100:101], v234 offset:25088\n\t"
    "ds_read_b64_tr_b16 a[102:103], v234 offset:28160\n\t"
    "v_add_f32 v228, v228, v216\n\t"
    "v_add_f32 v228, v228, v217\n\t"
    "v_mfma_f32_32x32x16_bf16 v[160:175], a[108:111], a[12:15], v[160:175]\n\t"
    "ds_read_b64_tr_b16 a[104:105], v234 offset:25600\n\t"
    "ds_read_b64_tr_b16 a[106:107], v234 offset:28672\n\t"
    "buffer_load_dwordx4 v239, s[20:23], s37 offen offset:1024 lds\n\t"
    "v_fma_f32 v216, v138, s63, v224\n\t"
    "v_fma_f32 v217, v139, s63, v224\n\t"
    "v_mfma_f32_32x32x16_bf16 v[176:191], a[108:111], a[60:63], v[176:191]\n\t"
    "ds_read_b64_tr_b16 a[108:109], v234 offset:26112\n\t"
    "ds_read_b64_tr_b16 a[110:111], v234 offset:29184\n\t"
    "v_exp_f32 v216, v216\n\t"
    "v_exp_f32 v217, v217\n\t"
    "v_mfma_f32_32x32x16_bf16 v[160:175], a[112:115], a[16:19], v[160:175]\n\t"
    "buffer_load_dwordx4 v240, s[20:23], s37 offen offset:2048 lds\n\t"
    "v_cvt_pk_bf16_f32 v212, v218, v219\n\t"
    "v_add_f32 v229, v229, v218\n\t"
    "v_add_f32 v229, v229, v219\n\t"
    "v_mfma_f32_32x32x16_bf16 v[176:191], a[112:115], a[64:67], v[176:191]\n\t"
    "ds_read_b64_tr_b16 a[112:113], v234 offset:26624\n\t"
    "ds_read_b64_tr_b16 a[114:115], v234 offset:29696\n\t"
    "v_fma_f32 v218, v154, s63, v225\n\t"
    "v_fma_f32 v219, v155, s63, v225\n\t"
    "v_mfma_f32_32x32x16_bf16 v[160:175], a[116:119], a[20:23], v[160:175]\n\t"
    "s_add_u32 m0, s46, 36864\n\t"
    "s_nop 0\n\t"
    "buffer_load_dwordx4 v235, s[16:19], s39 offen lds\n\t"
    "v_exp_f32 v218, v218\n\t"
    "v_mfma_f32_32x32x16_bf16 v[176:191], a[116:119], a[68:71], v[176:191]\n\t"
    "ds_read_b64_tr_b16 a[116:117], v234 offset:27136\n\t"
    "ds_read_b64_tr_b16 a[118:119], v234 offset:30208\n\t"
    "v_exp_f32 v219, v219\n\t"
    "v_cvt_pk_bf16_f32 v209, v216, v217\n\t"
    "v_add_f32 v228, v228, v216\n\t"
    "v_mfma_f32_32x32x16_bf16 v[160:175], a[120:123], a[24:27], v[160:175]\n\t"
    "buffer_load_dwordx4 v236, s[16:19], s39 offen offset:1024 lds\n\t"
    "v_add_f32 v228, v228, v217\n\t"
    "v_fma_f32 v216, v140, s63, v224\n\t"
    "v_fma_f32 v217, v141, s63, v224\n\t"
    "v_mfma_f32_32x32x16_bf16 v[176:191], a[120:123], a[72:75], v[176:191]\n\t"
    "ds_read_b64_tr_b16 a[120:121], v234 offset:30720\n\t"
    "ds_read_b64_tr_b16 a[122:123], v234 offset:33792\n\t"
    "v_exp_f32 v216, v216\n\t"
    "v_exp_f32 v217, v217\n\t"
    "v_mfma_f32_32x32x16_bf16 v[160:175], a[124:127], a[28:31], v[160:175]\n\t"
    "buffer_load_dwordx4 v237, s[16:19], s39 offen offset:2048 lds\n\t"
    "v_cvt_pk_bf16_f32 v213, v218, v219\n\t"
    "v_add_f32 v229, v229, v218\n\t"
    "v_add_f32 v229, v229, v219\n\t"
    "v_mfma_f32_32x32x16_bf16 v[176:191], a[124:127], a[76:79], v[176:191]\n\t"
    "ds_read_b64_tr_b16 a[124:125], v234 offset:31232\n\t"
    "ds_read_b64_tr_b16 a[126:127], v234 offset:34304\n\t"
    "v_fma_f32 v218, v156, s63, v225\n\t"
    "v_fma_f32 v219, v157, s63, v225\n\t"
    "v_exp_f32 v218, v218\n\t"
    "v_mfma_f32_32x32x16_bf16 v[160:175], a[128:131], a[32:35], v[160:175]\n\t"
    "v_exp_f32 v219, v219\n\t"
    "v_cvt_pk_bf16_f32 v210, v216, v217\n\t"
    "v_add_f32 v228, v228, v216\n\t"
    "v_mfma_f32_32x32x16_bf16 v[176:191], a[128:131], a[80:83], v[176:191]\n\t"
    "ds_read_b64_tr_b16 a[128:129], v234 offset:31744\n\t"
    "ds_read_b64_tr_b16 a[130:131], v234 offset:34816\n\t"
    "v_add_f32 v228, v228, v217\n\t"
    "v_fma_f32 v216, v142, s63, v224\n\t"
    "v_fma_f32 v217, v143, s63, v224\n\t"
    "v_mfma_f32_32x32x16_bf16 v[160:175], a[132:135], a[36:39], v[160:175]\n\t"
    "v_exp_f32 v216, v216\n\t"
    "v_exp_f32 v217, v217\n\t"
    "v_cvt_pk_bf16_f32 v214, v218, v219\n\t"
    "v_mfma_f32_32x32x16_bf16 v[176:191], a[132:135], a[84:87], v[176:191]\n\t"
    "ds_read_b64_tr_b16 a[132:133], v234 offset:32256\n\t"
    "ds_read_b64_tr_b16 a[134:135], v234 offset:35328\n\t"
    "v_add_f32 v229, v229, v218\n\t"
    "v_add_f32 v229, v229, v219\n\t"
    "v_fma_f32 v218, v158, s63, v225\n\t"
    "v_mfma_f32_32x32x16_bf16 v[160:175], a[136:139], a[40:43], v[160:175]\n\t"
    "v_fma_f32 v219, v159, s63, v225\n\t"
    "v_exp_f32 v218, v218\n\t"
    "v_exp_f32 v219, v219\n\t"
    "v_mfma_f32_32x32x16_bf16 v[176:191], a[136:139], a[88:91], v[176:191]\n\t"
    "ds_read_b64_tr_b16 a[136:137], v234 offset:32768\n\t"
    "ds_read_b64_tr_b16 a[138:139], v234 offset:35840\n\t"
    "v_cvt_pk_bf16_f32 v211, v216, v217\n\t"
    "v_add_f32 v228, v228, v216\n\t"
    "v_add_f32 v228, v228, v217\n\t"
    "v_mfma_f32_32x32x16_bf16 v[160:175], a[140:143], a[44:47], v[160:175]\n\t"
    "s_nop 0\n\t"
    "v_cvt_pk_bf16_f32 v215, v218, v219\n\t"
    "v_add_f32 v229, v229, v218\n\t"
    "v_add_f32 v229, v229, v219\n\t"
    "v_add_f32 v251, v228, v229\n\t"
    "v_mfma_f32_32x32x16_bf16 v[176:191], a[140:143], a[92:95], v[176:191]\n\t"
    "ds_read_b64_tr_b16 a[140:141], v234 offset:33280\n\t"
    "ds_read_b64_tr_b16 a[142:143], v234 offset:36352\n\t"
    "v_cmp_nge_f32 vcc, s64, v251\n\t"
    "s_cbranch_vccnz .Ld_stub13\n\t"
    ".Ld_back13:\n\t"
    "s_waitcnt lgkmcnt(12)\n\t"
    "v_mfma_f32_32x32x16_bf16 v[0:15], a[96:99], v[192:195], v[0:15]\n\t"
    "v_add_f32 v226, v226, v228\n\t"
    "v_add_f32 v227, v227, v229\n\t"
    "v_mfma_f32_32x32x16_bf16 v[96:111], a[96:99], v[196:199], v[96:111]\n\t"
    "v_mfma_f32_32x32x16_bf16 v[16:31], a[100:103], v[192:195], v[16:31]\n\t"
    "v_mfma_f32_32x32x16_bf16 v[112:127], a[100:103], v[196:199], v[112:127]\n\t"
    "v_fma_f32 v216, v160, s63, v224\n\t"
    "v_fma_f32 v217, v161, s63, v224\n\t"
    "v_exp_f32 v216, v216\n\t"
    "s_waitcnt vmcnt(19) lgkmcnt(0)\n\t"
    "s_barrier\n\t"
    "v_mfma_f32_32x32x16_bf16 v[32:47], a[104:107], v[192:195], v[32:47]\n\t"
    "ds_read_b64_tr_b16 a[96:97], v234 offset:36864\n\t"
    "ds_read_b64_tr_b16 a[98:99], v234 offset:39936\n\t"
    "v_exp_f32 v217, v217\n\t"
    "v_fma_f32 v218, v176, s63, v225\n\t"
    "v_fma_f32 v219, v177, s63, v225\n\t"
    "v_exp_f32 v218, v218\n\t"
    "v_exp_f32 v219, v219\n\t"
    "v_mfma_f32_32x32x16_bf16 a[144:159], a[104:107], v[196:199], a[144:159]\n\t"
    "ds_read_b64_tr_b16 a[100:101], v234 offset:37376\n\t"
    "ds_read_b64_tr_b16 a[102:103], v234 offset:40448\n\t"
    "v_cvt_pk_bf16_f32 v200, v216, v217\n\t"
    "v_add_f32 v228, v216, v217\n\t"
    "v_fma_f32 v216, v162, s63, v224\n\t"
    "v_fma_f32 v217, v163, s63, v224\n\t"
    "v_exp_f32 v216, v216\n\t"
    "v_mfma_f32_32x32x16_bf16 v[48:63], a[108:111], v[192:195], v[48:63]\n\t"
    "ds_read_b64_tr_b16 a[104:105], v234 offset:37888\n\t"
    "ds_read_b64_tr_b16 a[106:107], v234 offset:40960\n\t"
    "s_add_u32 m0, s49, 24576\n\t"
    "s_nop 0\n\t"
    "buffer_load_dwordx4 v238, s[20:23], s38 offen lds\n\t"
    "v_exp_f32 v217, v217\n\t"
    "v_cvt_pk_bf16_f32 v204, v218, v219\n\t"
    "v_mfma_f32_32x32x16_bf16 a[160:175], a[108:111], v[196:199], a[160:175]\n\t"
    "ds_read_b64_tr_b16 a[108:109], v234 offset:38400\n\t"
    "ds_read_b64_tr_b16 a[110:111], v234 offset:41472\n\t"
    "v_add_f32 v229, v218, v219\n\t"
    "v_fma_f32 v218, v178, s63, v225\n\t"
    "v_fma_f32 v219, v179, s63, v225\n\t"
    "v_exp_f32 v218, v218\n\t"
    "v_exp_f32 v219, v219\n\t"
    "v_mfma_f32_32x32x16_bf16 v[64:79], a[112:115], v[192:195], v[64:79]\n\t"
    "v_cvt_pk_bf16_f32 v201, v216, v217\n\t"
    "v_add_f32 v228, v228, v216\n\t"
    "v_add_f32 v228, v228, v217\n\t"
    "v_fma_f32 v216, v164, s63, v224\n\t"
    "v_fma_f32 v217, v165, s63, v224\n\t"
    "v_exp_f32 v216, v216\n\t"
    "v_exp_f32 v217, v217\n\t"
    "v_mfma_f32_32x32x16_bf16 a[176:191], a[112:115], v[196:199], a[176:191]\n\t"
    "ds_read_b64_tr_b16 a[112:113], v234 offset:38912\n\t"
    "ds_read_b64_tr_b16 a[114:115], v234 offset:41984\n\t"
    "v_cvt_pk_bf16_f32 v205, v218, v219\n\t"
    "v_add_f32 v229, v229, v218\n\t"
    "v_add_f32 v229, v229, v219\n\t"
    "v_fma_f32 v218, v180, s63, v225\n\t"
    "v_fma_f32 v219, v181, s63, v225\n\t"
    "v_mfma_f32_32x32x16_bf16 v[80:95], a[116:119], v[192:195], v[80:95]\n\t"
    "buffer_load_dwordx4 v239, s[20:23], s38 offen offset:1024 lds\n\t"
    "v_exp_f32 v218, v218\n\t"
    "v_exp_f32 v219, v219\n\t"
    "v_cvt_pk_bf16_f32 v202, v216, v217\n\t"
    "v_add_f32 v228, v228, v216\n\t"
    "v_add_f32 v228, v228, v217\n\t"
    "v_fma_f32 v216, v166, s63, v224\n\t"
    "v_mfma_f32_32x32x16_bf16 a[192:207], a[116:119], v[196:199], a[192:207]\n\t"
    "ds_read_b64_tr_b16 a[116:117], v234 offset:39424\n\t"
    "ds_read_b64_tr_b16 a[118:119], v234 offset:42496\n\t"
    "v_fma_f32 v217, v167, s63, v224\n\t"
    "v_exp_f32 v216, v216\n\t"
    "v_exp_f32 v217, v217\n\t"
    "v_cvt_pk_bf16_f32 v206, v218, v219\n\t"
    "v_add_f32 v229, v229, v218\n\t"
    "v_mfma_f32_32x32x16_bf16 v[0:15], a[120:123], v[208:211], v[0:15]\n\t"
    "v_add_f32 v229, v229, v219\n\t"
    "v_fma_f32 v218, v182, s63, v225\n\t"
    "v_fma_f32 v219, v183, s63, v225\n\t"
    "v_exp_f32 v218, v218\n\t"
    "v_exp_f32 v219, v219\n\t"
    "v_cvt_pk_bf16_f32 v203, v216, v217\n\t"
    "v_mfma_f32_32x32x16_bf16 v[96:111], a[120:123], v[212:215], v[96:111]\n\t"
    "ds_read_b64_tr_b16 a[120:121], v234 offset:43008\n\t"
    "ds_read_b64_tr_b16 a[122:123], v234 offset:46080\n\t"
    "v_add_f32 v228, v228, v216\n\t"
    "v_add_f32 v228, v228, v217\n\t"
    "s_nop 0\n\t"
    "v_cvt_pk_bf16_f32 v207, v218, v219\n\t"
    "v_add_f32 v229, v229, v218\n\t"
    "v_add_f32 v229, v229, v219\n\t"
    "v_fma_f32 v216, v168, s63, v224\n\t"
    "v_mfma_f32_32x32x16_bf16 v[16:31], a[124:127], v[208:211], v[16:31]\n\t"
    "buffer_load_dwordx4 v240, s[20:23], s38 offen offset:2048 lds\n\t"
    "v_fma_f32 v217, v169, s63, v224\n\t"
    "v_exp_f32 v216, v216\n\t"
    "v_exp_f32 v217, v217\n\t"
    "v_fma_f32 v218, v184, s63, v225\n\t"
    "v_fma_f32 v219, v185, s63, v225\n\t"
    "v_mfma_f32_32x32x16_bf16 v[112:127], a[124:127], v[212:215], v[112:127]\n\t"
    "ds_read_b64_tr_b16 a[124:125], v234 offset:43520\n\t"
    "ds_read_b64_tr_b16 a[126:127], v234 offset:46592\n\t"
    "v_exp_f32 v218, v218\n\t"
    "v_exp_f32 v219, v219\n\t"
    "v_cvt_pk_bf16_f32 v192, v216, v217\n\t"
    "v_add_f32 v228, v228, v216\n\t"
    "v_add_f32 v228, v228, v217\n\t"
    "v_mfma_f32_32x32x16_bf16 v[32:47], a[128:131], v[208:211], v[32:47]\n\t"
    "v_fma_f32 v216, v170, s63, v224\n\t"
    "v_fma_f32 v217, v171, s63, v224\n\t"
    "v_exp_f32 v216, v216\n\t"
    "v_exp_f32 v217, v217\n\t"
    "v_cvt_pk_bf16_f32 v196, v218, v219\n\t"
    "v_add_f32 v229, v229, v218\n\t"
    "v_add_f32 v229, v229, v219\n\t"
    "v_mfma_f32_32x32x16_bf16 a[144:159], a[128:131], v[212:215], a[144:159]\n\t"
    "ds_read_b64_tr_b16 a[128:129], v234 offset:44032\n\t"
    "ds_read_b64_tr_b16 a[130:131], v234 offset:47104\n\t"
    "v_fma_f32 v218, v186, s63, v225\n\t"
    "v_fma_f32 v219, v187, s63, v225\n\t"
    "v_exp_f32 v218, v218\n\t"
    "v_exp_f32 v219, v219\n\t"
    "v_mfma_f32_32x32x16_bf16 v[48:63], a[132:135], v[208:211], v[48:63]\n\t"
    "v_cvt_pk_bf16_f32 v193, v216, v217\n\t"
    "v_add_f32 v228, v228, v216\n\t"
    "v_add_f32 v228, v228, v217\n\t"
    "v_fma_f32 v216, v172, s63, v224\n\t"
    "v_fma_f32 v217, v173, s63, v224\n\t"
    "v_exp_f32 v216, v216\n\t"
    "v_exp_f32 v217, v217\n\t"
    "v_mfma_f32_32x32x16_bf16 a[160:175], a[132:135], v[212:215], a[160:175]\n\t"
    "ds_read_b64_tr_b16 a[132:133], v234 offset:44544\n\t"
    "ds_read_b64_tr_b16 a[134:135], v234 offset:47616\n\t"
    "v_cvt_pk_bf16_f32 v197, v218, v219\n\t"
    "v_add_f32 v229, v229, v218\n\t"
    "v_add_f32 v229, v229, v219\n\t"
    "v_fma_f32 v218, v188, s63, v225\n\t"
    "v_fma_f32 v219, v189, s63, v225\n\t"
    "v_exp_f32 v218, v218\n\t"
    "v_mfma_f32_32x32x16_bf16 v[64:79], a[136:139], v[208:211], v[64:79]\n\t"
    "v_exp_f32 v219, v219\n\t"
    "v_cvt_pk_bf16_f32 v194, v216, v217\n\t"
    "v_add_f32 v228, v228, v216\n\t"
    "v_add_f32 v228, v228, v217\n\t"
    "v_fma_f32 v216, v174, s63, v224\n\t"
    "v_fma_f32 v217, v175, s63, v224\n\t"
    "v_mfma_f32_32x32x16_bf16 a[176:191], a[136:139], v[212:215], a[176:191]\n\t"
    "ds_read_b64_tr_b16 a[136:137], v234 offset:45056\n\t"
    "ds_read_b64_tr_b16 a[138:139], v234 offset:48128\n\t"
    "v_exp_f32 v216, v216\n\t"
    "v_exp_f32 v217, v217\n\t"
    "v_cvt_pk_bf16_f32 v198, v218, v219\n\t"
    "v_add_f32 v229, v229, v218\n\t"
    "v_add_f32 v229, v229, v219\n\t"
    "v_mfma_f32_32x32x16_bf16 v[80:95], a[140:143], v[208:211], v[80:95]\n\t"
    "v_fma_f32 v218, v190, s63, v225\n\t"
    "v_fma_f32 v219, v191, s63, v225\n\t"
    "v_exp_f32 v218, v218\n\t"
    "v_exp_f32 v219, v219\n\t"
    "v_cvt_pk_bf16_f32 v195, v216, v217\n\t"
    "v_add_f32 v228, v228, v216\n\t"
    "v_add_f32 v228, v228, v217\n\t"
    "v_mfma_f32_32x32x16_bf16 a[192:207], a[140:143], v[212:215], a[192:207]\n\t"
    "ds_read_b64_tr_b16 a[140:141], v234 offset:45568\n\t"
    "ds_read_b64_tr_b16 a[142:143], v234 offset:48640\n\t"
    "s_nop 0\n\t"
    "v_cvt_pk_bf16_f32 v199, v218, v219\n\t"
    "v_add_f32 v229, v229, v218\n\t"
    "v_add_f32 v229, v229, v219\n\t"
    "v_add_f32 v251, v228, v229\n\t"
    "v_cmp_nge_f32 vcc, s64, v251\n\t"
    "s_cbranch_vccnz .Ld_stub14\n\t"
    ".Ld_back14:\n\t"
    "s_waitcnt lgkmcnt(14)\n\t"
    "v_mfma_f32_32x32x16_bf16 v[0:15], a[96:99], v[200:203], v[0:15]\n\t"
    "s_waitcnt vmcnt(9)\n\t"
    "ds_read_b128 a[0:3], v244 offset:0\n\t"
    "v_add_f32 v226, v226, v228\n\t"
    "v_add_f32 v227, v227, v229\n\t"
    "v_add_u32 v230, s54, v230\n\t"
    "v_add_u32 v231, s54, v231\n\t"
    "v_add_u32 v232, s54, v232\n\t"
    "v_mfma_f32_32x32x16_bf16 v[16:31], a[100:103], v[200:203], v[16:31]\n\t"
    "ds_read_b128 a[4:7], v245 offset:0\n\t"
    "v_add_u32 v233, s54, v233\n\t"
    "v_add_u32 v234, s54, v234\n\t"
    "v_mov_b32 v250, v226\n\t"
    "s_nop 1\n\t"
    "v_permlane32_swap_b32 v226, v250\n\t"
    "v_mfma_f32_32x32x16_bf16 v[32:47], a[104:107], v[200:203], v[32:47]\n\t"
    "ds_read_b128 a[8:11], v246 offset:0\n\t"
    "v_add_f32 v226, v226, v250\n\t"
    "v_rcp_f32 v242, v226\n\t"
    "v_mov_b32 v250, v227\n\t"
    "s_nop 1\n\t"
    "v_permlane32_swap_b32 v227, v250\n\t"
    "v_mfma_f32_32x32x16_bf16 v[48:63], a[108:111], v[200:203], v[48:63]\n\t"
    "ds_read_b128 a[12:15], v247 offset:0\n\t"
    "v_add_f32 v227, v227, v250\n\t"
    "v_rcp_f32 v243, v227\n\t"
    "v_accvgpr_mov_b32 a48, a208\n\t"
    "v_accvgpr_mov_b32 a49, a209\n\t"
    "s_waitcnt lgkmcnt(14)\n\t"
    "v_mfma_f32_32x32x16_bf16 v[64:79], a[112:115], v[200:203], v[64:79]\n\t"
    "ds_read_b128 a[16:19], v244 offset:4096\n\t"
    "v_accvgpr_mov_b32 a50, a210\n\t"
    "v_accvgpr_mov_b32 a51, a211\n\t"
    "v_accvgpr_mov_b32 a52, a212\n\t"
    "v_accvgpr_mov_b32 a53, a213\n\t"
    "v_accvgpr_mov_b32 a54, a214\n\t"
    "v_accvgpr_mov_b32 a55, a215\n\t"
    "v_mfma_f32_32x32x16_bf16 v[80:95], a[116:119], v[200:203], v[80:95]\n\t"
    "ds_read_b128 a[20:23], v245 offset:4096\n\t"
    "v_accvgpr_mov_b32 a56, a216\n\t"
    "s_waitcnt vmcnt(19) lgkmcnt(0)\n\t"
    "s_barrier\n\t"
    "v_mfma_f32_32x32x16_bf16 v[0:15], a[120:123], v[192:195], v[0:15]\n\t"
    "s_add_u32 m0, s49, 36864\n\t"
    "s_nop 0\n\t"
    "buffer_load_dwordx4 v238, s[20:23], s39 offen lds\n\t"
    "ds_read_b128 a[24:27], v246 offset:4096\n\t"
    "v_accvgpr_mov_b32 a57, a217\n\t"
    "v_accvgpr_mov_b32 a58, a218\n\t"
    "v_accvgpr_mov_b32 a59, a219\n\t"
    "v_mfma_f32_32x32x16_bf16 v[16:31], a[124:127], v[192:195], v[16:31]\n\t"
    "ds_read_b128 a[28:31], v247 offset:4096\n\t"
    "v_accvgpr_mov_b32 a60, a220\n\t"
    "v_accvgpr_mov_b32 a61, a221\n\t"
    "v_accvgpr_mov_b32 a62, a222\n\t"
    "v_accvgpr_mov_b32 a63, a223\n\t"
    "v_accvgpr_mov_b32 a64, a224\n\t"
    "v_mfma_f32_32x32x16_bf16 v[32:47], a[128:131], v[192:195], v[32:47]\n\t"
    "buffer_load_dwordx4 v239, s[20:23], s39 offen offset:1024 lds\n\t"
    "ds_read_b128 a[32:35], v244 offset:8192\n\t"
    "v_accvgpr_mov_b32 a65, a225\n\t"
    "v_accvgpr_mov_b32 a66, a226\n\t"
    "v_accvgpr_mov_b32 a67, a227\n\t"
    "v_accvgpr_mov_b32 a68, a228\n\t"
    "v_accvgpr_mov_b32 a69, a229\n\t"
    "v_mfma_f32_32x32x16_bf16 v[48:63], a[132:135], v[192:195], v[48:63]\n\t"
    "ds_read_b128 a[36:39], v245 offset:8192\n\t"
    "v_accvgpr_mov_b32 a70, a230\n\t"
    "v_accvgpr_mov_b32 a71, a231\n\t"
    "v_accvgpr_mov_b32 a72, a232\n\t"
    "v_accvgpr_mov_b32 a73, a233\n\t"
    "v_accvgpr_mov_b32 a74, a234\n\t"
    "v_mfma_f32_32x32x16_bf16 v[64:79], a[136:139], v[192:195], v[64:79]\n\t"
    "buffer_load_dwordx4 v240, s[20:23], s39 offen offset:2048 lds\n\t"
    "ds_read_b128 a[40:43], v246 offset:8192\n\t"
    "v_accvgpr_mov_b32 a75, a235\n\t"
    "v_accvgpr_mov_b32 a76, a236\n\t"
    "v_accvgpr_mov_b32 a77, a237\n\t"
    "v_accvgpr_mov_b32 a78, a238\n\t"
    "v_accvgpr_mov_b32 a79, a239\n\t"
    "v_mfma_f32_32x32x16_bf16 v[80:95], a[140:143], v[192:195], v[80:95]\n\t"
    "ds_read_b128 a[44:47], v247 offset:8192\n\t"
    "v_accvgpr_mov_b32 a80, a240\n\t"
    "v_accvgpr_mov_b32 a81, a241\n\t"
    "v_accvgpr_mov_b32 a82, a242\n\t"
    "v_accvgpr_mov_b32 a83, a243\n\t"
    "v_accvgpr_mov_b32 a84, a244\n\t"
    "v_accvgpr_mov_b32 a85, a245\n\t"
    "v_mfma_f32_32x32x16_bf16 v[96:111], a[96:99], v[204:207], v[96:111]\n\t"
    "ds_read_b128 a[96:99], v230 offset:0\n\t"
    "v_accvgpr_mov_b32 a86, a246\n\t"
    "v_accvgpr_mov_b32 a87, a247\n\t"
    "v_accvgpr_mov_b32 a88, a248\n\t"
    "v_accvgpr_mov_b32 a89, a249\n\t"
    "v_accvgpr_mov_b32 a90, a250\n\t"
    "v_mfma_f32_32x32x16_bf16 v[112:127], a[100:103], v[204:207], v[112:127]\n\t"
    "ds_read_b128 a[100:103], v231 offset:0\n\t"
    "v_accvgpr_mov_b32 a91, a251\n\t"
    "v_accvgpr_mov_b32 a92, a252\n\t"
    "v_accvgpr_mov_b32 a93, a253\n\t"
    "v_accvgpr_mov_b32 a94, a254\n\t"
    "v_accvgpr_mov_b32 a95, a255\n\t"
    "v_mfma_f32_32x32x16_bf16 a[144:159], a[104:107], v[204:207], a[144:159]\n\t"
    "ds_read_b128 a[104:107], v232 offset:0\n\t"
    "s_add_u32 s28, s84, s75\n\t"
    "s_addc_u32 s29, s85, 0\n\t"
    "s_mov_b32 s30, s78\n\t"
    "v_mul_f32 v0, v242, v0\n\t"
    "v_mul_f32 v1, v242, v1\n\t"
    "v_mul_f32 v2, v242, v2\n\t"
    "v_mfma_f32_32x32x16_bf16 a[160:175], a[108:111], v[204:207], a[160:175]\n\t"
    "ds_read_b128 a[108:111], v233 offset:0\n\t"
    "v_mul_f32 v3, v242, v3\n\t"
    "v_mul_f32 v4, v242, v4\n\t"
    "v_mul_f32 v5, v242, v5\n\t"
    "v_mul_f32 v6, v242, v6\n\t"
    "v_mul_f32 v7, v242, v7\n\t"
    "v_cvt_pk_bf16_f32 v0, v0, v1\n\t"
    "v_mfma_f32_32x32x16_bf16 a[176:191], a[112:115], v[204:207], a[176:191]\n\t"
    "ds_read_b128 a[112:115], v230 offset:1024\n\t"
    "v_cvt_pk_bf16_f32 v1, v2, v3\n\t"
    "v_cvt_pk_bf16_f32 v2, v4, v5\n\t"
    "v_cvt_pk_bf16_f32 v3, v6, v7\n\t"
    "ds_write_b128 v244, v[0:3] offset:0\n\t"
    "v_mul_f32 v8, v242, v8\n\t"
    "v_mfma_f32_32x32x16_bf16 a[192:207], a[116:119], v[204:207], a[192:207]\n\t"
    "ds_read_b128 a[116:119], v231 offset:1024\n\t"
    "v_mul_f32 v9, v242, v9\n\t"
    "v_mul_f32 v10, v242, v10\n\t"
    "v_mul_f32 v11, v242, v11\n\t"
    "v_mul_f32 v12, v242, v12\n\t"
    "v_mul_f32 v13, v242, v13\n\t"
    "v_mul_f32 v14, v242, v14\n\t"
    "v_mfma_f32_32x32x16_bf16 v[96:111], a[120:123], v[196:199], v[96:111]\n\t"
    "ds_read_b128 a[120:123], v232 offset:1024\n\t"
    "v_mul_f32 v15, v242, v15\n\t"
    "v_cvt_pk_bf16_f32 v8, v8, v9\n\t"
    "v_cvt_pk_bf16_f32 v9, v10, v11\n\t"
    "v_cvt_pk_bf16_f32 v10, v12, v13\n\t"
    "v_cvt_pk_bf16_f32 v11, v14, v15\n\t"
    "v_mfma_f32_32x32x16_bf16 v[112:127], a[124:127], v[196:199], v[112:127]\n\t"
    "ds_read_b128 a[124:127], v233 offset:1024\n\t"
    "ds_write_b128 v245, v[8:11] offset:0\n\t"
    "v_mul_f32 v16, v242, v16\n\t"
    "v_mul_f32 v17, v242, v17\n\t"
    "v_mul_f32 v18, v242, v18\n\t"
    "v_mul_f32 v19, v242, v19\n\t"
    "v_mul_f32 v20, v242, v20\n\t"
    "v_mfma_f32_32x32x16_bf16 a[144:159], a[128:131], v[196:199], a[144:159]\n\t"
    "ds_read_b128 a[128:131], v230 offset:2048\n\t"
    "v_mul_f32 v21, v242, v21\n\t"
    "v_mul_f32 v22, v242, v22\n\t"
    "v_mul_f32 v23, v242, v23\n\t"
    "v_cvt_pk_bf16_f32 v16, v16, v17\n\t"
    "v_cvt_pk_bf16_f32 v17, v18, v19\n\t"
    "v_cvt_pk_bf16_f32 v18, v20, v21\n\t"
    "v_mfma_f32_32x32x16_bf16 a[160:175], a[132:135], v[196:199], a[160:175]\n\t"
    "ds_read_b128 a[132:135], v231 offset:2048\n\t"
    "v_cvt_pk_bf16_f32 v19, v22, v23\n\t"
    "ds_write_b128 v246, v[16:19] offset:0\n\t"
    "v_mul_f32 v24, v242, v24\n\t"
    "v_mul_f32 v25, v242, v25\n\t"
    "v_mul_f32 v26, v242, v26\n\t"
    "v_mfma_f32_32x32x16_bf16 a[176:191], a[136:139], v[196:199], a[176:191]\n\t"
    "ds_read_b128 a[136:139], v232 offset:2048\n\t"
    "v_mul_f32 v27, v242, v27\n\t"
    "v_mul_f32 v28, v242, v28\n\t"
    "v_mul_f32 v29, v242, v29\n\t"
    "v_mul_f32 v30, v242, v30\n\t"
    "v_mul_f32 v31, v242, v31\n\t"
    "v_cvt_pk_bf16_f32 v24, v24, v25\n\t"
    "v_mfma_f32_32x32x16_bf16 a[192:207], a[140:143], v[196:199], a[192:207]\n\t"
    "ds_read_b128 a[140:143], v233 offset:2048\n\t"
    "v_cvt_pk_bf16_f32 v25, v26, v27\n\t"
    "v_cvt_pk_bf16_f32 v26, v28, v29\n\t"
    "v_cvt_pk_bf16_f32 v27, v30, v31\n\t"
    "ds_write_b128 v247, v[24:27] offset:0\n\t"
    "s_mov_b32 s66, s67\n\t"
    "s_mov_b32 s67, s68\n\t"
    "s_mov_b32 s68, 0\n\t"
    "s_mov_b32 s69, s70\n\t"
    "s_mov_b32 s70, s71\n\t"
    "s_mov_b32 s71, 0\n\t"
    "s_mov_b32 s72, s73\n\t"
    "s_mov_b32 s73, s74\n\t"
    "s_mov_b32 s74, 0\n\t"
    "s_mov_b32 s75, s76\n\t"
    "s_mov_b32 s76, s77\n\t"
    "s_mov_b32 s77, 0\n\t"
    "s_mov_b32 s78, s79\n\t"
    "s_mov_b32 s79, s80\n\t"
    "s_mov_b32 s80, 0\n\t"
    "s_cmp_eq_u32 s72, 24576\n\t"
    "s_cbranch_scc1 .Ld_u0\n\t"
    "s_cmp_eq_u32 s72, 0\n\t"
    "s_cbranch_scc1 .Ld_final\n\t"
    ".Ld_u1:\n\t"
    ".Ld_u1x:\n\t"
    "s_nop 3\n\t"
    ".Ld_u1c:\n\t"
    "s_add_u32 s32, s66, 24576\n\t"
    "s_add_u32 s33, s69, 24576\n\t"
    "s_add_u32 s34, s66, 24576\n\t"
    "s_add_u32 s35, s69, 24576\n\t"
    "s_mov_b32 s36, s67\n\t"
    "s_mov_b32 s37, s70\n\t"
    "s_add_u32 s38, s67, s73\n\t"
    "s_add_u32 s39, s70, s73\n\t"
    "s_cmp_eq_u32 s73, 12288\n\t"
    "s_cselect_b32 s54, s81, 0\n\t"
    "s_sub_u32 s54, s54, s81\n\t"
    "s_cmp_lg_u32 s73, 0\n\t"
    "s_cselect_b32 s42, 100665344, 0\n\t"
    "s_add_u32 s24, s82, s76\n\t"
    "s_addc_u32 s25, s83, 0\n\t"
    "s_mov_b32 s26, s79\n\t"
    "s_waitcnt lgkmcnt(14)\n\t"
    "v_mfma_f32_32x32x16_bf16 v[128:143], a[96:99], a[0:3], 0\n\t"
    "v_mul_f32 v32, v242, v32\n\t"
    "v_mul_f32 v33, v242, v33\n\t"
    "v_mul_f32 v34, v242, v34\n\t"
    "v_mul_f32 v35, v242, v35\n\t"
    "v_mul_f32 v36, v242, v36\n\t"
    "v_mul_f32 v37, v242, v37\n\t"
    "v_mul_f32 v38, v242, v38\n\t"
    "v_mul_f32 v39, v242, v39\n\t"
    "v_cvt_pk_bf16_f32 v32, v32, v33\n\t"
    "s_waitcnt lgkmcnt(13)\n\t"
    "v_mfma_f32_32x32x16_bf16 v[128:143], a[100:103], a[4:7], v[128:143]\n\t"
    "v_cvt_pk_bf16_f32 v33, v34, v35\n\t"
    "v_cvt_pk_bf16_f32 v34, v36, v37\n\t"
    "v_cvt_pk_bf16_f32 v35, v38, v39\n\t"
    "ds_write_b128 v244, v[32:35] offset:4096\n\t"
    "v_mul_f32 v40, v242, v40\n\t"
    "v_mul_f32 v41, v242, v41\n\t"
    "v_mul_f32 v42, v242, v42\n\t"
    "v_mul_f32 v43, v242, v43\n\t"
    "v_mul_f32 v44, v242, v44\n\t"
    "v_mul_f32 v45, v242, v45\n\t"
    "s_waitcnt lgkmcnt(13)\n\t"
    "v_mfma_f32_32x32x16_bf16 v[128:143], a[104:107], a[8:11], v[128:143]\n\t"
    "v_mul_f32 v46, v242, v46\n\t"
    "v_mul_f32 v47, v242, v47\n\t"
    "v_cvt_pk_bf16_f32 v40, v40, v41\n\t"
    "v_cvt_pk_bf16_f32 v41, v42, v43\n\t"
    "v_cvt_pk_bf16_f32 v42, v44, v45\n\t"
    "v_cvt_pk_bf16_f32 v43, v46, v47\n\t"
    "ds_write_b128 v245, v[40:43] offset:4096\n\t"
    "v_mul_f32 v48, v242, v48\n\t"
    "v_mul_f32 v49, v242, v49\n\t"
    "s_waitcnt lgkmcnt(13)\n\t"
    "v_mfma_f32_32x32x16_bf16 v[128:143], a[108:111], a[12:15], v[128:143]\n\t"
    "v_mul_f32 v50, v242, v50\n\t"
    "v_mul_f32 v51, v242, v51\n\t"
    "v_mul_f32 v52, v242, v52\n\t"
    "v_mul_f32 v53, v242, v53\n\t"
    "v_mul_f32 v54, v242, v54\n\t"
    "v_mul_f32 v55, v242, v55\n\t"
    "s_waitcnt vmcnt(6) lgkmcnt(0)\n\t"
    "s_barrier\n\t"
    "v_mfma_f32_32x32x16_bf16 v[128:143], a[112:115], a[16:19], v[128:143]\n\t"
    "s_mov_b32 m0, s46\n\t"
    "s_nop 0\n\t"
    "buffer_load_dwordx4 v235, s[16:19], s32 offen lds\n\t"
    "v_cvt_pk_bf16_f32 v48, v48, v49\n\t"
    "v_cvt_pk_bf16_f32 v49, v50, v51\n\t"
    "v_cvt_pk_bf16_f32 v50, v52, v53\n\t"
    "v_cvt_pk_bf16_f32 v51, v54, v55\n\t"
    "ds_write_b128 v246, v[48:51] offset:4096\n\t"
    "v_mul_f32 v56, v242, v56\n\t"
    "v_mfma_f32_32x32x16_bf16 v[128:143], a[116:119], a[20:23], v[128:143]\n\t"
    "v_mul_f32 v57, v242, v57\n\t"
    "v_mul_f32 v58, v242, v58\n\t"
    "v_mul_f32 v59, v242, v59\n\t"
    "v_mul_f32 v60, v242, v60\n\t"
    "v_mul_f32 v61, v242, v61\n\t"
    "v_mul_f32 v62, v242, v62\n\t"
    "v_mul_f32 v63, v242, v63\n\t"
    "v_cvt_pk_bf16_f32 v56, v56, v57\n\t"
    "v_cvt_pk_bf16_f32 v57, v58, v59\n\t"
    "v_cvt_pk_bf16_f32 v58, v60, v61\n\t"
    "v_mfma_f32_32x32x16_bf16 v[128:143], a[120:123], a[24:27], v[128:143]\n\t"
    "buffer_load_dwordx4 v236, s[16:19], s32 offen offset:1024 lds\n\t"
    "v_cvt_pk_bf16_f32 v59, v62, v63\n\t"
    "ds_write_b128 v247, v[56:59] offset:4096\n\t"
    "v_mul_f32 v64, v242, v64\n\t"
    "v_mul_f32 v65, v242, v65\n\t"
    "v_mul_f32 v66, v242, v66\n\t"
    "v_mul_f32 v67, v242, v67\n\t"
    "v_mul_f32 v68, v242, v68\n\t"
    "v_mul_f32 v69, v242, v69\n\t"
    "v_mfma_f32_32x32x16_bf16 v[128:143], a[124:127], a[28:31], v[128:143]\n\t"
    "v_mul_f32 v70, v242, v70\n\t"
    "v_mul_f32 v71, v242, v71\n\t"
    "v_cvt_pk_bf16_f32 v64, v64, v65\n\t"
    "v_cvt_pk_bf16_f32 v65, v66, v67\n\t"
    "v_cvt_pk_bf16_f32 v66, v68, v69\n\t"
    "v_cvt_pk_bf16_f32 v67, v70, v71\n\t"
    "ds_write_b128 v244, v[64:67] offset:8192\n\t"
    "v_mul_f32 v72, v242, v72\n\t"
    "v_mul_f32 v73, v242, v73\n\t"
    "v_mul_f32 v74, v242, v74\n\t"
    "v_mfma_f32_32x32x16_bf16 v[128:143], a[128:131], a[32:35], v[128:143]\n\t"
    "buffer_load_dwordx4 v237, s[16:19], s32 offen offset:2048 lds\n\t"
    "s_add_u32 s32, s32, 12288\n\t"
    "v_mul_f32 v75, v242, v75\n\t"
    "v_mul_f32 v76, v242, v76\n\t"
    "v_mul_f32 v77, v242, v77\n\t"
    "v_mul_f32 v78, v242, v78\n\t"
    "v_mul_f32 v79, v242, v79\n\t"
    "v_cvt_pk_bf16_f32 v72, v72, v73\n\t"
    "v_cvt_pk_bf16_f32 v73, v74, v75\n\t"
    "v_mfma_f32_32x32x16_bf16 v[128:143], a[132:135], a[36:39], v[128:143]\n\t"
    "v_cvt_pk_bf16_f32 v74, v76, v77\n\t"
    "v_cvt_pk_bf16_f32 v75, v78, v79\n\t"
    "ds_write_b128 v245, v[72:75] offset:8192\n\t"
    "v_mul_f32 v80, v242, v80\n\t"
    "v_mul_f32 v81, v242, v81\n\t"
    "v_mul_f32 v82, v242, v82\n\t"
    "v_mul_f32 v83, v242, v83\n\t"
    "v_mul_f32 v84, v242, v84\n\t"
    "v_mul_f32 v85, v242, v85\n\t"
    "v_mul_f32 v86, v242, v86\n\t"
    "v_mfma_f32_32x32x16_bf16 v[128:143], a[136:139], a[40:43], v[128:143]\n\t"
    "s_add_u32 m0, s46, 12288\n\t"
    "s_nop 0\n\t"
    "buffer_load_dwordx4 v235, s[16:19], s33 offen lds\n\t"
    "v_mul_f32 v87, v242, v87\n\t"
    "v_cvt_pk_bf16_f32 v80, v80, v81\n\t"
    "v_cvt_pk_bf16_f32 v81, v82, v83\n\t"
    "v_cvt_pk_bf16_f32 v82, v84, v85\n\t"
    "v_cvt_pk_bf16_f32 v83, v86, v87\n\t"
    "ds_write_b128 v246, v[80:83] offset:8192\n\t"
    "v_mfma_f32_32x32x16_bf16 v[128:143], a[140:143], a[44:47], v[128:143]\n\t"
    "v_mul_f32 v88, v242, v88\n\t"
    "v_mul_f32 v89, v242, v89\n\t"
    "v_mul_f32 v90, v242, v90\n\t"
    "v_mul_f32 v91, v242, v91\n\t"
    "v_mul_f32 v92, v242, v92\n\t"
    "v_mul_f32 v93, v242, v93\n\t"
    "v_mul_f32 v94, v242, v94\n\t"
    "v_mul_f32 v95, v242, v95\n\t"
    "v_cvt_pk_bf16_f32 v88, v88, v89\n\t"
    "v_mfma_f32_32x32x16_bf16 v[144:159], a[96:99], a[48:51], 0\n\t"
    "ds_read_b128 a[96:99], v230 offset:24576\n\t"
    "buffer_load_dwordx4 v236, s[16:19], s33 offen offset:1024 lds\n\t"
    "v_cvt_pk_bf16_f32 v89, v90, v91\n\t"
    "v_cvt_pk_bf16_f32 v90, v92, v93\n\t"
    "v_cvt_pk_bf16_f32 v91, v94, v95\n\t"
    "ds_write_b128 v247, v[88:91] offset:8192\n\t"
    "v_mul_f32 v96, v243, v96\n\t"
    "v_mul_f32 v97, v243, v97\n\t"
    "v_mul_f32 v98, v243, v98\n\t"
    "v_mul_f32 v99, v243, v99\n\t"
    "v_mfma_f32_32x32x16_bf16 v[144:159], a[100:103], a[52:55], v[144:159]\n\t"
    "ds_read_b128 a[100:103], v231 offset:24576\n\t"
    "v_mul_f32 v100, v243, v100\n\t"
    "v_mul_f32 v101, v243, v101\n\t"
    "v_mul_f32 v102, v243, v102\n\t"
    "v_mul_f32 v103, v243, v103\n\t"
    "v_cvt_pk_bf16_f32 v96, v96, v97\n\t"
    "v_cvt_pk_bf16_f32 v97, v98, v99\n\t"
    "v_cvt_pk_bf16_f32 v98, v100, v101\n\t"
    "v_cvt_pk_bf16_f32 v99, v102, v103\n\t"
    "v_mfma_f32_32x32x16_bf16 v[144:159], a[104:107], a[56:59], v[144:159]\n\t"
    "ds_read_b128 a[104:107], v232 offset:24576\n\t"
    "buffer_load_dwordx4 v237, s[16:19], s33 offen offset:2048 lds\n\t"
    "s_add_u32 s33, s33, 12288\n\t"
    "v_accvgpr_write_b32 a208, v96\n\t"
    "v_accvgpr_write_b32 a209, v97\n\t"
    "v_accvgpr_write_b32 a210, v98\n\t"
    "v_accvgpr_write_b32 a211, v99\n\t"
    "v_mul_f32 v104, v243, v104\n\t"
    "v_mul_f32 v105, v243, v105\n\t"
    "v_mul_f32 v106, v243, v106\n\t"
    "v_mfma_f32_32x32x16_bf16 v[144:159], a[108:111], a[60:63], v[144:159]\n\t"
    "ds_read_b128 a[108:111], v233 offset:24576\n\t"
    "v_mul_f32 v107, v243, v107\n\t"
    "v_mul_f32 v108, v243, v108\n\t"
    "v_mul_f32 v109, v243, v109\n\t"
    "v_mul_f32 v110, v243, v110\n\t"
    "v_mul_f32 v111, v243, v111\n\t"
    "v_cvt_pk_bf16_f32 v104, v104, v105\n\t"
    "v_cvt_pk_bf16_f32 v105, v106, v107\n\t"
    "v_cvt_pk_bf16_f32 v106, v108, v109\n\t"
    "v_mfma_f32_32x32x16_bf16 v[144:159], a[112:115], a[64:67], v[144:159]\n\t"
    "ds_read_b128 a[112:115], v230 offset:25600\n\t"
    "v_cvt_pk_bf16_f32 v107, v110, v111\n\t"
    "v_accvgpr_write_b32 a212, v104\n\t"
    "v_accvgpr_write_b32 a213, v105\n\t"
    "v_accvgpr_write_b32 a214, v106\n\t"
    "v_accvgpr_write_b32 a215, v107\n\t"
    "v_mul_f32 v112, v243, v112\n\t"
    "v_mul_f32 v113, v243, v113\n\t"
    "v_mul_f32 v114, v243, v114\n\t"
    "v_mul_f32 v115, v243, v115\n\t"
    "v_mfma_f32_32x32x16_bf16 v[144:159], a[116:119], a[68:71], v[144:159]\n\t"
    "ds_read_b128 a[116:119], v231 offset:25600\n\t"
    "v_mul_f32 v116, v243, v116\n\t"
    "v_mul_f32 v117, v243, v117\n\t"
    "v_mul_f32 v118, v243, v118\n\t"
    "v_mul_f32 v119, v243, v119\n\t"
    "v_cvt_pk_bf16_f32 v112, v112, v113\n\t"
    "v_cvt_pk_bf16_f32 v113, v114, v115\n\t"
    "v_cvt_pk_bf16_f32 v114, v116, v117\n\t"
    "v_cvt_pk_bf16_f32 v115, v118, v119\n\t"
    "v_mfma_f32_32x32x16_bf16 v[144:159], a[120:123], a[72:75], v[144:159]\n\t"
    "ds_read_b128 a[120:123], v232 offset:25600\n\t"
    "v_accvgpr_write_b32 a216, v112\n\t"
    "v_accvgpr_write_b32 a217, v113\n\t"
    "v_accvgpr_write_b32 a218, v114\n\t"
    "v_accvgpr_write_b32 a219, v115\n\t"
    "v_mul_f32 v120, v243, v120\n\t"
    "v_mul_f32 v121, v243, v121\n\t"
    "v_mul_f32 v122, v243, v122\n\t"
    "v_mul_f32 v123, v243, v123\n\t"
    "v_mul_f32 v124, v243, v124\n\t"
    "v_mfma_f32_32x32x16_bf16 v[144:159], a[124:127], a[76:79], v[144:159]\n\t"
    "ds_read_b128 a[124:127], v233 offset:25600\n\t"
    "v_mul_f32 v125, v243, v125\n\t"
    "v_mul_f32 v126, v243, v126\n\t"
    "v_mul_f32 v127, v243, v127\n\t"
    "v_cvt_pk_bf16_f32 v120, v120, v121\n\t"
    "v_cvt_pk_bf16_f32 v121, v122, v123\n\t"
    "v_cvt_pk_bf16_f32 v122, v124, v125\n\t"
    "v_cvt_pk_bf16_f32 v123, v126, v127\n\t"
    "v_accvgpr_write_b32 a220, v120\n\t"
    "v_mfma_f32_32x32x16_bf16 v[144:159], a[128:131], a[80:83], v[144:159]\n\t"
    "ds_read_b128 a[128:131], v230 offset:26624\n\t"
    "v_accvgpr_write_b32 a221, v121\n\t"
    "v_accvgpr_write_b32 a222, v122\n\t"
    "v_accvgpr_write_b32 a223, v123\n\t"
    "v_accvgpr_read_b32 v0, a144\n\t"
    "v_accvgpr_read_b32 v1, a145\n\t"
    "v_accvgpr_read_b32 v2, a146\n\t"
    "v_accvgpr_read_b32 v3, a147\n\t"
    "v_accvgpr_read_b32 v4, a148\n\t"
    "v_mfma_f32_32x32x16_bf16 v[144:159], a[132:135], a[84:87], v[144:159]\n\t"
    "ds_read_b128 a[132:135], v231 offset:26624\n\t"
    "v_accvgpr_read_b32 v5, a149\n\t"
    "v_accvgpr_read_b32 v6, a150\n\t"
    "v_accvgpr_read_b32 v7, a151\n\t"
    "v_mul_f32 v0, v243, v0\n\t"
    "v_mul_f32 v1, v243, v1\n\t"
    "v_mul_f32 v2, v243, v2\n\t"
    "v_mul_f32 v3, v243, v3\n\t"
    "v_mul_f32 v4, v243, v4\n\t"
    "v_mul_f32 v5, v243, v5\n\t"
    "v_mfma_f32_32x32x16_bf16 v[144:159], a[136:139], a[88:91], v[144:159]\n\t"
    "ds_read_b128 a[136:139], v232 offset:26624\n\t"
    "v_mul_f32 v6, v243, v6\n\t"
    "v_mul_f32 v7, v243, v7\n\t"
    "v_cvt_pk_bf16_f32 v0, v0, v1\n\t"
    "v_cvt_pk_bf16_f32 v1, v2, v3\n\t"
    "v_cvt_pk_bf16_f32 v2, v4, v5\n\t"
    "v_cvt_pk_bf16_f32 v3, v6, v7\n\t"
    "v_accvgpr_write_b32 a224, v0\n\t"
    "v_accvgpr_write_b32 a225, v1\n\t"
    "v_mfma_f32_32x32x16_bf16 v[144:159], a[140:143], a[92:95], v[144:159]\n\t"
    "ds_read_b128 a[140:143], v233 offset:26624\n\t"
    "v_accvgpr_write_b32 a226, v2\n\t"
    "v_accvgpr_write_b32 a227, v3\n\t"
    "v_accvgpr_read_b32 v8, a152\n\t"
    "v_accvgpr_read_b32 v9, a153\n\t"
    "v_accvgpr_read_b32 v10, a154\n\t"
    "v_accvgpr_read_b32 v11, a155\n\t"
    "v_accvgpr_read_b32 v12, a156\n\t"
    "v_accvgpr_read_b32 v13, a157\n\t"
    "s_waitcnt lgkmcnt(10)\n\t"
    "v_mfma_f32_32x32x16_bf16 v[160:175], a[96:99], a[0:3], 0\n\t"
    "v_max3_f32 v250, v128, v129, v130\n\t"
    "v_accvgpr_read_b32 v14, a158\n\t"
    "v_accvgpr_read_b32 v15, a159\n\t"
    "v_mul_f32 v8, v243, v8\n\t"
    "v_max3_f32 v251, v131, v132, v133\n\t"
    "v_mul_f32 v9, v243, v9\n\t"
    "v_mul_f32 v10, v243, v10\n\t"
    "v_max3_f32 v250, v250, v134, v135\n\t"
    "v_mul_f32 v11, v243, v11\n\t"
    "v_mul_f32 v12, v243, v12\n\t"
    "v_max3_f32 v251, v251, v136, v137\n\t"
    "v_mul_f32 v13, v243, v13\n\t"
    "v_mul_f32 v14, v243, v14\n\t"
    "v_mfma_f32_32x32x16_bf16 v[176:191], a[96:99], a[48:51], 0\n\t"
    "v_max3_f32 v250, v250, v138, v139\n\t"
    "v_mul_f32 v15, v243, v15\n\t"
    "v_cvt_pk_bf16_f32 v8, v8, v9\n\t"
    "v_max3_f32 v251, v251, v140, v141\n\t"
    "v_cvt_pk_bf16_f32 v9, v10, v11\n\t"
    "v_cvt_pk_bf16_f32 v10, v12, v13\n\t"
    "v_max3_f32 v250, v250, v142, v143\n\t"
    "v_cvt_pk_bf16_f32 v11, v14, v15\n\t"
    "v_accvgpr_write_b32 a228, v8\n\t"
    "v_max_f32 v250, v250, v251\n\t"
    "v_accvgpr_write_b32 a229, v9\n\t"
    "v_accvgpr_write_b32 a230, v10\n\t"
    "s_waitcnt lgkmcnt(9)\n\t"
    "v_mfma_f32_32x32x16_bf16 v[160:175], a[100:103], a[4:7], v[160:175]\n\t"
    "v_mov_b32 v251, v250\n\t"
    "s_nop 1\n\t"
    "v_permlane32_swap_b32 v250, v251\n\t"
    "v_accvgpr_write_b32 a231, v11\n\t"
    "v_accvgpr_read_b32 v16, a160\n\t"
    "v_max_f32 v250, v250, v251\n\t"
    "v_accvgpr_read_b32 v17, a161\n\t"
    "v_accvgpr_read_b32 v18, a162\n\t"
    "v_mul_f32_e64 v224, s63, -v250\n\t"
    "v_accvgpr_read_b32 v19, a163\n\t"
    "v_accvgpr_read_b32 v20, a164\n\t"
    "v_fma_f32 v216, v128, s63, v224\n\t"
    "v_accvgpr_read_b32 v21, a165\n\t"
    "v_accvgpr_read_b32 v22, a166\n\t"
    "v_mfma_f32_32x32x16_bf16 v[176:191], a[100:103], a[52:55], v[176:191]\n\t"
    "v_fma_f32 v217, v129, s63, v224\n\t"
    "v_accvgpr_read_b32 v23, a167\n\t"
    "v_mul_f32 v16, v243, v16\n\t"
    "v_exp_f32 v216, v216\n\t"
    "v_mul_f32 v17, v243, v17\n\t"
    "v_mul_f32 v18, v243, v18\n\t"
    "v_mul_f32 v19, v243, v19\n\t"
    "s_waitcnt vmcnt(0) lgkmcnt(0)\n\t"
    "s_barrier\n\t"
    "v_mfma_f32_32x32x16_bf16 v[160:175], a[104:107], a[8:11], v[160:175]\n\t"
    "ds_read_b64_tr_b16 a[96:97], v234 offset:0\n\t"
    "ds_read_b64_tr_b16 a[98:99], v234 offset:3072\n\t"
    "s_add_u32 m0, s46, 24576\n\t"
    "s_nop 0\n\t"
    "buffer_load_dwordx4 v235, s[16:19], s32 offen lds\n\t"
    "v_exp_f32 v217, v217\n\t"
    "v_mul_f32 v20, v243, v20\n\t"
    "v_mul_f32 v21, v243, v21\n\t"
    "v_fma_f32 v218, v130, s63, v224\n\t"
    "v_mul_f32 v22, v243, v22\n\t"
    "v_mul_f32 v23, v243, v23\n\t"
    "v_fma_f32 v219, v131, s63, v224\n\t"
    "v_mfma_f32_32x32x16_bf16 v[176:191], a[104:107], a[56:59], v[176:191]\n\t"
    "ds_read_b64_tr_b16 a[100:101], v234 offset:512\n\t"
    "ds_read_b64_tr_b16 a[102:103], v234 offset:3584\n\t"
    "v_cvt_pk_bf16_f32 v16, v16, v17\n\t"
    "v_cvt_pk_bf16_f32 v17, v18, v19\n\t"
    "v_exp_f32 v218, v218\n\t"
    "v_cvt_pk_bf16_f32 v18, v20, v21\n\t"
    "v_cvt_pk_bf16_f32 v19, v22, v23\n\t"
    "v_exp_f32 v219, v219\n\t"
    "v_accvgpr_write_b32 a232, v16\n\t"
    "v_accvgpr_write_b32 a233, v17\n\t"
    "v_cvt_pk_bf16_f32 v192, v216, v217\n\t"
    "v_mfma_f32_32x32x16_bf16 v[160:175], a[108:111], a[12:15], v[160:175]\n\t"
    "ds_read_b64_tr_b16 a[104:105], v234 offset:1024\n\t"
    "ds_read_b64_tr_b16 a[106:107], v234 offset:4096\n\t"
    "buffer_load_dwordx4 v236, s[16:19], s32 offen offset:1024 lds\n\t"
    "v_accvgpr_write_b32 a234, v18\n\t"
    "v_accvgpr_write_b32 a235, v19\n\t"
    "v_add_f32 v228, v216, v217\n\t"
    "v_accvgpr_read_b32 v24, a168\n\t"
    "v_accvgpr_read_b32 v25, a169\n\t"
    "v_fma_f32 v216, v132, s63, v224\n\t"
    "v_accvgpr_read_b32 v26, a170\n\t"
    "v_accvgpr_read_b32 v27, a171\n\t"
    "v_fma_f32 v217, v133, s63, v224\n\t"
    "v_accvgpr_read_b32 v28, a172\n\t"
    "v_mfma_f32_32x32x16_bf16 v[176:191], a[108:111], a[60:63], v[176:191]\n\t"
    "ds_read_b64_tr_b16 a[108:109], v234 offset:1536\n\t"
    "ds_read_b64_tr_b16 a[110:111], v234 offset:4608\n\t"
    "v_accvgpr_read_b32 v29, a173\n\t"
    "v_exp_f32 v216, v216\n\t"
    "v_accvgpr_read_b32 v30, a174\n\t"
    "v_accvgpr_read_b32 v31, a175\n\t"
    "v_exp_f32 v217, v217\n\t"
    "v_mul_f32 v24, v243, v24\n\t"
    "v_mul_f32 v25, v243, v25\n\t"
    "v_cvt_pk_bf16_f32 v193, v218, v219\n\t"
    "v_mul_f32 v26, v243, v26\n\t"
    "v_mfma_f32_32x32x16_bf16 v[160:175], a[112:115], a[16:19], v[160:175]\n\t"
    "buffer_load_dwordx4 v237, s[16:19], s32 offen offset:2048 lds\n\t"
    "s_add_u32 s32, s32, 12288\n\t"
    "v_mul_f32 v27, v243, v27\n\t"
    "v_add_f32 v228, v228, v218\n\t"
    "v_mul_f32 v28, v243, v28\n\t"
    "v_mul_f32 v29, v243, v29\n\t"
    "v_mul_f32 v30, v243, v30\n\t"
    "v_add_f32 v228, v228, v219\n\t"
    "v_mul_f32 v31, v243, v31\n\t"
    "v_cvt_pk_bf16_f32 v24, v24, v25\n\t"
    "v_fma_f32 v218, v134, s63, v224\n\t"
    "v_cvt_pk_bf16_f32 v25, v26, v27\n\t"
    "v_cvt_pk_bf16_f32 v26, v28, v29\n\t"
    "v_mfma_f32_32x32x16_bf16 v[176:191], a[112:115], a[64:67], v[176:191]\n\t"
    "ds_read_b64_tr_b16 a[112:113], v234 offset:2048\n\t"
    "ds_read_b64_tr_b16 a[114:115], v234 offset:5120\n\t"
    "v_fma_f32 v219, v135, s63, v224\n\t"
    "v_cvt_pk_bf16_f32 v27, v30, v31\n\t"
    "v_accvgpr_write_b32 a236, v24\n\t"
    "v_exp_f32 v218, v218\n\t"
    "v_accvgpr_write_b32 a237, v25\n\t"
    "v_accvgpr_write_b32 a238, v26\n\t"
    "v_exp_f32 v219, v219\n\t"
    "v_accvgpr_write_b32 a239, v27\n\t"
    "v_accvgpr_read_b32 v32, a176\n\t"
    "v_mfma_f32_32x32x16_bf16 v[160:175], a[116:119], a[20:23], v[160:175]\n\t"
    "s_add_u32 m0, s46, 36864\n\t"
    "s_nop 0\n\t"
    "buffer_load_dwordx4 v235, s[16:19], s33 offen lds\n\t"
    "v_cvt_pk_bf16_f32 v194, v216, v217\n\t"
    "v_accvgpr_read_b32 v33, a177\n\t"
    "v_accvgpr_read_b32 v34, a178\n\t"
    "v_add_f32 v228, v228, v216\n\t"
    "v_accvgpr_read_b32 v35, a179\n\t"
    "v_accvgpr_read_b32 v36, a180\n\t"
    "v_add_f32 v228, v228, v217\n\t"
    "v_accvgpr_read_b32 v37, a181\n\t"
    "v_accvgpr_read_b32 v38, a182\n\t"
    "v_mfma_f32_32x32x16_bf16 v[176:191], a[116:119], a[68:71], v[176:191]\n\t"
    "ds_read_b64_tr_b16 a[116:117], v234 offset:2560\n\t"
    "ds_read_b64_tr_b16 a[118:119], v234 offset:5632\n\t"
    "s_nop 0\n\t"
    "v_accvgpr_read_b32 v39, a183\n\t"
    "v_mul_f32 v32, v243, v32\n\t"
    "v_cvt_pk_bf16_f32 v195, v218, v219\n\t"
    "v_mul_f32 v33, v243, v33\n\t"
    "v_mul_f32 v34, v243, v34\n\t"
    "v_add_f32 v228, v228, v218\n\t"
    "v_mul_f32 v35, v243, v35\n\t"
    "v_mul_f32 v36, v243, v36\n\t"
    "v_add_f32 v228, v228, v219\n\t"
    "v_mul_f32 v37, v243, v37\n\t"
    "v_mfma_f32_32x32x16_bf16 v[160:175], a[120:123], a[24:27], v[160:175]\n\t"
    "buffer_load_dwordx4 v236, s[16:19], s33 offen offset:1024 lds\n\t"
    "v_mul_f32 v38, v243, v38\n\t"
    "v_max3_f32 v250, v144, v145, v146\n\t"
    "v_mul_f32 v39, v243, v39\n\t"
    "v_cvt_pk_bf16_f32 v32, v32, v33\n\t"
    "v_cvt_pk_bf16_f32 v33, v34, v35\n\t"
    "v_max3_f32 v251, v147, v148, v149\n\t"
    "v_cvt_pk_bf16_f32 v34, v36, v37\n\t"
    "v_cvt_pk_bf16_f32 v35, v38, v39\n\t"
    "v_max3_f32 v250, v250, v150, v151\n\t"
    "v_accvgpr_write_b32 a240, v32\n\t"
    "v_accvgpr_write_b32 a241, v33\n\t"
    "v_max3_f32 v251, v251, v152, v153\n\t"
    "v_mfma_f32_32x32x16_bf16 v[176:191], a[120:123], a[72:75], v[176:191]\n\t"
    "ds_read_b64_tr_b16 a[120:121], v234 offset:6144\n\t"
    "ds_read_b64_tr_b16 a[122:123], v234 offset:9216\n\t"
    "v_accvgpr_write_b32 a242, v34\n\t"
    "v_accvgpr_write_b32 a243, v35\n\t"
    "v_max3_f32 v250, v250, v154, v155\n\t"
    "v_accvgpr_read_b32 v40, a184\n\t"
    "v_accvgpr_read_b32 v41, a185\n\t"
    "v_max3_f32 v251, v251, v156, v157\n\t"
    "v_accvgpr_read_b32 v42, a186\n\t"
    "v_accvgpr_read_b32 v43, a187\n\t"
    "v_max3_f32 v250, v250, v158, v159\n\t"
    "v_accvgpr_read_b32 v44, a188\n\t"
    "v_accvgpr_read_b32 v45, a189\n\t"
    "v_mfma_f32_32x32x16_bf16 v[160:175], a[124:127], a[28:31], v[160:175]\n\t"
    "buffer_load_dwordx4 v237, s[16:19], s33 offen offset:2048 lds\n\t"
    "s_add_u32 s33, s33, 12288\n\t"
    "v_max_f32 v250, v250, v251\n\t"
    "v_accvgpr_read_b32 v46, a190\n\t"
    "v_accvgpr_read_b32 v47, a191\n\t"
    "v_mov_b32 v251, v250\n\t"
    "s_nop 1\n\t"
    "v_permlane32_swap_b32 v250, v251\n\t"
    "v_mul_f32 v40, v243, v40\n\t"
    "v_mul_f32 v41, v243, v41\n\t"
    "v_max_f32 v250, v250, v251\n\t"
    "v_mul_f32 v42, v243, v42\n\t"
    "v_mul_f32 v43, v243, v43\n\t"
    "v_mfma_f32_32x32x16_bf16 v[176:191], a[124:127], a[76:79], v[176:191]\n\t"
    "ds_read_b64_tr_b16 a[124:125], v234 offset:6656\n\t"
    "ds_read_b64_tr_b16 a[126:127], v234 offset:9728\n\t"
    "v_mul_f32_e64 v225, s63, -v250\n\t"
    "v_mul_f32 v44, v243, v44\n\t"
    "v_mul_f32 v45, v243, v45\n\t"
    "v_fma_f32 v216, v144, s63, v225\n\t"
    "v_mul_f32 v46, v243, v46\n\t"
    "v_mul_f32 v47, v243, v47\n\t"
    "v_fma_f32 v217, v145, s63, v225\n\t"
    "v_cvt_pk_bf16_f32 v40, v40, v41\n\t"
    "v_cvt_pk_bf16_f32 v41, v42, v43\n\t"
    "v_exp_f32 v216, v216\n\t"
    "v_mfma_f32_32x32x16_bf16 v[160:175], a[128:131], a[32:35], v[160:175]\n\t"
    "v_cvt_pk_bf16_f32 v42, v44, v45\n\t"
    "v_cvt_pk_bf16_f32 v43, v46, v47\n\t"
    "v_accvgpr_write_b32 a244, v40\n\t"
    "v_exp_f32 v217, v217\n\t"
    "v_accvgpr_write_b32 a245, v41\n\t"
    "v_accvgpr_write_b32 a246, v42\n\t"
    "v_fma_f32 v218, v146, s63, v225\n\t"
    "v_accvgpr_write_b32 a247, v43\n\t"
    "v_accvgpr_read_b32 v48, a192\n\t"
    "v_fma_f32 v219, v147, s63, v225\n\t"
    "v_accvgpr_read_b32 v49, a193\n\t"
    "v_mfma_f32_32x32x16_bf16 v[176:191], a[128:131], a[80:83], v[176:191]\n\t"
    "ds_read_b64_tr_b16 a[128:129], v234 offset:7168\n\t"
    "ds_read_b64_tr_b16 a[130:131], v234 offset:10240\n\t"
    "v_accvgpr_read_b32 v50, a194\n\t"
    "v_exp_f32 v218, v218\n\t"
    "v_accvgpr_read_b32 v51, a195\n\t"
    "v_accvgpr_read_b32 v52, a196\n\t"
    "v_exp_f32 v219, v219\n\t"
    "v_accvgpr_read_b32 v53, a197\n\t"
    "v_accvgpr_read_b32 v54, a198\n\t"
    "v_cvt_pk_bf16_f32 v196, v216, v217\n\t"
    "v_accvgpr_read_b32 v55, a199\n\t"
    "v_mfma_f32_32x32x16_bf16 v[160:175], a[132:135], a[36:39], v[160:175]\n\t"
    "v_mul_f32 v48, v243, v48\n\t"
    "v_add_f32 v229, v216, v217\n\t"
    "v_mul_f32 v49, v243, v49\n\t"
    "v_mul_f32 v50, v243, v50\n\t"
    "v_fma_f32 v216, v148, s63, v225\n\t"
    "v_mul_f32 v51, v243, v51\n\t"
    "v_mul_f32 v52, v243, v52\n\t"
    "v_fma_f32 v217, v149, s63, v225\n\t"
    "v_mul_f32 v53, v243, v53\n\t"
    "v_mul_f32 v54, v243, v54\n\t"
    "v_exp_f32 v216, v216\n\t"
    "v_mul_f32 v55, v243, v55\n\t"
    "v_mfma_f32_32x32x16_bf16 v[176:191], a[132:135], a[84:87], v[176:191]\n\t"
    "ds_read_b64_tr_b16 a[132:133], v234 offset:7680\n\t"
    "ds_read_b64_tr_b16 a[134:135], v234 offset:10752\n\t"
    "v_cvt_pk_bf16_f32 v48, v48, v49\n\t"
    "v_exp_f32 v217, v217\n\t"
    "v_cvt_pk_bf16_f32 v49, v50, v51\n\t"
    "v_cvt_pk_bf16_f32 v50, v52, v53\n\t"
    "v_cvt_pk_bf16_f32 v197, v218, v219\n\t"
    "v_cvt_pk_bf16_f32 v51, v54, v55\n\t"
    "v_accvgpr_write_b32 a248, v48\n\t"
    "v_add_f32 v229, v229, v218\n\t"
    "v_accvgpr_write_b32 a249, v49\n\t"
    "v_accvgpr_write_b32 a250, v50\n\t"
    "v_mfma_f32_32x32x16_bf16 v[160:175], a[136:139], a[40:43], v[160:175]\n\t"
    "v_accvgpr_write_b32 a251, v51\n\t"
    "v_add_f32 v229, v229, v219\n\t"
    "v_accvgpr_read_b32 v56, a200\n\t"
    "v_accvgpr_read_b32 v57, a201\n\t"
    "v_fma_f32 v218, v150, s63, v225\n\t"
    "v_accvgpr_read_b32 v58, a202\n\t"
    "v_accvgpr_read_b32 v59, a203\n\t"
    "v_fma_f32 v219, v151, s63, v225\n\t"
    "v_accvgpr_read_b32 v60, a204\n\t"
    "v_accvgpr_read_b32 v61, a205\n\t"
    "v_exp_f32 v218, v218\n\t"
    "v_accvgpr_read_b32 v62, a206\n\t"
    "v_mfma_f32_32x32x16_bf16 v[176:191], a[136:139], a[88:91], v[176:191]\n\t"
    "ds_read_b64_tr_b16 a[136:137], v234 offset:8192\n\t"
    "ds_read_b64_tr_b16 a[138:139], v234 offset:11264\n\t"
    "v_accvgpr_read_b32 v63, a207\n\t"
    "v_exp_f32 v219, v219\n\t"
    "v_mul_f32 v56, v243, v56\n\t"
    "v_mul_f32 v57, v243, v57\n\t"
    "v_cvt_pk_bf16_f32 v198, v216, v217\n\t"
    "v_mul_f32 v58, v243, v58\n\t"
    "v_mul_f32 v59, v243, v59\n\t"
    "v_add_f32 v229, v229, v216\n\t"
    "v_mul_f32 v60, v243, v60\n\t"
    "v_mul_f32 v61, v243, v61\n\t"
    "v_mfma_f32_32x32x16_bf16 v[160:175], a[140:143], a[44:47], v[160:175]\n\t"
    "v_add_f32 v229, v229, v217\n\t"
    "v_mul_f32 v62, v243, v62\n\t"
    "v_mul_f32 v63, v243, v63\n\t"
    "s_nop 0\n\t"
    "v_cvt_pk_bf16_f32 v56, v56, v57\n\t"
    "v_cvt_pk_bf16_f32 v57, v58, v59\n\t"
    "v_cvt_pk_bf16_f32 v199, v218, v219\n\t"
    "v_cvt_pk_bf16_f32 v58, v60, v61\n\t"
    "v_cvt_pk_bf16_f32 v59, v62, v63\n\t"
    "v_add_f32 v229, v229, v218\n\t"
    "v_accvgpr_write_b32 a252, v56\n\t"
    "v_accvgpr_write_b32 a253, v57\n\t"
    "v_add_f32 v229, v229, v219\n\t"
    "v_mfma_f32_32x32x16_bf16 v[176:191], a[140:143], a[92:95], v[176:191]\n\t"
    "ds_read_b64_tr_b16 a[140:141], v234 offset:8704\n\t"
    "ds_read_b64_tr_b16 a[142:143], v234 offset:11776\n\t"
    "v_accvgpr_write_b32 a254, v58\n\t"
    "v_accvgpr_write_b32 a255, v59\n\t"
    "s_waitcnt lgkmcnt(12)\n\t"
    "v_mfma_f32_32x32x16_bf16 v[0:15], a[96:99], v[192:195], 0\n\t"
    "ds_read_b128 v[220:223], v241 offset:0\n\t"
    "v_fma_f32 v216, v136, s63, v224\n\t"
    "v_fma_f32 v217, v137, s63, v224\n\t"
    "v_exp_f32 v216, v216\n\t"
    "v_exp_f32 v217, v217\n\t"
    "v_fma_f32 v218, v152, s63, v225\n\t"
    "v_mfma_f32_32x32x16_bf16 v[96:111], a[96:99], v[196:199], 0\n\t"
    "ds_read_b128 a[96:99], v230 offset:0\n\t"
    "v_fma_f32 v219, v153, s63, v225\n\t"
    "v_exp_f32 v218, v218\n\t"
    "v_exp_f32 v219, v219\n\t"
    "v_cvt_pk_bf16_f32 v208, v216, v217\n\t"
    "v_mfma_f32_32x32x16_bf16 v[16:31], a[100:103], v[192:195], 0\n\t"
    "s_waitcnt lgkmcnt(1)\n\t"
    "buffer_store_dwordx4 v[220:223], v248, s[28:31], 0 offen nt\n\t"
    "ds_read_b128 v[252:255], v241 offset:1024\n\t"
    "v_add_f32 v228, v228, v216\n\t"
    "v_add_f32 v228, v228, v217\n\t"
    "v_fma_f32 v216, v138, s63, v224\n\t"
    "v_fma_f32 v217, v139, s63, v224\n\t"
    "v_exp_f32 v216, v216\n\t"
    "v_mfma_f32_32x32x16_bf16 v[112:127], a[100:103], v[196:199], 0\n\t"
    "ds_read_b128 a[100:103], v231 offset:0\n\t"
    "v_exp_f32 v217, v217\n\t"
    "v_cvt_pk_bf16_f32 v212, v218, v219\n\t"
    "v_add_f32 v229, v229, v218\n\t"
    "v_add_f32 v229, v229, v219\n\t"
    "v_fma_f32 v218, v154, s63, v225\n\t"
    "v_mfma_f32_32x32x16_bf16 v[32:47], a[104:107], v[192:195], 0\n\t"
    "v_fma_f32 v219, v155, s63, v225\n\t"
    "v_exp_f32 v218, v218\n\t"
    "v_exp_f32 v219, v219\n\t"
    "v_cvt_pk_bf16_f32 v209, v216, v217\n\t"
    "v_add_f32 v228, v228, v216\n\t"
    "v_add_f32 v228, v228, v217\n\t"
    "v_mfma_f32_32x32x16_bf16 a[144:159], a[104:107], v[196:199], 0\n\t"
    "ds_read_b128 a[104:107], v232 offset:0\n\t"
    "v_fma_f32 v216, v140, s63, v224\n\t"
    "v_fma_f32 v217, v141, s63, v224\n\t"
    "v_exp_f32 v216, v216\n\t"
    "v_exp_f32 v217, v217\n\t"
    "v_mfma_f32_32x32x16_bf16 v[48:63], a[108:111], v[192:195], 0\n\t"
    "s_waitcnt lgkmcnt(2)\n\t"
    "buffer_store_dwordx4 v[252:255], v249, s[28:31], 0 offen nt\n\t"
    "v_cvt_pk_bf16_f32 v213, v218, v219\n\t"
    "v_add_f32 v229, v229, v218\n\t"
    "v_add_f32 v229, v229, v219\n\t"
    "v_fma_f32 v218, v156, s63, v225\n\t"
    "v_fma_f32 v219, v157, s63, v225\n\t"
    "v_exp_f32 v218, v218\n\t"
    "v_mfma_f32_32x32x16_bf16 a[160:175], a[108:111], v[196:199], 0\n\t"
    "ds_read_b128 a[108:111], v233 offset:0\n\t"
    "v_exp_f32 v219, v219\n\t"
    "v_cvt_pk_bf16_f32 v210, v216, v217\n\t"
    "v_add_f32 v228, v228, v216\n\t"
    "v_add_f32 v228, v228, v217\n\t"
    "v_fma_f32 v216, v142, s63, v224\n\t"
    "v_mfma_f32_32x32x16_bf16 v[64:79], a[112:115], v[192:195], 0\n\t"
    "ds_read_b128 v[220:223], v241 offset:2048\n\t"
    "v_fma_f32 v217, v143, s63, v224\n\t"
    "v_exp_f32 v216, v216\n\t"
    "v_exp_f32 v217, v217\n\t"
    "v_cvt_pk_bf16_f32 v214, v218, v219\n\t"
    "v_add_f32 v229, v229, v218\n\t"
    "v_mfma_f32_32x32x16_bf16 a[176:191], a[112:115], v[196:199], 0\n\t"
    "ds_read_b128 a[112:115], v230 offset:1024\n\t"
    "v_add_f32 v229, v229, v219\n\t"
    "v_fma_f32 v218, v158, s63, v225\n\t"
    "v_fma_f32 v219, v159, s63, v225\n\t"
    "v_exp_f32 v218, v218\n\t"
    "v_exp_f32 v219, v219\n\t"
    "v_mfma_f32_32x32x16_bf16 v[80:95], a[116:119], v[192:195], 0\n\t"
    "v_cvt_pk_bf16_f32 v211, v216, v217\n\t"
    "v_add_f32 v228, v228, v216\n\t"
    "v_add_f32 v228, v228, v217\n\t"
    "s_nop 0\n\t"
    "v_cvt_pk_bf16_f32 v215, v218, v219\n\t"
    "v_add_f32 v229, v229, v218\n\t"
    "v_add_f32 v229, v229, v219\n\t"
    "v_mfma_f32_32x32x16_bf16 a[192:207], a[116:119], v[196:199], 0\n\t"
    "ds_read_b128 a[116:119], v231 offset:1024\n\t"
    "v_mov_b32 v226, v228\n\t"
    "v_mov_b32 v227, v229\n\t"
    "v_fma_f32 v216, v160, s63, v224\n\t"
    "v_fma_f32 v217, v161, s63, v224\n\t"
    "v_exp_f32 v216, v216\n\t"
    "v_mfma_f32_32x32x16_bf16 v[0:15], a[120:123], v[208:211], v[0:15]\n\t"
    "s_waitcnt lgkmcnt(2)\n\t"
    "buffer_store_dwordx4 v[220:223], v248, s[28:31], s86 offen nt\n\t"
    "v_exp_f32 v217, v217\n\t"
    "v_fma_f32 v218, v176, s63, v225\n\t"
    "v_fma_f32 v219, v177, s63, v225\n\t"
    "v_exp_f32 v218, v218\n\t"
    "v_exp_f32 v219, v219\n\t"
    "v_mfma_f32_32x32x16_bf16 v[96:111], a[120:123], v[212:215], v[96:111]\n\t"
    "ds_read_b128 a[120:123], v232 offset:1024\n\t"
    "v_cvt_pk_bf16_f32 v200, v216, v217\n\t"
    "v_add_f32 v228, v216, v217\n\t"
    "v_fma_f32 v216, v162, s63, v224\n\t"
    "v_fma_f32 v217, v163, s63, v224\n\t"
    "v_exp_f32 v216, v216\n\t"
    "v_mfma_f32_32x32x16_bf16 v[16:31], a[124:127], v[208:211], v[16:31]\n\t"
    "ds_read_b128 v[252:255], v241 offset:3072\n\t"
    "v_exp_f32 v217, v217\n\t"
    "v_cvt_pk_bf16_f32 v204, v218, v219\n\t"
    "v_add_f32 v229, v218, v219\n\t"
    "v_fma_f32 v218, v178, s63, v225\n\t"
    "v_fma_f32 v219, v179, s63, v225\n\t"
    "v_mfma_f32_32x32x16_bf16 v[112:127], a[124:127], v[212:215], v[112:127]\n\t"
    "ds_read_b128 a[124:127], v233 offset:1024\n\t"
    "ds_write_b128 v244, a[208:211] offset:0\n\t"
    "v_exp_f32 v218, v218\n\t"
    "v_exp_f32 v219, v219\n\t"
    "v_cvt_pk_bf16_f32 v201, v216, v217\n\t"
    "v_add_f32 v228, v228, v216\n\t"
    "v_mfma_f32_32x32x16_bf16 v[32:47], a[128:131], v[208:211], v[32:47]\n\t"
    "v_add_f32 v228, v228, v217\n\t"
    "v_fma_f32 v216, v164, s63, v224\n\t"
    "v_fma_f32 v217, v165, s63, v224\n\t"
    "v_exp_f32 v216, v216\n\t"
    "v_exp_f32 v217, v217\n\t"
    "v_mfma_f32_32x32x16_bf16 a[144:159], a[128:131], v[212:215], a[144:159]\n\t"
    "ds_read_b128 a[128:131], v230 offset:2048\n\t"
    "ds_write_b128 v245, a[212:215] offset:0\n\t"
    "v_cvt_pk_bf16_f32 v205, v218, v219\n\t"
    "v_add_f32 v229, v229, v218\n\t"
    "v_add_f32 v229, v229, v219\n\t"
    "v_fma_f32 v218, v180, s63, v225\n\t"
    "v_fma_f32 v219, v181, s63, v225\n\t"
    "v_mfma_f32_32x32x16_bf16 v[48:63], a[132:135], v[208:211], v[48:63]\n\t"
    "s_waitcnt lgkmcnt(4)\n\t"
    "buffer_store_dwordx4 v[252:255], v249, s[28:31], s86 offen nt\n\t"
    "v_exp_f32 v218, v218\n\t"
    "v_exp_f32 v219, v219\n\t"
    "v_cvt_pk_bf16_f32 v202, v216, v217\n\t"
    "v_add_f32 v228, v228, v216\n\t"
    "v_add_f32 v228, v228, v217\n\t"
    "v_mfma_f32_32x32x16_bf16 a[160:175], a[132:135], v[212:215], a[160:175]\n\t"
    "ds_read_b128 a[132:135], v231 offset:2048\n\t"
    "ds_write_b128 v246, a[216:219] offset:0\n\t"
    "v_fma_f32 v216, v166, s63, v224\n\t"
    "v_fma_f32 v217, v167, s63, v224\n\t"
    "v_exp_f32 v216, v216\n\t"
    "v_exp_f32 v217, v217\n\t"
    "v_mfma_f32_32x32x16_bf16 v[64:79], a[136:139], v[208:211], v[64:79]\n\t"
    "v_cvt_pk_bf16_f32 v206, v218, v219\n\t"
    "v_add_f32 v229, v229, v218\n\t"
    "v_add_f32 v229, v229, v219\n\t"
    "v_fma_f32 v218, v182, s63, v225\n\t"
    "v_fma_f32 v219, v183, s63, v225\n\t"
    "v_exp_f32 v218, v218\n\t"
    "v_mfma_f32_32x32x16_bf16 a[176:191], a[136:139], v[212:215], a[176:191]\n\t"
    "ds_read_b128 a[136:139], v232 offset:2048\n\t"
    "ds_write_b128 v247, a[220:223] offset:0\n\t"
    "v_exp_f32 v219, v219\n\t"
    "v_cvt_pk_bf16_f32 v203, v216, v217\n\t"
    "v_add_f32 v228, v228, v216\n\t"
    "v_add_f32 v228, v228, v217\n\t"
    "s_nop 0\n\t"
    "v_mfma_f32_32x32x16_bf16 v[80:95], a[140:143], v[208:211], v[80:95]\n\t"
    "v_cvt_pk_bf16_f32 v207, v218, v219\n\t"
    "v_add_f32 v229, v229, v218\n\t"
    "v_add_f32 v229, v229, v219\n\t"
    "v_mfma_f32_32x32x16_bf16 a[192:207], a[140:143], v[212:215], a[192:207]\n\t"
    "ds_read_b128 a[140:143], v233 offset:2048\n\t"
    "ds_read_b128 v[220:223], v241 offset:0\n\t"
    "v_mfma_f32_32x32x16_bf16 v[128:143], a[96:99], a[0:3], 0\n\t"
    "v_fma_f32 v216, v168, s63, v224\n\t"
    "v_fma_f32 v217, v169, s63, v224\n\t"
    "v_exp_f32 v216, v216\n\t"
    "v_exp_f32 v217, v217\n\t"
    "v_mfma_f32_32x32x16_bf16 v[144:159], a[96:99], a[48:51], 0\n\t"
    "s_waitcnt lgkmcnt(0)\n\t"
    "buffer_store_dwordx4 v[220:223], v248, s[28:31], s87 offen nt\n\t"
    "v_fma_f32 v218, v184, s63, v225\n\t"
    "v_fma_f32 v219, v185, s63, v225\n\t"
    "v_exp_f32 v218, v218\n\t"
    "v_mfma_f32_32x32x16_bf16 v[128:143], a[100:103], a[4:7], v[128:143]\n\t"
    "v_exp_f32 v219, v219\n\t"
    "v_cvt_pk_bf16_f32 v208, v216, v217\n\t"
    "v_add_f32 v228, v228, v216\n\t"
    "v_add_f32 v228, v228, v217\n\t"
    "v_mfma_f32_32x32x16_bf16 v[144:159], a[100:103], a[52:55], v[144:159]\n\t"
    "v_fma_f32 v216, v170, s63, v224\n\t"
    "s_waitcnt vmcnt(5) lgkmcnt(0)\n\t"
    "s_barrier\n\t"
    "v_mfma_f32_32x32x16_bf16 v[128:143], a[104:107], a[8:11], v[128:143]\n\t"
    "ds_read_b64_tr_b16 a[96:97], v234 offset:24576\n\t"
    "ds_read_b64_tr_b16 a[98:99], v234 offset:27648\n\t"
    "s_mov_b32 m0, s49\n\t"
    "s_nop 0\n\t"
    "buffer_load_dwordx4 v238, s[20:23], s34 offen lds\n\t"
    "v_mfma_f32_32x32x16_bf16 v[144:159], a[104:107], a[56:59], v[144:159]\n\t"
    "ds_read_b64_tr_b16 a[100:101], v234 offset:25088\n\t"
    "ds_read_b64_tr_b16 a[102:103], v234 offset:28160\n\t"
    "v_fma_f32 v217, v171, s63, v224\n\t"
    "v_exp_f32 v216, v216\n\t"
    "v_mfma_f32_32x32x16_bf16 v[128:143], a[108:111], a[12:15], v[128:143]\n\t"
    "ds_read_b64_tr_b16 a[104:105], v234 offset:25600\n\t"
    "ds_read_b64_tr_b16 a[106:107], v234 offset:28672\n\t"
    "buffer_load_dwordx4 v239, s[20:23], s34 offen offset:1024 lds\n\t"
    "v_exp_f32 v217, v217\n\t"
    "v_cvt_pk_bf16_f32 v212, v218, v219\n\t"
    "v_mfma_f32_32x32x16_bf16 v[144:159], a[108:111], a[60:63], v[144:159]\n\t"
    "ds_read_b64_tr_b16 a[108:109], v234 offset:26112\n\t"
    "ds_read_b64_tr_b16 a[110:111], v234 offset:29184\n\t"
    "v_add_f32 v229, v229, v218\n\t"
    "v_add_f32 v229, v229, v219\n\t"
    "v_fma_f32 v218, v186, s63, v225\n\t"
    "v_mfma_f32_32x32x16_bf16 v[128:143], a[112:115], a[16:19], v[128:143]\n\t"
    "buffer_load_dwordx4 v240, s[20:23], s34 offen offset:2048 lds\n\t"
    "s_add_u32 s34, s34, 12288\n\t"
    "v_fma_f32 v219, v187, s63, v225\n\t"
    "v_exp_f32 v218, v218\n\t"
    "v_mfma_f32_32x32x16_bf16 v[144:159], a[112:115], a[64:67], v[144:159]\n\t"
    "ds_read_b64_tr_b16 a[112:113], v234 offset:26624\n\t"
    "ds_read_b64_tr_b16 a[114:115], v234 offset:29696\n\t"
    "v_exp_f32 v219, v219\n\t"
    "v_cvt_pk_bf16_f32 v209, v216, v217\n\t"
    "v_mfma_f32_32x32x16_bf16 v[128:143], a[116:119], a[20:23], v[128:143]\n\t"
    "s_add_u32 m0, s49, 12288\n\t"
    "s_nop 0\n\t"
    "buffer_load_dwordx4 v238, s[20:23], s35 offen lds\n\t"
    "v_add_f32 v228, v228, v216\n\t"
    "v_add_f32 v228, v228, v217\n\t"
    "v_fma_f32 v216, v172, s63, v224\n\t"
    "v_mfma_f32_32x32x16_bf16 v[144:159], a[116:119], a[68:71], v[144:159]\n\t"
    "ds_read_b64_tr_b16 a[116:117], v234 offset:27136\n\t"
    "ds_read_b64_tr_b16 a[118:119], v234 offset:30208\n\t"
    "v_fma_f32 v217, v173, s63, v224\n\t"
    "v_exp_f32 v216, v216\n\t"
    "v_mfma_f32_32x32x16_bf16 v[128:143], a[120:123], a[24:27], v[128:143]\n\t"
    "buffer_load_dwordx4 v239, s[20:23], s35 offen offset:1024 lds\n\t"
    "v_exp_f32 v217, v217\n\t"
    "v_cvt_pk_bf16_f32 v213, v218, v219\n\t"
    "v_add_f32 v229, v229, v218\n\t"
    "v_mfma_f32_32x32x16_bf16 v[144:159], a[120:123], a[72:75], v[144:159]\n\t"
    "ds_read_b64_tr_b16 a[120:121], v234 offset:30720\n\t"
    "ds_read_b64_tr_b16 a[122:123], v234 offset:33792\n\t"
    "v_add_f32 v229, v229, v219\n\t"
    "v_fma_f32 v218, v188, s63, v225\n\t"
    "v_fma_f32 v219, v189, s63, v225\n\t"
    "v_mfma_f32_32x32x16_bf16 v[128:143], a[124:127], a[28:31], v[128:143]\n\t"
    "buffer_load_dwordx4 v240, s[20:23], s35 offen offset:2048 lds\n\t"
    "s_add_u32 s35, s35, 12288\n\t"
    "v_exp_f32 v218, v218\n\t"
    "v_exp_f32 v219, v219\n\t"
    "v_mfma_f32_32x32x16_bf16 v[144:159], a[124:127], a[76:79], v[144:159]\n\t"
    "ds_read_b64_tr_b16 a[124:125], v234 offset:31232\n\t"
    "ds_read_b64_tr_b16 a[126:127], v234 offset:34304\n\t"
    "v_cvt_pk_bf16_f32 v210, v216, v217\n\t"
    "v_add_f32 v228, v228, v216\n\t"
    "v_add_f32 v228, v228, v217\n\t"
    "v_mfma_f32_32x32x16_bf16 v[128:143], a[128:131], a[32:35], v[128:143]\n\t"
    "s_mov_b32 m0, s46\n\t"
    "s_nop 0\n\t"
    "buffer_load_dwordx4 v235, s[16:19], s32 offen lds\n\t"
    "v_fma_f32 v216, v174, s63, v224\n\t"
    "v_fma_f32 v217, v175, s63, v224\n\t"
    "v_mfma_f32_32x32x16_bf16 v[144:159], a[128:131], a[80:83], v[144:159]\n\t"
    "ds_read_b64_tr_b16 a[128:129], v234 offset:31744\n\t"
    "ds_read_b64_tr_b16 a[130:131], v234 offset:34816\n\t"
    "v_exp_f32 v216, v216\n\t"
    "v_exp_f32 v217, v217\n\t"
    "v_mfma_f32_32x32x16_bf16 v[128:143], a[132:135], a[36:39], v[128:143]\n\t"
    "buffer_load_dwordx4 v236, s[16:19], s32 offen offset:1024 lds\n\t"
    "v_cvt_pk_bf16_f32 v214, v218, v219\n\t"
    "v_add_f32 v229, v229, v218\n\t"
    "v_add_f32 v229, v229, v219\n\t"
    "v_fma_f32 v218, v190, s63, v225\n\t"
    "v_mfma_f32_32x32x16_bf16 v[144:159], a[132:135], a[84:87], v[144:159]\n\t"
    "ds_read_b64_tr_b16 a[132:133], v234 offset:32256\n\t"
    "ds_read_b64_tr_b16 a[134:135], v234 offset:35328\n\t"
    "v_fma_f32 v219, v191, s63, v225\n\t"
    "v_exp_f32 v218, v218\n\t"
    "v_mfma_f32_32x32x16_bf16 v[128:143], a[136:139], a[40:43], v[128:143]\n\t"
    "buffer_load_dwordx4 v237, s[16:19], s32 offen offset:2048 lds\n\t"
    "s_add_u32 s32, s32, 12288\n\t"
    "v_exp_f32 v219, v219\n\t"
    "v_cvt_pk_bf16_f32 v211, v216, v217\n\t"
    "v_mfma_f32_32x32x16_bf16 v[144:159], a[136:139], a[88:91], v[144:159]\n\t"
    "ds_read_b64_tr_b16 a[136:137], v234 offset:32768\n\t"
    "ds_read_b64_tr_b16 a[138:139], v234 offset:35840\n\t"
    "v_add_f32 v228, v228, v216\n\t"
    "v_add_f32 v228, v228, v217\n\t"
    "s_nop 0\n\t"
    "v_mfma_f32_32x32x16_bf16 v[128:143], a[140:143], a[44:47], v[128:143]\n\t"
    "s_add_u32 m0, s46, 12288\n\t"
    "s_nop 0\n\t"
    "buffer_load_dwordx4 v235, s[16:19], s33 offen lds\n\t"
    "v_cvt_pk_bf16_f32 v215, v218, v219\n\t"
    "v_add_f32 v229, v229, v218\n\t"
    "v_add_f32 v229, v229, v219\n\t"
    "v_mfma_f32_32x32x16_bf16 v[144:159], a[140:143], a[92:95], v[144:159]\n\t"
    "ds_read_b64_tr_b16 a[140:141], v234 offset:33280\n\t"
    "ds_read_b64_tr_b16 a[142:143], v234 offset:36352\n\t"
    "v_add_f32 v251, v228, v229\n\t"
    "v_cmp_nge_f32 vcc, s64, v251\n\t"
    "s_cbranch_vccnz .Ld_stub15\n\t"
    ".Ld_back15:\n\t"
    "s_waitcnt lgkmcnt(12)\n\t"
    "v_mfma_f32_32x32x16_bf16 v[0:15], a[96:99], v[200:203], v[0:15]\n\t"
    "buffer_load_dwordx4 v236, s[16:19], s33 offen offset:1024 lds\n\t"
    "v_add_f32 v226, v226, v228\n\t"
    "v_add_f32 v227, v227, v229\n\t"
    "v_mfma_f32_32x32x16_bf16 v[96:111], a[96:99], v[204:207], v[96:111]\n\t"
    "ds_read_b128 a[96:99], v230 offset:24576\n\t"
    "v_mfma_f32_32x32x16_bf16 v[16:31], a[100:103], v[200:203], v[16:31]\n\t"
    "buffer_load_dwordx4 v237, s[16:19], s33 offen offset:2048 lds\n\t"
    "s_add_u32 s33, s33, 12288\n\t"
    "v_mfma_f32_32x32x16_bf16 v[112:127], a[100:103], v[204:207], v[112:127]\n\t"
    "ds_read_b128 a[100:103], v231 offset:24576\n\t"
    "ds_read_b128 v[252:255], v241 offset:1024\n\t"
    "v_fma_f32 v216, v128, s63, v224\n\t"
    "v_fma_f32 v217, v129, s63, v224\n\t"
    "v_mfma_f32_32x32x16_bf16 v[32:47], a[104:107], v[200:203], v[32:47]\n\t"
    "v_exp_f32 v216, v216\n\t"
    "v_exp_f32 v217, v217\n\t"
    "v_mfma_f32_32x32x16_bf16 a[144:159], a[104:107], v[204:207], a[144:159]\n\t"
    "ds_read_b128 a[104:107], v232 offset:24576\n\t"
    "v_fma_f32 v218, v144, s63, v225\n\t"
    "v_fma_f32 v219, v145, s63, v225\n\t"
    "v_exp_f32 v218, v218\n\t"
    "v_mfma_f32_32x32x16_bf16 v[48:63], a[108:111], v[200:203], v[48:63]\n\t"
    "v_exp_f32 v219, v219\n\t"
    "v_cvt_pk_bf16_f32 v192, v216, v217\n\t"
    "v_add_f32 v228, v216, v217\n\t"
    "v_mfma_f32_32x32x16_bf16 a[160:175], a[108:111], v[204:207], a[160:175]\n\t"
    "s_waitcnt lgkmcnt(1)\n\t"
    "buffer_store_dwordx4 v[252:255], v249, s[28:31], s87 offen nt\n\t"
    "ds_read_b128 a[108:111], v233 offset:24576\n\t"
    "v_fma_f32 v216, v130, s63, v224\n\t"
    "v_fma_f32 v217, v131, s63, v224\n\t"
    "v_mfma_f32_32x32x16_bf16 v[64:79], a[112:115], v[200:203], v[64:79]\n\t"
    "v_exp_f32 v216, v216\n\t"
    "v_exp_f32 v217, v217\n\t"
    "v_mfma_f32_32x32x16_bf16 a[176:191], a[112:115], v[204:207], a[176:191]\n\t"
    "ds_read_b128 a[112:115], v230 offset:25600\n\t"
    "v_cvt_pk_bf16_f32 v196, v218, v219\n\t"
    "v_add_f32 v229, v218, v219\n\t"
    "v_fma_f32 v218, v146, s63, v225\n\t"
    "v_fma_f32 v219, v147, s63, v225\n\t"
    "v_mfma_f32_32x32x16_bf16 v[80:95], a[116:119], v[200:203], v[80:95]\n\t"
    "v_exp_f32 v218, v218\n\t"
    "v_exp_f32 v219, v219\n\t"
    "v_mfma_f32_32x32x16_bf16 a[192:207], a[116:119], v[204:207], a[192:207]\n\t"
    "ds_read_b128 a[116:119], v231 offset:25600\n\t"
    "v_cvt_pk_bf16_f32 v193, v216, v217\n\t"
    "v_add_f32 v228, v228, v216\n\t"
    "v_add_f32 v228, v228, v217\n\t"
    "v_mfma_f32_32x32x16_bf16 v[0:15], a[120:123], v[208:211], v[0:15]\n\t"
    "ds_read_b128 v[220:223], v241 offset:2048\n\t"
    "v_fma_f32 v216, v132, s63, v224\n\t"
    "v_fma_f32 v217, v133, s63, v224\n\t"
    "v_exp_f32 v216, v216\n\t"
    "v_mfma_f32_32x32x16_bf16 v[96:111], a[120:123], v[212:215], v[96:111]\n\t"
    "ds_read_b128 a[120:123], v232 offset:25600\n\t"
    "v_exp_f32 v217, v217\n\t"
    "v_cvt_pk_bf16_f32 v197, v218, v219\n\t"
    "v_mfma_f32_32x32x16_bf16 v[16:31], a[124:127], v[208:211], v[16:31]\n\t"
    "v_add_f32 v229, v229, v218\n\t"
    "v_add_f32 v229, v229, v219\n\t"
    "v_fma_f32 v218, v148, s63, v225\n\t"
    "v_fma_f32 v219, v149, s63, v225\n\t"
    "v_mfma_f32_32x32x16_bf16 v[112:127], a[124:127], v[212:215], v[112:127]\n\t"
    "ds_read_b128 a[124:127], v233 offset:25600\n\t"
    "v_exp_f32 v218, v218\n\t"
    "v_exp_f32 v219, v219\n\t"
    "v_mfma_f32_32x32x16_bf16 v[32:47], a[128:131], v[208:211], v[32:47]\n\t"
    "s_waitcnt lgkmcnt(2)\n\t"
    "buffer_store_dwordx4 v[220:223], v248, s[28:31], s43 offen nt\n\t"
    "v_cvt_pk_bf16_f32 v194, v216, v217\n\t"
    "v_add_f32 v228, v228, v216\n\t"
    "v_mfma_f32_32x32x16_bf16 a[144:159], a[128:131], v[212:215], a[144:159]\n\t"
    "ds_read_b128 a[128:131], v230 offset:26624\n\t"
    "v_add_f32 v228, v228, v217\n\t"
    "v_fma_f32 v216, v134, s63, v224\n\t"
    "v_fma_f32 v217, v135, s63, v224\n\t"
    "v_mfma_f32_32x32x16_bf16 v[48:63], a[132:135], v[208:211], v[48:63]\n\t"
    "ds_read_b128 v[252:255], v241 offset:3072\n\t"
    "v_exp_f32 v216, v216\n\t"
    "v_exp_f32 v217, v217\n\t"
    "v_mfma_f32_32x32x16_bf16 a[160:175], a[132:135], v[212:215], a[160:175]\n\t"
    "ds_read_b128 a[132:135], v231 offset:26624\n\t"
    "v_cvt_pk_bf16_f32 v198, v218, v219\n\t"
    "v_add_f32 v229, v229, v218\n\t"
    "v_add_f32 v229, v229, v219\n\t"
    "v_mfma_f32_32x32x16_bf16 v[64:79], a[136:139], v[208:211], v[64:79]\n\t"
    "v_fma_f32 v218, v150, s63, v225\n\t"
    "v_fma_f32 v219, v151, s63, v225\n\t"
    "v_exp_f32 v218, v218\n\t"
    "v_mfma_f32_32x32x16_bf16 a[176:191], a[136:139], v[212:215], a[176:191]\n\t"
    "ds_read_b128 a[136:139], v232 offset:26624\n\t"
    "v_exp_f32 v219, v219\n\t"
    "v_cvt_pk_bf16_f32 v195, v216, v217\n\t"
    "v_add_f32 v228, v228, v216\n\t"
    "v_mfma_f32_32x32x16_bf16 v[80:95], a[140:143], v[208:211], v[80:95]\n\t"
    "s_waitcnt lgkmcnt(2)\n\t"
    "buffer_store_dwordx4 v[252:255], v249, s[28:31], s43 offen nt\n\t"
    "v_add_f32 v228, v228, v217\n\t"
    "s_nop 0\n\t"
    "v_cvt_pk_bf16_f32 v199, v218, v219\n\t"
    "v_mfma_f32_32x32x16_bf16 a[192:207], a[140:143], v[212:215], a[192:207]\n\t"
    "ds_read_b128 a[140:143], v233 offset:26624\n\t"
    "v_add_f32 v229, v229, v218\n\t"
    "v_add_f32 v229, v229, v219\n\t"
    "v_mfma_f32_32x32x16_bf16 v[160:175], a[96:99], a[0:3], 0\n\t"
    "v_fma_f32 v216, v136, s63, v224\n\t"
    "v_fma_f32 v217, v137, s63, v224\n\t"
    "v_exp_f32 v216, v216\n\t"
    "v_exp_f32 v217, v217\n\t"
    "v_mfma_f32_32x32x16_bf16 v[176:191], a[96:99], a[48:51], 0\n\t"
    "v_fma_f32 v218, v152, s63, v225\n\t"
    "v_fma_f32 v219, v153, s63, v225\n\t"
    "v_exp_f32 v218, v218\n\t"
    "v_mfma_f32_32x32x16_bf16 v[160:175], a[100:103], a[4:7], v[160:175]\n\t"
    "v_exp_f32 v219, v219\n\t"
    "v_cvt_pk_bf16_f32 v208, v216, v217\n\t"
    "v_add_f32 v228, v228, v216\n\t"
    "v_add_f32 v228, v228, v217\n\t"
    "v_fma_f32 v216, v138, s63, v224\n\t"
    "v_mfma_f32_32x32x16_bf16 v[176:191], a[100:103], a[52:55], v[176:191]\n\t"
    "v_fma_f32 v217, v139, s63, v224\n\t"
    "s_waitcnt vmcnt(3) lgkmcnt(0)\n\t"
    "s_barrier\n\t"
    "v_mfma_f32_32x32x16_bf16 v[160:175], a[104:107], a[8:11], v[160:175]\n\t"
    "ds_read_b64_tr_b16 a[96:97], v234 offset:0\n\t"
    "ds_read_b64_tr_b16 a[98:99], v234 offset:3072\n\t"
    "s_add_u32 m0, s49, 24576\n\t"
    "s_nop 0\n\t"
    "buffer_load_dwordx4 v238, s[20:23], s34 offen lds\n\t"
    "v_mfma_f32_32x32x16_bf16 v[176:191], a[104:107], a[56:59], v[176:191]\n\t"
    "ds_read_b64_tr_b16 a[100:101], v234 offset:512\n\t"
    "ds_read_b64_tr_b16 a[102:103], v234 offset:3584\n\t"
    "v_exp_f32 v216, v216\n\t"
    "v_exp_f32 v217, v217\n\t"
    "v_mfma_f32_32x32x16_bf16 v[160:175], a[108:111], a[12:15], v[160:175]\n\t"
    "ds_read_b64_tr_b16 a[104:105], v234 offset:1024\n\t"
    "ds_read_b64_tr_b16 a[106:107], v234 offset:4096\n\t"
    "buffer_load_dwordx4 v239, s[20:23], s34 offen offset:1024 lds\n\t"
    "v_cvt_pk_bf16_f32 v212, v218, v219\n\t"
    "v_mfma_f32_32x32x16_bf16 v[176:191], a[108:111], a[60:63], v[176:191]\n\t"
    "ds_read_b64_tr_b16 a[108:109], v234 offset:1536\n\t"
    "ds_read_b64_tr_b16 a[110:111], v234 offset:4608\n\t"
    "v_add_f32 v229, v229, v218\n\t"
    "v_add_f32 v229, v229, v219\n\t"
    "v_fma_f32 v218, v154, s63, v225\n\t"
    "v_fma_f32 v219, v155, s63, v225\n\t"
    "v_mfma_f32_32x32x16_bf16 v[160:175], a[112:115], a[16:19], v[160:175]\n\t"
    "buffer_load_dwordx4 v240, s[20:23], s34 offen offset:2048 lds\n\t"
    "s_add_u32 s34, s34, 12288\n\t"
    "v_exp_f32 v218, v218\n\t"
    "v_mfma_f32_32x32x16_bf16 v[176:191], a[112:115], a[64:67], v[176:191]\n\t"
    "ds_read_b64_tr_b16 a[112:113], v234 offset:2048\n\t"
    "ds_read_b64_tr_b16 a[114:115], v234 offset:5120\n\t"
    "v_exp_f32 v219, v219\n\t"
    "v_cvt_pk_bf16_f32 v209, v216, v217\n\t"
    "v_add_f32 v228, v228, v216\n\t"
    "v_mfma_f32_32x32x16_bf16 v[160:175], a[116:119], a[20:23], v[160:175]\n\t"
    "s_add_u32 m0, s49, 36864\n\t"
    "s_nop 0\n\t"
    "buffer_load_dwordx4 v238, s[20:23], s35 offen lds\n\t"
    "v_add_f32 v228, v228, v217\n\t"
    "v_fma_f32 v216, v140, s63, v224\n\t"
    "v_mfma_f32_32x32x16_bf16 v[176:191], a[116:119], a[68:71], v[176:191]\n\t"
    "ds_read_b64_tr_b16 a[116:117], v234 offset:2560\n\t"
    "ds_read_b64_tr_b16 a[118:119], v234 offset:5632\n\t"
    "v_fma_f32 v217, v141, s63, v224\n\t"
    "v_exp_f32 v216, v216\n\t"
    "v_mfma_f32_32x32x16_bf16 v[160:175], a[120:123], a[24:27], v[160:175]\n\t"
    "buffer_load_dwordx4 v239, s[20:23], s35 offen offset:1024 lds\n\t"
    "v_exp_f32 v217, v217\n\t"
    "v_cvt_pk_bf16_f32 v213, v218, v219\n\t"
    "v_add_f32 v229, v229, v218\n\t"
    "v_add_f32 v229, v229, v219\n\t"
    "v_mfma_f32_32x32x16_bf16 v[176:191], a[120:123], a[72:75], v[176:191]\n\t"
    "ds_read_b64_tr_b16 a[120:121], v234 offset:6144\n\t"
    "ds_read_b64_tr_b16 a[122:123], v234 offset:9216\n\t"
    "v_fma_f32 v218, v156, s63, v225\n\t"
    "v_fma_f32 v219, v157, s63, v225\n\t"
    "v_mfma_f32_32x32x16_bf16 v[160:175], a[124:127], a[28:31], v[160:175]\n\t"
    "buffer_load_dwordx4 v240, s[20:23], s35 offen offset:2048 lds\n\t"
    "s_add_u32 s35, s35, 12288\n\t"
    "v_exp_f32 v218, v218\n\t"
    "v_exp_f32 v219, v219\n\t"
    "v_mfma_f32_32x32x16_bf16 v[176:191], a[124:127], a[76:79], v[176:191]\n\t"
    "ds_read_b64_tr_b16 a[124:125], v234 offset:6656\n\t"
    "ds_read_b64_tr_b16 a[126:127], v234 offset:9728\n\t"
    "v_cvt_pk_bf16_f32 v210, v216, v217\n\t"
    "v_add_f32 v228, v228, v216\n\t"
    "v_add_f32 v228, v228, v217\n\t"
    "v_mfma_f32_32x32x16_bf16 v[160:175], a[128:131], a[32:35], v[160:175]\n\t"
    "s_add_u32 m0, s46, 24576\n\t"
    "s_nop 0\n\t"
    "buffer_load_dwordx4 v235, s[16:19], s32 offen lds\n\t"
    "v_fma_f32 v216, v142, s63, v224\n\t"
    "v_fma_f32 v217, v143, s63, v224\n\t"
    "v_mfma_f32_32x32x16_bf16 v[176:191], a[128:131], a[80:83], v[176:191]\n\t"
    "ds_read_b64_tr_b16 a[128:129], v234 offset:7168\n\t"
    "ds_read_b64_tr_b16 a[130:131], v234 offset:10240\n\t"
    "v_exp_f32 v216, v216\n\t"
    "v_exp_f32 v217, v217\n\t"
    "v_mfma_f32_32x32x16_bf16 v[160:175], a[132:135], a[36:39], v[160:175]\n\t"
    "buffer_load_dwordx4 v236, s[16:19], s32 offen offset:1024 lds\n\t"
    "v_cvt_pk_bf16_f32 v214, v218, v219\n\t"
    "v_add_f32 v229, v229, v218\n\t"
    "v_add_f32 v229, v229, v219\n\t"
    "v_fma_f32 v218, v158, s63, v225\n\t"
    "v_mfma_f32_32x32x16_bf16 v[176:191], a[132:135], a[84:87], v[176:191]\n\t"
    "ds_read_b64_tr_b16 a[132:133], v234 offset:7680\n\t"
    "ds_read_b64_tr_b16 a[134:135], v234 offset:10752\n\t"
    "v_fma_f32 v219, v159, s63, v225\n\t"
    "v_exp_f32 v218, v218\n\t"
    "v_mfma_f32_32x32x16_bf16 v[160:175], a[136:139], a[40:43], v[160:175]\n\t"
    "buffer_load_dwordx4 v237, s[16:19], s32 offen offset:2048 lds\n\t"
    "s_add_u32 s32, s32, 12288\n\t"
    "v_exp_f32 v219, v219\n\t"
    "v_cvt_pk_bf16_f32 v211, v216, v217\n\t"
    "v_mfma_f32_32x32x16_bf16 v[176:191], a[136:139], a[88:91], v[176:191]\n\t"
    "ds_read_b64_tr_b16 a[136:137], v234 offset:8192\n\t"
    "ds_read_b64_tr_b16 a[138:139], v234 offset:11264\n\t"
    "v_add_f32 v228, v228, v216\n\t"
    "v_add_f32 v228, v228, v217\n\t"
    "s_nop 0\n\t"
    "v_mfma_f32_32x32x16_bf16 v[160:175], a[140:143], a[44:47], v[160:175]\n\t"
    "s_add_u32 m0, s46, 36864\n\t"
    "s_nop 0\n\t"
    "buffer_load_dwordx4 v235, s[16:19], s33 offen lds\n\t"
    "v_cvt_pk_bf16_f32 v215, v218, v219\n\t"
    "v_add_f32 v229, v229, v218\n\t"
    "v_add_f32 v229, v229, v219\n\t"
    "v_mfma_f32_32x32x16_bf16 v[176:191], a[140:143], a[92:95], v[176:191]\n\t"
    "ds_read_b64_tr_b16 a[140:141], v234 offset:8704\n\t"
    "ds_read_b64_tr_b16 a[142:143], v234 offset:11776\n\t"
    "v_add_f32 v251, v228, v229\n\t"
    "v_cmp_nge_f32 vcc, s64, v251\n\t"
    "s_cbranch_vccnz .Ld_stub16\n\t"
    ".Ld_back16:\n\t"
    "s_waitcnt lgkmcnt(12)\n\t"
    "v_mfma_f32_32x32x16_bf16 v[0:15], a[96:99], v[192:195], v[0:15]\n\t"
    "buffer_load_dwordx4 v236, s[16:19], s33 offen offset:1024 lds\n\t"
    "v_add_f32 v226, v226, v228\n\t"
    "v_add_f32 v227, v227, v229\n\t"
    "v_mfma_f32_32x32x16_bf16 v[96:111], a[96:99], v[196:199], v[96:111]\n\t"
    "ds_read_b128 a[96:99], v230 offset:0\n\t"
    "v_mfma_f32_32x32x16_bf16 v[16:31], a[100:103], v[192:195], v[16:31]\n\t"
    "buffer_load_dwordx4 v237, s[16:19], s33 offen offset:2048 lds\n\t"
    "s_add_u32 s33, s33, 12288\n\t"
    "v_mfma_f32_32x32x16_bf16 v[112:127], a[100:103], v[196:199], v[112:127]\n\t"
    "ds_read_b128 a[100:103], v231 offset:0\n\t"
    "v_fma_f32 v216, v160, s63, v224\n\t"
    "v_fma_f32 v217, v161, s63, v224\n\t"
    "v_exp_f32 v216, v216\n\t"
    "v_mfma_f32_32x32x16_bf16 v[32:47], a[104:107], v[192:195], v[32:47]\n\t"
    "s_add_u32 m0, s61, 0\n\t"
    "s_nop 0\n\t"
    "buffer_load_dwordx4 v248, s[24:27], s87 offen nt lds\n\t"
    "v_exp_f32 v217, v217\n\t"
    "v_mfma_f32_32x32x16_bf16 a[144:159], a[104:107], v[196:199], a[144:159]\n\t"
    "ds_read_b128 a[104:107], v232 offset:0\n\t"
    "v_fma_f32 v218, v176, s63, v225\n\t"
    "v_fma_f32 v219, v177, s63, v225\n\t"
    "v_mfma_f32_32x32x16_bf16 v[48:63], a[108:111], v[192:195], v[48:63]\n\t"
    "v_exp_f32 v218, v218\n\t"
    "v_exp_f32 v219, v219\n\t"
    "v_cvt_pk_bf16_f32 v200, v216, v217\n\t"
    "v_mfma_f32_32x32x16_bf16 a[160:175], a[108:111], v[196:199], a[160:175]\n\t"
    "ds_read_b128 a[108:111], v233 offset:0\n\t"
    "v_add_f32 v228, v216, v217\n\t"
    "v_fma_f32 v216, v162, s63, v224\n\t"
    "v_fma_f32 v217, v163, s63, v224\n\t"
    "v_mfma_f32_32x32x16_bf16 v[64:79], a[112:115], v[192:195], v[64:79]\n\t"
    "v_exp_f32 v216, v216\n\t"
    "v_exp_f32 v217, v217\n\t"
    "v_cvt_pk_bf16_f32 v204, v218, v219\n\t"
    "v_mfma_f32_32x32x16_bf16 a[176:191], a[112:115], v[196:199], a[176:191]\n\t"
    "ds_read_b128 a[112:115], v230 offset:1024\n\t"
    "v_add_f32 v229, v218, v219\n\t"
    "v_fma_f32 v218, v178, s63, v225\n\t"
    "v_fma_f32 v219, v179, s63, v225\n\t"
    "v_mfma_f32_32x32x16_bf16 v[80:95], a[116:119], v[192:195], v[80:95]\n\t"
    "s_add_u32 m0, s61, 1024\n\t"
    "s_nop 0\n\t"
    "buffer_load_dwordx4 v249, s[24:27], s87 offen nt lds\n\t"
    "v_exp_f32 v218, v218\n\t"
    "v_mfma_f32_32x32x16_bf16 a[192:207], a[116:119], v[196:199], a[192:207]\n\t"
    "ds_read_b128 a[116:119], v231 offset:1024\n\t"
    "v_exp_f32 v219, v219\n\t"
    "v_cvt_pk_bf16_f32 v201, v216, v217\n\t"
    "v_add_f32 v228, v228, v216\n\t"
    "s_waitcnt lgkmcnt(10)\n\t"
    "v_mfma_f32_32x32x16_bf16 v[0:15], a[120:123], v[208:211], v[0:15]\n\t"
    "v_add_f32 v228, v228, v217\n\t"
    "v_fma_f32 v216, v164, s63, v224\n\t"
    "v_fma_f32 v217, v165, s63, v224\n\t"
    "v_exp_f32 v216, v216\n\t"
    "v_mfma_f32_32x32x16_bf16 v[96:111], a[120:123], v[212:215], v[96:111]\n\t"
    "ds_read_b128 a[120:123], v232 offset:1024\n\t"
    "v_exp_f32 v217, v217\n\t"
    "v_cvt_pk_bf16_f32 v205, v218, v219\n\t"
    "v_mfma_f32_32x32x16_bf16 v[16:31], a[124:127], v[208:211], v[16:31]\n\t"
    "v_add_f32 v229, v229, v218\n\t"
    "v_add_f32 v229, v229, v219\n\t"
    "v_fma_f32 v218, v180, s63, v225\n\t"
    "v_fma_f32 v219, v181, s63, v225\n\t"
    "v_mfma_f32_32x32x16_bf16 v[112:127], a[124:127], v[212:215], v[112:127]\n\t"
    "ds_read_b128 a[124:127], v233 offset:1024\n\t"
    "v_exp_f32 v218, v218\n\t"
    "v_exp_f32 v219, v219\n\t"
    "v_mfma_f32_32x32x16_bf16 v[32:47], a[128:131], v[208:211], v[32:47]\n\t"
    "s_add_u32 m0, s61, 2048\n\t"
    "s_nop 0\n\t"
    "buffer_load_dwordx4 v248, s[24:27], s43 offen nt lds\n\t"
    "v_cvt_pk_bf16_f32 v202, v216, v217\n\t"
    "v_add_f32 v228, v228, v216\n\t"
    "v_mfma_f32_32x32x16_bf16 a[144:159], a[128:131], v[212:215], a[144:159]\n\t"
    "ds_read_b128 a[128:131], v230 offset:2048\n\t"
    "v_add_f32 v228, v228, v217\n\t"
    "v_fma_f32 v216, v166, s63, v224\n\t"
    "v_fma_f32 v217, v167, s63, v224\n\t"
    "v_mfma_f32_32x32x16_bf16 v[48:63], a[132:135], v[208:211], v[48:63]\n\t"
    "v_exp_f32 v216, v216\n\t"
    "v_exp_f32 v217, v217\n\t"
    "v_cvt_pk_bf16_f32 v206, v218, v219\n\t"
    "v_mfma_f32_32x32x16_bf16 a[160:175], a[132:135], v[212:215], a[160:175]\n\t"
    "ds_read_b128 a[132:135], v231 offset:2048\n\t"
    "v_add_f32 v229, v229, v218\n\t"
    "v_add_f32 v229, v229, v219\n\t"
    "v_fma_f32 v218, v182, s63, v225\n\t"
    "s_waitcnt lgkmcnt(10)\n\t"
    "v_mfma_f32_32x32x16_bf16 v[64:79], a[136:139], v[208:211], v[64:79]\n\t"
    "v_fma_f32 v219, v183, s63, v225\n\t"
    "v_exp_f32 v218, v218\n\t"
    "v_exp_f32 v219, v219\n\t"
    "v_mfma_f32_32x32x16_bf16 a[176:191], a[136:139], v[212:215], a[176:191]\n\t"
    "ds_read_b128 a[136:139], v232 offset:2048\n\t"
    "v_cvt_pk_bf16_f32 v203, v216, v217\n\t"
    "v_add_f32 v228, v228, v216\n\t"
    "v_add_f32 v228, v228, v217\n\t"
    "v_mfma_f32_32x32x16_bf16 v[80:95], a[140:143], v[208:211], v[80:95]\n\t"
    "s_add_u32 m0, s61, 3072\n\t"
    "s_nop 0\n\t"
    "buffer_load_dwordx4 v249, s[24:27], s43 offen nt lds\n\t"
    "s_nop 0\n\t"
    "v_cvt_pk_bf16_f32 v207, v218, v219\n\t"
    "v_mfma_f32_32x32x16_bf16 a[192:207], a[140:143], v[212:215], a[192:207]\n\t"
    "ds_read_b128 a[140:143], v233 offset:2048\n\t"
    "v_add_f32 v229, v229, v218\n\t"
    "v_add_f32 v229, v229, v219\n\t"
    "s_waitcnt lgkmcnt(10)\n\t"
    "v_mfma_f32_32x32x16_bf16 v[128:143], a[96:99], a[0:3], 0\n\t"
    "v_fma_f32 v216, v168, s63, v224\n\t"
    "v_fma_f32 v217, v169, s63, v224\n\t"
    "v_exp_f32 v216, v216\n\t"
    "v_exp_f32 v217, v217\n\t"
    "v_mfma_f32_32x32x16_bf16 v[144:159], a[96:99], a[48:51], 0\n\t"
    "v_fma_f32 v218, v184, s63, v225\n\t"
    "v_fma_f32 v219, v185, s63, v225\n\t"
    "v_exp_f32 v218, v218\n\t"
    "s_waitcnt lgkmcnt(9)\n\t"
    "v_mfma_f32_32x32x16_bf16 v[128:143], a[100:103], a[4:7], v[128:143]\n\t"
    "v_exp_f32 v219, v219\n\t"
    "v_cvt_pk_bf16_f32 v208, v216, v217\n\t"
    "v_add_f32 v228, v228, v216\n\t"
    "v_add_f32 v228, v228, v217\n\t"
    "v_fma_f32 v216, v170, s63, v224\n\t"
    "v_mfma_f32_32x32x16_bf16 v[144:159], a[100:103], a[52:55], v[144:159]\n\t"
    "v_fma_f32 v217, v171, s63, v224\n\t"
    "s_waitcnt vmcnt(4) lgkmcnt(0)\n\t"
    "s_barrier\n\t"
    "v_mfma_f32_32x32x16_bf16 v[128:143], a[104:107], a[8:11], v[128:143]\n\t"
    "ds_read_b64_tr_b16 a[96:97], v234 offset:24576\n\t"
    "ds_read_b64_tr_b16 a[98:99], v234 offset:27648\n\t"
    "s_mov_b32 m0, s49\n\t"
    "s_nop 0\n\t"
    "buffer_load_dwordx4 v238, s[20:23], s34 offen lds\n\t"
    "v_mfma_f32_32x32x16_bf16 v[144:159], a[104:107], a[56:59], v[144:159]\n\t"
    "ds_read_b64_tr_b16 a[100:101], v234 offset:25088\n\t"
    "ds_read_b64_tr_b16 a[102:103], v234 offset:28160\n\t"
    "v_exp_f32 v216, v216\n\t"
    "v_exp_f32 v217, v217\n\t"
    "v_mfma_f32_32x32x16_bf16 v[128:143], a[108:111], a[12:15], v[128:143]\n\t"
    "ds_read_b64_tr_b16 a[104:105], v234 offset:25600\n\t"
    "ds_read_b64_tr_b16 a[106:107], v234 offset:28672\n\t"
    "buffer_load_dwordx4 v239, s[20:23], s34 offen offset:1024 lds\n\t"
    "v_cvt_pk_bf16_f32 v212, v218, v219\n\t"
    "v_mfma_f32_32x32x16_bf16 v[144:159], a[108:111], a[60:63], v[144:159]\n\t"
    "ds_read_b64_tr_b16 a[108:109], v234 offset:26112\n\t"
    "ds_read_b64_tr_b16 a[110:111], v234 offset:29184\n\t"
    "v_add_f32 v229, v229, v218\n\t"
    "v_add_f32 v229, v229, v219\n\t"
    "v_fma_f32 v218, v186, s63, v225\n\t"
    "v_fma_f32 v219, v187, s63, v225\n\t"
    "v_mfma_f32_32x32x16_bf16 v[128:143], a[112:115], a[16:19], v[128:143]\n\t"
    "buffer_load_dwordx4 v240, s[20:23], s34 offen offset:2048 lds\n\t"
    "s_add_u32 s34, s34, 12288\n\t"
    "v_exp_f32 v218, v218\n\t"
    "v_mfma_f32_32x32x16_bf16 v[144:159], a[112:115], a[64:67], v[144:159]\n\t"
    "ds_read_b64_tr_b16 a[112:113], v234 offset:26624\n\t"
    "ds_read_b64_tr_b16 a[114:115], v234 offset:29696\n\t"
    "v_exp_f32 v219, v219\n\t"
    "v_cvt_pk_bf16_f32 v209, v216, v217\n\t"
    "v_add_f32 v228, v228, v216\n\t"
    "v_mfma_f32_32x32x16_bf16 v[128:143], a[116:119], a[20:23], v[128:143]\n\t"
    "s_add_u32 m0, s49, 12288\n\t"
    "s_nop 0\n\t"
    "buffer_load_dwordx4 v238, s[20:23], s35 offen lds\n\t"
    "v_add_f32 v228, v228, v217\n\t"
    "v_fma_f32 v216, v172, s63, v224\n\t"
    "v_mfma_f32_32x32x16_bf16 v[144:159], a[116:119], a[68:71], v[144:159]\n\t"
    "ds_read_b64_tr_b16 a[116:117], v234 offset:27136\n\t"
    "ds_read_b64_tr_b16 a[118:119], v234 offset:30208\n\t"
    "v_fma_f32 v217, v173, s63, v224\n\t"
    "v_exp_f32 v216, v216\n\t"
    "v_mfma_f32_32x32x16_bf16 v[128:143], a[120:123], a[24:27], v[128:143]\n\t"
    "buffer_load_dwordx4 v239, s[20:23], s35 offen offset:1024 lds\n\t"
    "v_exp_f32 v217, v217\n\t"
    "v_cvt_pk_bf16_f32 v213, v218, v219\n\t"
    "v_add_f32 v229, v229, v218\n\t"
    "v_add_f32 v229, v229, v219\n\t"
    "v_mfma_f32_32x32x16_bf16 v[144:159], a[120:123], a[72:75], v[144:159]\n\t"
    "ds_read_b64_tr_b16 a[120:121], v234 offset:30720\n\t"
    "ds_read_b64_tr_b16 a[122:123], v234 offset:33792\n\t"
    "v_fma_f32 v218, v188, s63, v225\n\t"
    "v_fma_f32 v219, v189, s63, v225\n\t"
    "v_mfma_f32_32x32x16_bf16 v[128:143], a[124:127], a[28:31], v[128:143]\n\t"
    "buffer_load_dwordx4 v240, s[20:23], s35 offen offset:2048 lds\n\t"
    "s_add_u32 s35, s35, 12288\n\t"
    "v_exp_f32 v218, v218\n\t"
    "v_exp_f32 v219, v219\n\t"
    "v_mfma_f32_32x32x16_bf16 v[144:159], a[124:127], a[76:79], v[144:159]\n\t"
    "ds_read_b64_tr_b16 a[124:125], v234 offset:31232\n\t"
    "ds_read_b64_tr_b16 a[126:127], v234 offset:34304\n\t"
    "v_cvt_pk_bf16_f32 v210, v216, v217\n\t"
    "v_add_f32 v228, v228, v216\n\t"
    "v_add_f32 v228, v228, v217\n\t"
    "v_mfma_f32_32x32x16_bf16 v[128:143], a[128:131], a[32:35], v[128:143]\n\t"
    "s_mov_b32 m0, s46\n\t"
    "s_nop 0\n\t"
    "buffer_load_dwordx4 v235, s[16:19], s32 offen lds\n\t"
    "v_fma_f32 v216, v174, s63, v224\n\t"
    "v_fma_f32 v217, v175, s63, v224\n\t"
    "v_mfma_f32_32x32x16_bf16 v[144:159], a[128:131], a[80:83], v[144:159]\n\t"
    "ds_read_b64_tr_b16 a[128:129], v234 offset:31744\n\t"
    "ds_read_b64_tr_b16 a[130:131], v234 offset:34816\n\t"
    "v_exp_f32 v216, v216\n\t"
    "v_exp_f32 v217, v217\n\t"
    "v_mfma_f32_32x32x16_bf16 v[128:143], a[132:135], a[36:39], v[128:143]\n\t"
    "buffer_load_dwordx4 v236, s[16:19], s32 offen offset:1024 lds\n\t"
    "v_cvt_pk_bf16_f32 v214, v218, v219\n\t"
    "v_add_f32 v229, v229, v218\n\t"
    "v_add_f32 v229, v229, v219\n\t"
    "v_fma_f32 v218, v190, s63, v225\n\t"
    "v_mfma_f32_32x32x16_bf16 v[144:159], a[132:135], a[84:87], v[144:159]\n\t"
    "ds_read_b64_tr_b16 a[132:133], v234 offset:32256\n\t"
    "ds_read_b64_tr_b16 a[134:135], v234 offset:35328\n\t"
    "v_fma_f32 v219, v191, s63, v225\n\t"
    "v_exp_f32 v218, v218\n\t"
    "v_mfma_f32_32x32x16_bf16 v[128:143], a[136:139], a[40:43], v[128:143]\n\t"
    "buffer_load_dwordx4 v237, s[16:19], s32 offen offset:2048 lds\n\t"
    "s_add_u32 s32, s32, 12288\n\t"
    "v_exp_f32 v219, v219\n\t"
    "v_cvt_pk_bf16_f32 v211, v216, v217\n\t"
    "v_mfma_f32_32x32x16_bf16 v[144:159], a[136:139], a[88:91], v[144:159]\n\t"
    "ds_read_b64_tr_b16 a[136:137], v234 offset:32768\n\t"
    "ds_read_b64_tr_b16 a[138:139], v234 offset:35840\n\t"
    "v_add_f32 v228, v228, v216\n\t"
    "v_add_f32 v228, v228, v217\n\t"
    "s_nop 0\n\t"
    "v_mfma_f32_32x32x16_bf16 v[128:143], a[140:143], a[44:47], v[128:143]\n\t"
    "s_add_u32 m0, s46, 12288\n\t"
    "s_nop 0\n\t"
    "buffer_load_dwordx4 v235, s[16:19], s33 offen lds\n\t"
    "v_cvt_pk_bf16_f32 v215, v218, v219\n\t"
    "v_add_f32 v229, v229, v218\n\t"
    "v_add_f32 v229, v229, v219\n\t"
    "v_mfma_f32_32x32x16_bf16 v[144:159], a[140:143], a[92:95], v[144:159]\n\t"
    "ds_read_b64_tr_b16 a[140:141], v234 offset:33280\n\t"
    "ds_read_b64_tr_b16 a[142:143], v234 offset:36352\n\t"
    "v_add_f32 v251, v228, v229\n\t"
    "v_cmp_nge_f32 vcc, s64, v251\n\t"
    "s_cbranch_vccnz .Ld_stub17\n\t"
    ".Ld_back17:\n\t"
    "s_waitcnt lgkmcnt(12)\n\t"
    "v_mfma_f32_32x32x16_bf16 v[0:15], a[96:99], v[200:203], v[0:15]\n\t"
    "buffer_load_dwordx4 v236, s[16:19], s33 offen offset:1024 lds\n\t"
    "v_add_f32 v226, v226, v228\n\t"
    "v_add_f32 v227, v227, v229\n\t"
    "v_mfma_f32_32x32x16_bf16 v[96:111], a[96:99], v[204:207], v[96:111]\n\t"
    "ds_read_b128 a[96:99], v230 offset:24576\n\t"
    "v_mfma_f32_32x32x16_bf16 v[16:31], a[100:103], v[200:203], v[16:31]\n\t"
    "buffer_load_dwordx4 v237, s[16:19], s33 offen offset:2048 lds\n\t"
    "s_add_u32 s33, s33, 12288\n\t"
    "v_mfma_f32_32x32x16_bf16 v[112:127], a[100:103], v[204:207], v[112:127]\n\t"
    "ds_read_b128 a[100:103], v231 offset:24576\n\t"
    "ds_read_b128 v[220:223], v241 offset:4096\n\t"
    "v_fma_f32 v216, v128, s63, v224\n\t"
    "v_fma_f32 v217, v129, s63, v224\n\t"
    "v_mfma_f32_32x32x16_bf16 v[32:47], a[104:107], v[200:203], v[32:47]\n\t"
    "v_exp_f32 v216, v216\n\t"
    "v_exp_f32 v217, v217\n\t"
    "v_fma_f32 v218, v144, s63, v225\n\t"
    "v_mfma_f32_32x32x16_bf16 a[144:159], a[104:107], v[204:207], a[144:159]\n\t"
    "ds_read_b128 a[104:107], v232 offset:24576\n\t"
    "v_fma_f32 v219, v145, s63, v225\n\t"
    "v_exp_f32 v218, v218\n\t"
    "v_mfma_f32_32x32x16_bf16 v[48:63], a[108:111], v[200:203], v[48:63]\n\t"
    "v_exp_f32 v219, v219\n\t"
    "v_cvt_pk_bf16_f32 v192, v216, v217\n\t"
    "v_add_f32 v228, v216, v217\n\t"
    "v_mfma_f32_32x32x16_bf16 a[160:175], a[108:111], v[204:207], a[160:175]\n\t"
    "s_waitcnt lgkmcnt(1)\n\t"
    "buffer_store_dwordx4 v[220:223], v248, s[28:31], 0 offen offset:128 nt\n\t"
    "ds_read_b128 a[108:111], v233 offset:24576\n\t"
    "v_fma_f32 v216, v130, s63, v224\n\t"
    "v_fma_f32 v217, v131, s63, v224\n\t"
    "v_mfma_f32_32x32x16_bf16 v[64:79], a[112:115], v[200:203], v[64:79]\n\t"
    "v_exp_f32 v216, v216\n\t"
    "v_exp_f32 v217, v217\n\t"
    "v_cvt_pk_bf16_f32 v196, v218, v219\n\t"
    "v_mfma_f32_32x32x16_bf16 a[176:191], a[112:115], v[204:207], a[176:191]\n\t"
    "ds_read_b128 a[112:115], v230 offset:25600\n\t"
    "ds_read_b128 v[252:255], v241 offset:5120\n\t"
    "v_add_f32 v229, v218, v219\n\t"
    "v_fma_f32 v218, v146, s63, v225\n\t"
    "v_mfma_f32_32x32x16_bf16 v[80:95], a[116:119], v[200:203], v[80:95]\n\t"
    "v_fma_f32 v219, v147, s63, v225\n\t"
    "v_exp_f32 v218, v218\n\t"
    "v_exp_f32 v219, v219\n\t"
    "v_mfma_f32_32x32x16_bf16 a[192:207], a[116:119], v[204:207], a[192:207]\n\t"
    "ds_read_b128 a[116:119], v231 offset:25600\n\t"
    "v_cvt_pk_bf16_f32 v193, v216, v217\n\t"
    "v_add_f32 v228, v228, v216\n\t"
    "v_add_f32 v228, v228, v217\n\t"
    "v_mfma_f32_32x32x16_bf16 v[0:15], a[120:123], v[208:211], v[0:15]\n\t"
    "v_fma_f32 v216, v132, s63, v224\n\t"
    "v_fma_f32 v217, v133, s63, v224\n\t"
    "v_exp_f32 v216, v216\n\t"
    "v_mfma_f32_32x32x16_bf16 v[96:111], a[120:123], v[212:215], v[96:111]\n\t"
    "s_waitcnt lgkmcnt(1)\n\t"
    "buffer_store_dwordx4 v[252:255], v249, s[28:31], 0 offen offset:128 nt\n\t"
    "ds_read_b128 a[120:123], v232 offset:25600\n\t"
    "v_exp_f32 v217, v217\n\t"
    "v_cvt_pk_bf16_f32 v197, v218, v219\n\t"
    "v_mfma_f32_32x32x16_bf16 v[16:31], a[124:127], v[208:211], v[16:31]\n\t"
    "v_add_f32 v229, v229, v218\n\t"
    "v_add_f32 v229, v229, v219\n\t"
    "v_fma_f32 v218, v148, s63, v225\n\t"
    "v_fma_f32 v219, v149, s63, v225\n\t"
    "v_mfma_f32_32x32x16_bf16 v[112:127], a[124:127], v[212:215], v[112:127]\n\t"
    "ds_read_b128 a[124:127], v233 offset:25600\n\t"
    "ds_read_b128 v[220:223], v241 offset:6144\n\t"
    "v_exp_f32 v218, v218\n\t"
    "v_mfma_f32_32x32x16_bf16 v[32:47], a[128:131], v[208:211], v[32:47]\n\t"
    "v_exp_f32 v219, v219\n\t"
    "v_cvt_pk_bf16_f32 v194, v216, v217\n\t"
    "v_add_f32 v228, v228, v216\n\t"
    "v_add_f32 v228, v228, v217\n\t"
    "v_mfma_f32_32x32x16_bf16 a[144:159], a[128:131], v[212:215], a[144:159]\n\t"
    "ds_read_b128 a[128:131], v230 offset:26624\n\t"
    "v_fma_f32 v216, v134, s63, v224\n\t"
    "v_fma_f32 v217, v135, s63, v224\n\t"
    "v_exp_f32 v216, v216\n\t"
    "v_mfma_f32_32x32x16_bf16 v[48:63], a[132:135], v[208:211], v[48:63]\n\t"
    "v_exp_f32 v217, v217\n\t"
    "v_cvt_pk_bf16_f32 v198, v218, v219\n\t"
    "v_add_f32 v229, v229, v218\n\t"
    "v_mfma_f32_32x32x16_bf16 a[160:175], a[132:135], v[212:215], a[160:175]\n\t"
    "s_waitcnt lgkmcnt(1)\n\t"
    "buffer_store_dwordx4 v[220:223], v248, s[28:31], s86 offen offset:128 nt\n\t"
    "ds_read_b128 a[132:135], v231 offset:26624\n\t"
    "v_add_f32 v229, v229, v219\n\t"
    "v_fma_f32 v218, v150, s63, v225\n\t"
    "v_mfma_f32_32x32x16_bf16 v[64:79], a[136:139], v[208:211], v[64:79]\n\t"
    "v_fma_f32 v219, v151, s63, v225\n\t"
    "v_exp_f32 v218, v218\n\t"
    "v_exp_f32 v219, v219\n\t"
    "v_mfma_f32_32x32x16_bf16 a[176:191], a[136:139], v[212:215], a[176:191]\n\t"
    "ds_read_b128 a[136:139], v232 offset:26624\n\t"
    "ds_read_b128 v[252:255], v241 offset:7168\n\t"
    "v_cvt_pk_bf16_f32 v195, v216, v217\n\t"
    "v_add_f32 v228, v228, v216\n\t"
    "v_mfma_f32_32x32x16_bf16 v[80:95], a[140:143], v[208:211], v[80:95]\n\t"
    "ds_write_b128 v244, a[224:227] offset:4096\n\t"
    "v_add_f32 v228, v228, v217\n\t"
    "s_nop 0\n\t"
    "v_cvt_pk_bf16_f32 v199, v218, v219\n\t"
    "v_mfma_f32_32x32x16_bf16 a[192:207], a[140:143], v[212:215], a[192:207]\n\t"
    "ds_read_b128 a[140:143], v233 offset:26624\n\t"
    "v_add_f32 v229, v229, v218\n\t"
    "v_add_f32 v229, v229, v219\n\t"
    "v_mfma_f32_32x32x16_bf16 v[160:175], a[96:99], a[0:3], 0\n\t"
    "ds_write_b128 v245, a[228:231] offset:4096\n\t"
    "v_fma_f32 v216, v136, s63, v224\n\t"
    "v_fma_f32 v217, v137, s63, v224\n\t"
    "v_exp_f32 v216, v216\n\t"
    "v_mfma_f32_32x32x16_bf16 v[176:191], a[96:99], a[48:51], 0\n\t"
    "s_waitcnt lgkmcnt(3)\n\t"
    "buffer_store_dwordx4 v[252:255], v249, s[28:31], s86 offen offset:128 nt\n\t"
    "v_exp_f32 v217, v217\n\t"
    "v_fma_f32 v218, v152, s63, v225\n\t"
    "v_fma_f32 v219, v153, s63, v225\n\t"
    "v_mfma_f32_32x32x16_bf16 v[160:175], a[100:103], a[4:7], v[160:175]\n\t"
    "ds_write_b128 v246, a[232:235] offset:4096\n\t"
    "v_exp_f32 v218, v218\n\t"
    "v_exp_f32 v219, v219\n\t"
    "v_cvt_pk_bf16_f32 v208, v216, v217\n\t"
    "v_mfma_f32_32x32x16_bf16 v[176:191], a[100:103], a[52:55], v[176:191]\n\t"
    "v_add_f32 v228, v228, v216\n\t"
    "v_add_f32 v228, v228, v217\n\t"
    "s_waitcnt vmcnt(4) lgkmcnt(0)\n\t"
    "s_barrier\n\t"
    "v_mfma_f32_32x32x16_bf16 v[160:175], a[104:107], a[8:11], v[160:175]\n\t"
    "ds_read_b64_tr_b16 a[96:97], v234 offset:0\n\t"
    "ds_read_b64_tr_b16 a[98:99], v234 offset:3072\n\t"
    "s_add_u32 m0, s49, 24576\n\t"
    "s_nop 0\n\t"
    "buffer_load_dwordx4 v238, s[20:23], s34 offen lds\n\t"
    "ds_write_b128 v247, a[236:239] offset:4096\n\t"
    "v_mfma_f32_32x32x16_bf16 v[176:191], a[104:107], a[56:59], v[176:191]\n\t"
    "ds_read_b64_tr_b16 a[100:101], v234 offset:512\n\t"
    "ds_read_b64_tr_b16 a[102:103], v234 offset:3584\n\t"
    "v_fma_f32 v216, v138, s63, v224\n\t"
    "v_fma_f32 v217, v139, s63, v224\n\t"
    "v_mfma_f32_32x32x16_bf16 v[160:175], a[108:111], a[12:15], v[160:175]\n\t"
    "ds_read_b64_tr_b16 a[104:105], v234 offset:1024\n\t"
    "ds_read_b64_tr_b16 a[106:107], v234 offset:4096\n\t"
    "buffer_load_dwordx4 v239, s[20:23], s34 offen offset:1024 lds\n\t"
    "v_exp_f32 v216, v216\n\t"
    "v_exp_f32 v217, v217\n\t"
    "v_mfma_f32_32x32x16_bf16 v[176:191], a[108:111], a[60:63], v[176:191]\n\t"
    "ds_read_b64_tr_b16 a[108:109], v234 offset:1536\n\t"
    "ds_read_b64_tr_b16 a[110:111], v234 offset:4608\n\t"
    "v_cvt_pk_bf16_f32 v212, v218, v219\n\t"
    "v_add_f32 v229, v229, v218\n\t"
    "v_add_f32 v229, v229, v219\n\t"
    "v_mfma_f32_32x32x16_bf16 v[160:175], a[112:115], a[16:19], v[160:175]\n\t"
    "buffer_load_dwordx4 v240, s[20:23], s34 offen offset:2048 lds\n\t"
    "s_add_u32 s34, s34, 12288\n\t"
    "v_fma_f32 v218, v154, s63, v225\n\t"
    "v_fma_f32 v219, v155, s63, v225\n\t"
    "v_exp_f32 v218, v218\n\t"
    "v_mfma_f32_32x32x16_bf16 v[176:191], a[112:115], a[64:67], v[176:191]\n\t"
    "ds_read_b64_tr_b16 a[112:113], v234 offset:2048\n\t"
    "ds_read_b64_tr_b16 a[114:115], v234 offset:5120\n\t"
    "v_exp_f32 v219, v219\n\t"
    "v_mfma_f32_32x32x16_bf16 v[160:175], a[116:119], a[20:23], v[160:175]\n\t"
    "s_add_u32 m0, s49, 36864\n\t"
    "s_nop 0\n\t"
    "buffer_load_dwordx4 v238, s[20:23], s35 offen lds\n\t"
    "v_cvt_pk_bf16_f32 v209, v216, v217\n\t"
    "v_add_f32 v228, v228, v216\n\t"
    "v_add_f32 v228, v228, v217\n\t"
    "v_mfma_f32_32x32x16_bf16 v[176:191], a[116:119], a[68:71], v[176:191]\n\t"
    "ds_read_b64_tr_b16 a[116:117], v234 offset:2560\n\t"
    "ds_read_b64_tr_b16 a[118:119], v234 offset:5632\n\t"
    "v_fma_f32 v216, v140, s63, v224\n\t"
    "v_fma_f32 v217, v141, s63, v224\n\t"
    "v_exp_f32 v216, v216\n\t"
    "v_mfma_f32_32x32x16_bf16 v[160:175], a[120:123], a[24:27], v[160:175]\n\t"
    "buffer_load_dwordx4 v239, s[20:23], s35 offen offset:1024 lds\n\t"
    "v_exp_f32 v217, v217\n\t"
    "v_cvt_pk_bf16_f32 v213, v218, v219\n\t"
    "v_add_f32 v229, v229, v218\n\t"
    "v_mfma_f32_32x32x16_bf16 v[176:191], a[120:123], a[72:75], v[176:191]\n\t"
    "ds_read_b64_tr_b16 a[120:121], v234 offset:6144\n\t"
    "ds_read_b64_tr_b16 a[122:123], v234 offset:9216\n\t"
    "v_add_f32 v229, v229, v219\n\t"
    "v_fma_f32 v218, v156, s63, v225\n\t"
    "v_fma_f32 v219, v157, s63, v225\n\t"
    "v_mfma_f32_32x32x16_bf16 v[160:175], a[124:127], a[28:31], v[160:175]\n\t"
    "buffer_load_dwordx4 v240, s[20:23], s35 offen offset:2048 lds\n\t"
    "s_add_u32 s35, s35, 12288\n\t"
    "v_exp_f32 v218, v218\n\t"
    "v_exp_f32 v219, v219\n\t"
    "v_mfma_f32_32x32x16_bf16 v[176:191], a[124:127], a[76:79], v[176:191]\n\t"
    "ds_read_b64_tr_b16 a[124:125], v234 offset:6656\n\t"
    "ds_read_b64_tr_b16 a[126:127], v234 offset:9728\n\t"
    "v_cvt_pk_bf16_f32 v210, v216, v217\n\t"
    "v_add_f32 v228, v228, v216\n\t"
    "v_add_f32 v228, v228, v217\n\t"
    "v_mfma_f32_32x32x16_bf16 v[160:175], a[128:131], a[32:35], v[160:175]\n\t"
    "s_add_u32 m0, s46, 24576\n\t"
    "s_nop 0\n\t"
    "buffer_load_dwordx4 v235, s[16:19], s32 offen lds\n\t"
    "v_fma_f32 v216, v142, s63, v224\n\t"
    "v_fma_f32 v217, v143, s63, v224\n\t"
    "v_mfma_f32_32x32x16_bf16 v[176:191], a[128:131], a[80:83], v[176:191]\n\t"
    "ds_read_b64_tr_b16 a[128:129], v234 offset:7168\n\t"
    "ds_read_b64_tr_b16 a[130:131], v234 offset:10240\n\t"
    "v_exp_f32 v216, v216\n\t"
    "v_exp_f32 v217, v217\n\t"
    "v_mfma_f32_32x32x16_bf16 v[160:175], a[132:135], a[36:39], v[160:175]\n\t"
    "buffer_load_dwordx4 v236, s[16:19], s32 offen offset:1024 lds\n\t"
    "v_cvt_pk_bf16_f32 v214, v218, v219\n\t"
    "v_add_f32 v229, v229, v218\n\t"
    "v_add_f32 v229, v229, v219\n\t"
    "v_fma_f32 v218, v158, s63, v225\n\t"
    "v_fma_f32 v219, v159, s63, v225\n\t"
    "v_mfma_f32_32x32x16_bf16 v[176:191], a[132:135], a[84:87], v[176:191]\n\t"
    "ds_read_b64_tr_b16 a[132:133], v234 offset:7680\n\t"
    "ds_read_b64_tr_b16 a[134:135], v234 offset:10752\n\t"
    "v_exp_f32 v218, v218\n\t"
    "v_mfma_f32_32x32x16_bf16 v[160:175], a[136:139], a[40:43], v[160:175]\n\t"
    "buffer_load_dwordx4 v237, s[16:19], s32 offen offset:2048 lds\n\t"
    "s_add_u32 s32, s32, 12288\n\t"
    "v_exp_f32 v219, v219\n\t"
    "v_cvt_pk_bf16_f32 v211, v216, v217\n\t"
    "v_add_f32 v228, v228, v216\n\t"
    "v_mfma_f32_32x32x16_bf16 v[176:191], a[136:139], a[88:91], v[176:191]\n\t"
    "ds_read_b64_tr_b16 a[136:137], v234 offset:8192\n\t"
    "ds_read_b64_tr_b16 a[138:139], v234 offset:11264\n\t"
    "v_add_f32 v228, v228, v217\n\t"
    "s_nop 0\n\t"
    "v_cvt_pk_bf16_f32 v215, v218, v219\n\t"
    "v_add_f32 v229, v229, v218\n\t"
    "v_mfma_f32_32x32x16_bf16 v[160:175], a[140:143], a[44:47], v[160:175]\n\t"
    "s_add_u32 m0, s46, 36864\n\t"
    "s_nop 0\n\t"
    "buffer_load_dwordx4 v235, s[16:19], s33 offen lds\n\t"
    "s_waitcnt vmcnt(26)\n\t"
    "ds_read_b128 a[208:211], v244 offset:0\n\t"
    "v_mfma_f32_32x32x16_bf16 v[176:191], a[140:143], a[92:95], v[176:191]\n\t"
    "ds_read_b64_tr_b16 a[140:141], v234 offset:8704\n\t"
    "ds_read_b64_tr_b16 a[142:143], v234 offset:11776\n\t"
    "v_add_f32 v229, v229, v219\n\t"
    "v_add_f32 v251, v228, v229\n\t"
    "v_cmp_nge_f32 vcc, s64, v251\n\t"
    "s_cbranch_vccnz .Ld_stub18\n\t"
    ".Ld_back18:\n\t"
    "s_waitcnt lgkmcnt(13)\n\t"
    "v_mfma_f32_32x32x16_bf16 v[0:15], a[96:99], v[192:195], v[0:15]\n\t"
    "buffer_load_dwordx4 v236, s[16:19], s33 offen offset:1024 lds\n\t"
    "ds_read_b128 a[212:215], v245 offset:0\n\t"
    "v_add_f32 v226, v226, v228\n\t"
    "v_add_f32 v227, v227, v229\n\t"
    "v_mfma_f32_32x32x16_bf16 v[96:111], a[96:99], v[196:199], v[96:111]\n\t"
    "ds_read_b128 a[96:99], v230 offset:0\n\t"
    "v_mfma_f32_32x32x16_bf16 v[16:31], a[100:103], v[192:195], v[16:31]\n\t"
    "buffer_load_dwordx4 v237, s[16:19], s33 offen offset:2048 lds\n\t"
    "s_add_u32 s33, s33, 12288\n\t"
    "ds_read_b128 a[216:219], v246 offset:0\n\t"
    "v_mfma_f32_32x32x16_bf16 v[112:127], a[100:103], v[196:199], v[112:127]\n\t"
    "ds_read_b128 a[100:103], v231 offset:0\n\t"
    "v_fma_f32 v216, v160, s63, v224\n\t"
    "v_fma_f32 v217, v161, s63, v224\n\t"
    "v_exp_f32 v216, v216\n\t"
    "v_mfma_f32_32x32x16_bf16 v[32:47], a[104:107], v[192:195], v[32:47]\n\t"
    "ds_read_b128 a[220:223], v247 offset:0\n\t"
    "v_exp_f32 v217, v217\n\t"
    "v_fma_f32 v218, v176, s63, v225\n\t"
    "v_mfma_f32_32x32x16_bf16 a[144:159], a[104:107], v[196:199], a[144:159]\n\t"
    "ds_read_b128 a[104:107], v232 offset:0\n\t"
    "v_fma_f32 v219, v177, s63, v225\n\t"
    "v_exp_f32 v218, v218\n\t"
    "v_mfma_f32_32x32x16_bf16 v[48:63], a[108:111], v[192:195], v[48:63]\n\t"
    "v_exp_f32 v219, v219\n\t"
    "v_cvt_pk_bf16_f32 v200, v216, v217\n\t"
    "v_add_f32 v228, v216, v217\n\t"
    "v_fma_f32 v216, v162, s63, v224\n\t"
    "v_mfma_f32_32x32x16_bf16 a[160:175], a[108:111], v[196:199], a[160:175]\n\t"
    "ds_read_b128 a[108:111], v233 offset:0\n\t"
    "s_waitcnt lgkmcnt(2)\n\t"
    "s_add_u32 m0, s61, 0\n\t"
    "s_nop 0\n\t"
    "buffer_load_dwordx4 v248, s[24:27], 0 offen nt lds\n\t"
    "v_mfma_f32_32x32x16_bf16 v[64:79], a[112:115], v[192:195], v[64:79]\n\t"
    "v_fma_f32 v217, v163, s63, v224\n\t"
    "v_exp_f32 v216, v216\n\t"
    "v_exp_f32 v217, v217\n\t"
    "v_mfma_f32_32x32x16_bf16 a[176:191], a[112:115], v[196:199], a[176:191]\n\t"
    "ds_read_b128 a[112:115], v230 offset:1024\n\t"
    "v_cvt_pk_bf16_f32 v204, v218, v219\n\t"
    "v_add_f32 v229, v218, v219\n\t"
    "v_fma_f32 v218, v178, s63, v225\n\t"
    "v_mfma_f32_32x32x16_bf16 v[80:95], a[116:119], v[192:195], v[80:95]\n\t"
    "v_fma_f32 v219, v179, s63, v225\n\t"
    "v_exp_f32 v218, v218\n\t"
    "v_exp_f32 v219, v219\n\t"
    "v_mfma_f32_32x32x16_bf16 a[192:207], a[116:119], v[196:199], a[192:207]\n\t"
    "ds_read_b128 a[116:119], v231 offset:1024\n\t"
    "v_cvt_pk_bf16_f32 v201, v216, v217\n\t"
    "v_add_f32 v228, v228, v216\n\t"
    "v_add_f32 v228, v228, v217\n\t"
    "v_mfma_f32_32x32x16_bf16 v[0:15], a[120:123], v[208:211], v[0:15]\n\t"
    "v_fma_f32 v216, v164, s63, v224\n\t"
    "v_fma_f32 v217, v165, s63, v224\n\t"
    "v_exp_f32 v216, v216\n\t"
    "v_mfma_f32_32x32x16_bf16 v[96:111], a[120:123], v[212:215], v[96:111]\n\t"
    "ds_read_b128 a[120:123], v232 offset:1024\n\t"
    "s_add_u32 m0, s61, 1024\n\t"
    "s_nop 0\n\t"
    "buffer_load_dwordx4 v249, s[24:27], 0 offen nt lds\n\t"
    "v_mfma_f32_32x32x16_bf16 v[16:31], a[124:127], v[208:211], v[16:31]\n\t"
    "v_exp_f32 v217, v217\n\t"
    "v_cvt_pk_bf16_f32 v205, v218, v219\n\t"
    "v_add_f32 v229, v229, v218\n\t"
    "v_add_f32 v229, v229, v219\n\t"
    "v_mfma_f32_32x32x16_bf16 v[112:127], a[124:127], v[212:215], v[112:127]\n\t"
    "ds_read_b128 a[124:127], v233 offset:1024\n\t"
    "v_fma_f32 v218, v180, s63, v225\n\t"
    "v_fma_f32 v219, v181, s63, v225\n\t"
    "v_exp_f32 v218, v218\n\t"
    "v_mfma_f32_32x32x16_bf16 v[32:47], a[128:131], v[208:211], v[32:47]\n\t"
    "v_exp_f32 v219, v219\n\t"
    "v_cvt_pk_bf16_f32 v202, v216, v217\n\t"
    "v_add_f32 v228, v228, v216\n\t"
    "v_mfma_f32_32x32x16_bf16 a[144:159], a[128:131], v[212:215], a[144:159]\n\t"
    "ds_read_b128 a[128:131], v230 offset:2048\n\t"
    "v_add_f32 v228, v228, v217\n\t"
    "v_fma_f32 v216, v166, s63, v224\n\t"
    "v_fma_f32 v217, v167, s63, v224\n\t"
    "v_mfma_f32_32x32x16_bf16 v[48:63], a[132:135], v[208:211], v[48:63]\n\t"
    "v_exp_f32 v216, v216\n\t"
    "v_exp_f32 v217, v217\n\t"
    "v_cvt_pk_bf16_f32 v206, v218, v219\n\t"
    "v_mfma_f32_32x32x16_bf16 a[160:175], a[132:135], v[212:215], a[160:175]\n\t"
    "ds_read_b128 a[132:135], v231 offset:2048\n\t"
    "v_add_f32 v229, v229, v218\n\t"
    "v_add_f32 v229, v229, v219\n\t"
    "v_fma_f32 v218, v182, s63, v225\n\t"
    "v_fma_f32 v219, v183, s63, v225\n\t"
    "v_mfma_f32_32x32x16_bf16 v[64:79], a[136:139], v[208:211], v[64:79]\n\t"
    "v_exp_f32 v218, v218\n\t"
    "v_exp_f32 v219, v219\n\t"
    "v_mfma_f32_32x32x16_bf16 a[176:191], a[136:139], v[212:215], a[176:191]\n\t"
    "ds_read_b128 a[136:139], v232 offset:2048\n\t"
    "v_cvt_pk_bf16_f32 v203, v216, v217\n\t"
    "v_add_f32 v228, v228, v216\n\t"
    "v_add_f32 v228, v228, v217\n\t"
    "s_nop 0\n\t"
    "v_mfma_f32_32x32x16_bf16 v[80:95], a[140:143], v[208:211], v[80:95]\n\t"
    "s_add_u32 m0, s61, 2048\n\t"
    "s_nop 0\n\t"
    "buffer_load_dwordx4 v248, s[24:27], s86 offen nt lds\n\t"
    "v_cvt_pk_bf16_f32 v207, v218, v219\n\t"
    "v_mfma_f32_32x32x16_bf16 a[192:207], a[140:143], v[212:215], a[192:207]\n\t"
    "ds_read_b128 a[140:143], v233 offset:2048\n\t"
    "v_add_f32 v229, v229, v218\n\t"
    "v_add_f32 v229, v229, v219\n\t"
    "v_mfma_f32_32x32x16_bf16 v[128:143], a[96:99], a[0:3], 0\n\t"
    "v_fma_f32 v216, v168, s63, v224\n\t"
    "v_fma_f32 v217, v169, s63, v224\n\t"
    "v_exp_f32 v216, v216\n\t"
    "v_exp_f32 v217, v217\n\t"
    "v_mfma_f32_32x32x16_bf16 v[144:159], a[96:99], a[48:51], 0\n\t"
    "v_fma_f32 v218, v184, s63, v225\n\t"
    "v_fma_f32 v219, v185, s63, v225\n\t"
    "v_exp_f32 v218, v218\n\t"
    "s_waitcnt lgkmcnt(9)\n\t"
    "v_mfma_f32_32x32x16_bf16 v[128:143], a[100:103], a[4:7], v[128:143]\n\t"
    "v_exp_f32 v219, v219\n\t"
    "v_cvt_pk_bf16_f32 v208, v216, v217\n\t"
    "v_add_f32 v228, v228, v216\n\t"
    "v_add_f32 v228, v228, v217\n\t"
    "v_fma_f32 v216, v170, s63, v224\n\t"
    "v_mfma_f32_32x32x16_bf16 v[144:159], a[100:103], a[52:55], v[144:159]\n\t"
    "v_fma_f32 v217, v171, s63, v224\n\t"
    "s_waitcnt vmcnt(3) lgkmcnt(0)\n\t"
    "s_barrier\n\t"
    "v_mfma_f32_32x32x16_bf16 v[128:143], a[104:107], a[8:11], v[128:143]\n\t"
    "ds_read_b64_tr_b16 a[96:97], v234 offset:24576\n\t"
    "ds_read_b64_tr_b16 a[98:99], v234 offset:27648\n\t"
    "s_mov_b32 m0, s49\n\t"
    "s_nop 0\n\t"
    "buffer_load_dwordx4 v238, s[20:23], s34 offen lds\n\t"
    "v_mfma_f32_32x32x16_bf16 v[144:159], a[104:107], a[56:59], v[144:159]\n\t"
    "ds_read_b64_tr_b16 a[100:101], v234 offset:25088\n\t"
    "ds_read_b64_tr_b16 a[102:103], v234 offset:28160\n\t"
    "v_exp_f32 v216, v216\n\t"
    "v_exp_f32 v217, v217\n\t"
    "v_mfma_f32_32x32x16_bf16 v[128:143], a[108:111], a[12:15], v[128:143]\n\t"
    "ds_read_b64_tr_b16 a[104:105], v234 offset:25600\n\t"
    "ds_read_b64_tr_b16 a[106:107], v234 offset:28672\n\t"
    "buffer_load_dwordx4 v239, s[20:23], s34 offen offset:1024 lds\n\t"
    "v_cvt_pk_bf16_f32 v212, v218, v219\n\t"
    "v_mfma_f32_32x32x16_bf16 v[144:159], a[108:111], a[60:63], v[144:159]\n\t"
    "ds_read_b64_tr_b16 a[108:109], v234 offset:26112\n\t"
    "ds_read_b64_tr_b16 a[110:111], v234 offset:29184\n\t"
    "v_add_f32 v229, v229, v218\n\t"
    "v_add_f32 v229, v229, v219\n\t"
    "v_fma_f32 v218, v186, s63, v225\n\t"
    "v_fma_f32 v219, v187, s63, v225\n\t"
    "v_mfma_f32_32x32x16_bf16 v[128:143], a[112:115], a[16:19], v[128:143]\n\t"
    "buffer_load_dwordx4 v240, s[20:23], s34 offen offset:2048 lds\n\t"
    "s_add_u32 s34, s34, 12288\n\t"
    "v_exp_f32 v218, v218\n\t"
    "v_mfma_f32_32x32x16_bf16 v[144:159], a[112:115], a[64:67], v[144:159]\n\t"
    "ds_read_b64_tr_b16 a[112:113], v234 offset:26624\n\t"
    "ds_read_b64_tr_b16 a[114:115], v234 offset:29696\n\t"
    "v_exp_f32 v219, v219\n\t"
    "v_cvt_pk_bf16_f32 v209, v216, v217\n\t"
    "v_add_f32 v228, v228, v216\n\t"
    "v_mfma_f32_32x32x16_bf16 v[128:143], a[116:119], a[20:23], v[128:143]\n\t"
    "s_add_u32 m0, s49, 12288\n\t"
    "s_nop 0\n\t"
    "buffer_load_dwordx4 v238, s[20:23], s35 offen lds\n\t"
    "v_add_f32 v228, v228, v217\n\t"
    "v_fma_f32 v216, v172, s63, v224\n\t"
    "v_mfma_f32_32x32x16_bf16 v[144:159], a[116:119], a[68:71], v[144:159]\n\t"
    "ds_read_b64_tr_b16 a[116:117], v234 offset:27136\n\t"
    "ds_read_b64_tr_b16 a[118:119], v234 offset:30208\n\t"
    "v_fma_f32 v217, v173, s63, v224\n\t"
    "v_exp_f32 v216, v216\n\t"
    "v_mfma_f32_32x32x16_bf16 v[128:143], a[120:123], a[24:27], v[128:143]\n\t"
    "buffer_load_dwordx4 v239, s[20:23], s35 offen offset:1024 lds\n\t"
    "v_exp_f32 v217, v217\n\t"
    "v_cvt_pk_bf16_f32 v213, v218, v219\n\t"
    "v_add_f32 v229, v229, v218\n\t"
    "v_add_f32 v229, v229, v219\n\t"
    "v_mfma_f32_32x32x16_bf16 v[144:159], a[120:123], a[72:75], v[144:159]\n\t"
    "ds_read_b64_tr_b16 a[120:121], v234 offset:30720\n\t"
    "ds_read_b64_tr_b16 a[122:123], v234 offset:33792\n\t"
    "v_fma_f32 v218, v188, s63, v225\n\t"
    "v_fma_f32 v219, v189, s63, v225\n\t"
    "v_mfma_f32_32x32x16_bf16 v[128:143], a[124:127], a[28:31], v[128:143]\n\t"
    "buffer_load_dwordx4 v240, s[20:23], s35 offen offset:2048 lds\n\t"
    "s_add_u32 s35, s35, 12288\n\t"
    "v_exp_f32 v218, v218\n\t"
    "v_exp_f32 v219, v219\n\t"
    "v_mfma_f32_32x32x16_bf16 v[144:159], a[124:127], a[76:79], v[144:159]\n\t"
    "ds_read_b64_tr_b16 a[124:125], v234 offset:31232\n\t"
    "ds_read_b64_tr_b16 a[126:127], v234 offset:34304\n\t"
    "v_cvt_pk_bf16_f32 v210, v216, v217\n\t"
    "v_add_f32 v228, v228, v216\n\t"
    "v_add_f32 v228, v228, v217\n\t"
    "v_mfma_f32_32x32x16_bf16 v[128:143], a[128:131], a[32:35], v[128:143]\n\t"
    "s_mov_b32 m0, s46\n\t"
    "s_nop 0\n\t"
    "buffer_load_dwordx4 v235, s[16:19], s32 offen lds\n\t"
    "v_fma_f32 v216, v174, s63, v224\n\t"
    "v_fma_f32 v217, v175, s63, v224\n\t"
    "v_mfma_f32_32x32x16_bf16 v[144:159], a[128:131], a[80:83], v[144:159]\n\t"
    "ds_read_b64_tr_b16 a[128:129], v234 offset:31744\n\t"
    "ds_read_b64_tr_b16 a[130:131], v234 offset:34816\n\t"
    "v_exp_f32 v216, v216\n\t"
    "v_exp_f32 v217, v217\n\t"
    "v_mfma_f32_32x32x16_bf16 v[128:143], a[132:135], a[36:39], v[128:143]\n\t"
    "buffer_load_dwordx4 v236, s[16:19], s32 offen offset:1024 lds\n\t"
    "v_cvt_pk_bf16_f32 v214, v218, v219\n\t"
    "v_add_f32 v229, v229, v218\n\t"
    "v_add_f32 v229, v229, v219\n\t"
    "v_fma_f32 v218, v190, s63, v225\n\t"
    "v_mfma_f32_32x32x16_bf16 v[144:159], a[132:135], a[84:87], v[144:159]\n\t"
    "ds_read_b64_tr_b16 a[132:133], v234 offset:32256\n\t"
    "ds_read_b64_tr_b16 a[134:135], v234 offset:35328\n\t"
    "v_fma_f32 v219, v191, s63, v225\n\t"
    "v_exp_f32 v218, v218\n\t"
    "v_mfma_f32_32x32x16_bf16 v[128:143], a[136:139], a[40:43], v[128:143]\n\t"
    "buffer_load_dwordx4 v237, s[16:19], s32 offen offset:2048 lds\n\t"
    "s_add_u32 s32, s32, 12288\n\t"
    "v_exp_f32 v219, v219\n\t"
    "v_cvt_pk_bf16_f32 v211, v216, v217\n\t"
    "v_mfma_f32_32x32x16_bf16 v[144:159], a[136:139], a[88:91], v[144:159]\n\t"
    "ds_read_b64_tr_b16 a[136:137], v234 offset:32768\n\t"
    "ds_read_b64_tr_b16 a[138:139], v234 offset:35840\n\t"
    "v_add_f32 v228, v228, v216\n\t"
    "v_add_f32 v228, v228, v217\n\t"
    "s_nop 0\n\t"
    "v_mfma_f32_32x32x16_bf16 v[128:143], a[140:143], a[44:47], v[128:143]\n\t"
    "s_add_u32 m0, s46, 12288\n\t"
    "s_nop 0\n\t"
    "buffer_load_dwordx4 v235, s[16:19], s33 offen lds\n\t"
    "v_cvt_pk_bf16_f32 v215, v218, v219\n\t"
    "v_add_f32 v229, v229, v218\n\t"
    "v_add_f32 v229, v229, v219\n\t"
    "v_mfma_f32_32x32x16_bf16 v[144:159], a[140:143], a[92:95], v[144:159]\n\t"
    "ds_read_b64_tr_b16 a[140:141], v234 offset:33280\n\t"
    "ds_read_b64_tr_b16 a[142:143], v234 offset:36352\n\t"
    "v_add_f32 v251, v228, v229\n\t"
    "v_cmp_nge_f32 vcc, s64, v251\n\t"
    "s_cbranch_vccnz .Ld_stub19\n\t"
    ".Ld_back19:\n\t"
    "s_waitcnt lgkmcnt(12)\n\t"
    "v_mfma_f32_32x32x16_bf16 v[0:15], a[96:99], v[200:203], v[0:15]\n\t"
    "buffer_load_dwordx4 v236, s[16:19], s33 offen offset:1024 lds\n\t"
    "v_add_f32 v226, v226, v228\n\t"
    "v_add_f32 v227, v227, v229\n\t"
    "v_mfma_f32_32x32x16_bf16 v[96:111], a[96:99], v[204:207], v[96:111]\n\t"
    "ds_read_b128 a[96:99], v230 offset:24576\n\t"
    "v_mfma_f32_32x32x16_bf16 v[16:31], a[100:103], v[200:203], v[16:31]\n\t"
    "buffer_load_dwordx4 v237, s[16:19], s33 offen offset:2048 lds\n\t"
    "s_add_u32 s33, s33, 12288\n\t"
    "v_mfma_f32_32x32x16_bf16 v[112:127], a[100:103], v[204:207], v[112:127]\n\t"
    "ds_read_b128 a[100:103], v231 offset:24576\n\t"
    "v_fma_f32 v216, v128, s63, v224\n\t"
    "v_fma_f32 v217, v129, s63, v224\n\t"
    "v_exp_f32 v216, v216\n\t"
    "v_mfma_f32_32x32x16_bf16 v[32:47], a[104:107], v[200:203], v[32:47]\n\t"
    "s_add_u32 m0, s61, 3072\n\t"
    "s_nop 0\n\t"
    "buffer_load_dwordx4 v249, s[24:27], s86 offen nt lds\n\t"
    "v_mfma_f32_32x32x16_bf16 a[144:159], a[104:107], v[204:207], a[144:159]\n\t"
    "ds_read_b128 a[104:107], v232 offset:24576\n\t"
    "v_exp_f32 v217, v217\n\t"
    "v_fma_f32 v218, v144, s63, v225\n\t"
    "v_fma_f32 v219, v145, s63, v225\n\t"
    "v_mfma_f32_32x32x16_bf16 v[48:63], a[108:111], v[200:203], v[48:63]\n\t"
    "ds_read_b128 v[220:223], v241 offset:4096\n\t"
    "v_exp_f32 v218, v218\n\t"
    "v_exp_f32 v219, v219\n\t"
    "v_mfma_f32_32x32x16_bf16 a[160:175], a[108:111], v[204:207], a[160:175]\n\t"
    "ds_read_b128 a[108:111], v233 offset:24576\n\t"
    "v_cvt_pk_bf16_f32 v192, v216, v217\n\t"
    "v_add_f32 v228, v216, v217\n\t"
    "v_fma_f32 v216, v130, s63, v224\n\t"
    "v_mfma_f32_32x32x16_bf16 v[64:79], a[112:115], v[200:203], v[64:79]\n\t"
    "v_fma_f32 v217, v131, s63, v224\n\t"
    "v_exp_f32 v216, v216\n\t"
    "v_exp_f32 v217, v217\n\t"
    "v_mfma_f32_32x32x16_bf16 a[176:191], a[112:115], v[204:207], a[176:191]\n\t"
    "ds_read_b128 a[112:115], v230 offset:25600\n\t"
    "v_cvt_pk_bf16_f32 v196, v218, v219\n\t"
    "v_add_f32 v229, v218, v219\n\t"
    "v_fma_f32 v218, v146, s63, v225\n\t"
    "v_mfma_f32_32x32x16_bf16 v[80:95], a[116:119], v[200:203], v[80:95]\n\t"
    "s_waitcnt lgkmcnt(2)\n\t"
    "buffer_store_dwordx4 v[220:223], v248, s[28:31], s87 offen offset:128 nt\n\t"
    "v_fma_f32 v219, v147, s63, v225\n\t"
    "v_exp_f32 v218, v218\n\t"
    "v_mfma_f32_32x32x16_bf16 a[192:207], a[116:119], v[204:207], a[192:207]\n\t"
    "ds_read_b128 a[116:119], v231 offset:25600\n\t"
    "v_exp_f32 v219, v219\n\t"
    "v_cvt_pk_bf16_f32 v193, v216, v217\n\t"
    "v_add_f32 v228, v228, v216\n\t"
    "v_mfma_f32_32x32x16_bf16 v[0:15], a[120:123], v[208:211], v[0:15]\n\t"
    "ds_read_b128 v[252:255], v241 offset:5120\n\t"
    "v_add_f32 v228, v228, v217\n\t"
    "v_fma_f32 v216, v132, s63, v224\n\t"
    "v_fma_f32 v217, v133, s63, v224\n\t"
    "v_mfma_f32_32x32x16_bf16 v[96:111], a[120:123], v[212:215], v[96:111]\n\t"
    "ds_read_b128 a[120:123], v232 offset:25600\n\t"
    "v_exp_f32 v216, v216\n\t"
    "v_exp_f32 v217, v217\n\t"
    "v_mfma_f32_32x32x16_bf16 v[16:31], a[124:127], v[208:211], v[16:31]\n\t"
    "v_cvt_pk_bf16_f32 v197, v218, v219\n\t"
    "v_add_f32 v229, v229, v218\n\t"
    "v_add_f32 v229, v229, v219\n\t"
    "v_fma_f32 v218, v148, s63, v225\n\t"
    "v_mfma_f32_32x32x16_bf16 v[112:127], a[124:127], v[212:215], v[112:127]\n\t"
    "ds_read_b128 a[124:127], v233 offset:25600\n\t"
    "v_fma_f32 v219, v149, s63, v225\n\t"
    "v_exp_f32 v218, v218\n\t"
    "v_mfma_f32_32x32x16_bf16 v[32:47], a[128:131], v[208:211], v[32:47]\n\t"
    "s_waitcnt lgkmcnt(2)\n\t"
    "buffer_store_dwordx4 v[252:255], v249, s[28:31], s87 offen offset:128 nt\n\t"
    "v_exp_f32 v219, v219\n\t"
    "v_cvt_pk_bf16_f32 v194, v216, v217\n\t"
    "v_add_f32 v228, v228, v216\n\t"
    "v_mfma_f32_32x32x16_bf16 a[144:159], a[128:131], v[212:215], a[144:159]\n\t"
    "ds_read_b128 a[128:131], v230 offset:26624\n\t"
    "v_add_f32 v228, v228, v217\n\t"
    "v_fma_f32 v216, v134, s63, v224\n\t"
    "v_fma_f32 v217, v135, s63, v224\n\t"
    "v_mfma_f32_32x32x16_bf16 v[48:63], a[132:135], v[208:211], v[48:63]\n\t"
    "ds_read_b128 v[220:223], v241 offset:6144\n\t"
    "v_exp_f32 v216, v216\n\t"
    "v_exp_f32 v217, v217\n\t"
    "v_mfma_f32_32x32x16_bf16 a[160:175], a[132:135], v[212:215], a[160:175]\n\t"
    "ds_read_b128 a[132:135], v231 offset:26624\n\t"
    "v_cvt_pk_bf16_f32 v198, v218, v219\n\t"
    "v_add_f32 v229, v229, v218\n\t"
    "v_add_f32 v229, v229, v219\n\t"
    "v_mfma_f32_32x32x16_bf16 v[64:79], a[136:139], v[208:211], v[64:79]\n\t"
    "v_fma_f32 v218, v150, s63, v225\n\t"
    "v_fma_f32 v219, v151, s63, v225\n\t"
    "v_exp_f32 v218, v218\n\t"
    "v_mfma_f32_32x32x16_bf16 a[176:191], a[136:139], v[212:215], a[176:191]\n\t"
    "ds_read_b128 a[136:139], v232 offset:26624\n\t"
    "v_exp_f32 v219, v219\n\t"
    "v_cvt_pk_bf16_f32 v195, v216, v217\n\t"
    "v_add_f32 v228, v228, v216\n\t"
    "v_mfma_f32_32x32x16_bf16 v[80:95], a[140:143], v[208:211], v[80:95]\n\t"
    "s_waitcnt lgkmcnt(2)\n\t"
    "buffer_store_dwordx4 v[220:223], v248, s[28:31], s43 offen offset:128 nt\n\t"
    "v_add_f32 v228, v228, v217\n\t"
    "s_nop 0\n\t"
    "v_cvt_pk_bf16_f32 v199, v218, v219\n\t"
    "v_mfma_f32_32x32x16_bf16 a[192:207], a[140:143], v[212:215], a[192:207]\n\t"
    "ds_read_b128 a[140:143], v233 offset:26624\n\t"
    "v_add_f32 v229, v229, v218\n\t"
    "v_add_f32 v229, v229, v219\n\t"
    "v_mfma_f32_32x32x16_bf16 v[160:175], a[96:99], a[0:3], 0\n\t"
    "v_fma_f32 v216, v136, s63, v224\n\t"
    "v_fma_f32 v217, v137, s63, v224\n\t"
    "v_exp_f32 v216, v216\n\t"
    "v_exp_f32 v217, v217\n\t"
    "v_mfma_f32_32x32x16_bf16 v[176:191], a[96:99], a[48:51], 0\n\t"
    "v_fma_f32 v218, v152, s63, v225\n\t"
    "v_fma_f32 v219, v153, s63, v225\n\t"
    "v_exp_f32 v218, v218\n\t"
    "v_mfma_f32_32x32x16_bf16 v[160:175], a[100:103], a[4:7], v[160:175]\n\t"
    "v_exp_f32 v219, v219\n\t"
    "v_cvt_pk_bf16_f32 v208, v216, v217\n\t"
    "v_add_f32 v228, v228, v216\n\t"
    "v_add_f32 v228, v228, v217\n\t"
    "v_fma_f32 v216, v138, s63, v224\n\t"
    "v_mfma_f32_32x32x16_bf16 v[176:191], a[100:103], a[52:55], v[176:191]\n\t"
    "v_fma_f32 v217, v139, s63, v224\n\t"
    "s_waitcnt vmcnt(4) lgkmcnt(0)\n\t"
    "s_barrier\n\t"
    "v_mfma_f32_32x32x16_bf16 v[160:175], a[104:107], a[8:11], v[160:175]\n\t"
    "ds_read_b64_tr_b16 a[96:97], v234 offset:0\n\t"
    "ds_read_b64_tr_b16 a[98:99], v234 offset:3072\n\t"
    "s_add_u32 m0, s49, 24576\n\t"
    "s_nop 0\n\t"
    "buffer_load_dwordx4 v238, s[20:23], s34 offen lds\n\t"
    "v_mfma_f32_32x32x16_bf16 v[176:191], a[104:107], a[56:59], v[176:191]\n\t"
    "ds_read_b64_tr_b16 a[100:101], v234 offset:512\n\t"
    "ds_read_b64_tr_b16 a[102:103], v234 offset:3584\n\t"
    "v_exp_f32 v216, v216\n\t"
    "v_exp_f32 v217, v217\n\t"
    "v_mfma_f32_32x32x16_bf16 v[160:175], a[108:111], a[12:15], v[160:175]\n\t"
    "ds_read_b64_tr_b16 a[104:105], v234 offset:1024\n\t"
    "ds_read_b64_tr_b16 a[106:107], v234 offset:4096\n\t"
    "buffer_load_dwordx4 v239, s[20:23], s34 offen offset:1024 lds\n\t"
    "v_cvt_pk_bf16_f32 v212, v218, v219\n\t"
    "v_mfma_f32_32x32x16_bf16 v[176:191], a[108:111], a[60:63], v[176:191]\n\t"
    "ds_read_b64_tr_b16 a[108:109], v234 offset:1536\n\t"
    "ds_read_b64_tr_b16 a[110:111], v234 offset:4608\n\t"
    "v_add_f32 v229, v229, v218\n\t"
    "v_add_f32 v229, v229, v219\n\t"
    "v_fma_f32 v218, v154, s63, v225\n\t"
    "v_fma_f32 v219, v155, s63, v225\n\t"
    "v_mfma_f32_32x32x16_bf16 v[160:175], a[112:115], a[16:19], v[160:175]\n\t"
    "buffer_load_dwordx4 v240, s[20:23], s34 offen offset:2048 lds\n\t"
    "s_add_u32 s34, s34, 12288\n\t"
    "v_exp_f32 v218, v218\n\t"
    "v_mfma_f32_32x32x16_bf16 v[176:191], a[112:115], a[64:67], v[176:191]\n\t"
    "ds_read_b64_tr_b16 a[112:113], v234 offset:2048\n\t"
    "ds_read_b64_tr_b16 a[114:115], v234 offset:5120\n\t"
    "v_exp_f32 v219, v219\n\t"
    "v_cvt_pk_bf16_f32 v209, v216, v217\n\t"
    "v_add_f32 v228, v228, v216\n\t"
    "v_mfma_f32_32x32x16_bf16 v[160:175], a[116:119], a[20:23], v[160:175]\n\t"
    "s_add_u32 m0, s49, 36864\n\t"
    "s_nop 0\n\t"
    "buffer_load_dwordx4 v238, s[20:23], s35 offen lds\n\t"
    "v_add_f32 v228, v228, v217\n\t"
    "v_fma_f32 v216, v140, s63, v224\n\t"
    "v_mfma_f32_32x32x16_bf16 v[176:191], a[116:119], a[68:71], v[176:191]\n\t"
    "ds_read_b64_tr_b16 a[116:117], v234 offset:2560\n\t"
    "ds_read_b64_tr_b16 a[118:119], v234 offset:5632\n\t"
    "v_fma_f32 v217, v141, s63, v224\n\t"
    "v_exp_f32 v216, v216\n\t"
    "v_mfma_f32_32x32x16_bf16 v[160:175], a[120:123], a[24:27], v[160:175]\n\t"
    "buffer_load_dwordx4 v239, s[20:23], s35 offen offset:1024 lds\n\t"
    "v_exp_f32 v217, v217\n\t"
    "v_cvt_pk_bf16_f32 v213, v218, v219\n\t"
    "v_add_f32 v229, v229, v218\n\t"
    "v_add_f32 v229, v229, v219\n\t"
    "v_mfma_f32_32x32x16_bf16 v[176:191], a[120:123], a[72:75], v[176:191]\n\t"
    "ds_read_b64_tr_b16 a[120:121], v234 offset:6144\n\t"
    "ds_read_b64_tr_b16 a[122:123], v234 offset:9216\n\t"
    "v_fma_f32 v218, v156, s63, v225\n\t"
    "v_fma_f32 v219, v157, s63, v225\n\t"
    "v_mfma_f32_32x32x16_bf16 v[160:175], a[124:127], a[28:31], v[160:175]\n\t"
    "buffer_load_dwordx4 v240, s[20:23], s35 offen offset:2048 lds\n\t"
    "s_add_u32 s35, s35, 12288\n\t"
    "v_exp_f32 v218, v218\n\t"
    "v_exp_f32 v219, v219\n\t"
    "v_mfma_f32_32x32x16_bf16 v[176:191], a[124:127], a[76:79], v[176:191]\n\t"
    "ds_read_b64_tr_b16 a[124:125], v234 offset:6656\n\t"
    "ds_read_b64_tr_b16 a[126:127], v234 offset:9728\n\t"
    "v_cvt_pk_bf16_f32 v210, v216, v217\n\t"
    "v_add_f32 v228, v228, v216\n\t"
    "v_add_f32 v228, v228, v217\n\t"
    "v_mfma_f32_32x32x16_bf16 v[160:175], a[128:131], a[32:35], v[160:175]\n\t"
    "s_add_u32 m0, s46, 24576\n\t"
    "s_nop 0\n\t"
    "buffer_load_dwordx4 v235, s[16:19], s32 offen lds\n\t"
    "v_fma_f32 v216, v142, s63, v224\n\t"
    "v_fma_f32 v217, v143, s63, v224\n\t"
    "v_mfma_f32_32x32x16_bf16 v[176:191], a[128:131], a[80:83], v[176:191]\n\t"
    "ds_read_b64_tr_b16 a[128:129], v234 offset:7168\n\t"
    "ds_read_b64_tr_b16 a[130:131], v234 offset:10240\n\t"
    "v_exp_f32 v216, v216\n\t"
    "v_exp_f32 v217, v217\n\t"
    "v_mfma_f32_32x32x16_bf16 v[160:175], a[132:135], a[36:39], v[160:175]\n\t"
    "buffer_load_dwordx4 v236, s[16:19], s32 offen offset:1024 lds\n\t"
    "v_cvt_pk_bf16_f32 v214, v218, v219\n\t"
    "v_add_f32 v229, v229, v218\n\t"
    "v_add_f32 v229, v229, v219\n\t"
    "v_fma_f32 v218, v158, s63, v225\n\t"
    "v_mfma_f32_32x32x16_bf16 v[176:191], a[132:135], a[84:87], v[176:191]\n\t"
    "ds_read_b64_tr_b16 a[132:133], v234 offset:7680\n\t"
    "ds_read_b64_tr_b16 a[134:135], v234 offset:10752\n\t"
    "v_fma_f32 v219, v159, s63, v225\n\t"
    "v_exp_f32 v218, v218\n\t"
    "v_mfma_f32_32x32x16_bf16 v[160:175], a[136:139], a[40:43], v[160:175]\n\t"
    "buffer_load_dwordx4 v237, s[16:19], s32 offen offset:2048 lds\n\t"
    "s_add_u32 s32, s32, 12288\n\t"
    "v_exp_f32 v219, v219\n\t"
    "v_cvt_pk_bf16_f32 v211, v216, v217\n\t"
    "v_mfma_f32_32x32x16_bf16 v[176:191], a[136:139], a[88:91], v[176:191]\n\t"
    "ds_read_b64_tr_b16 a[136:137], v234 offset:8192\n\t"
    "ds_read_b64_tr_b16 a[138:139], v234 offset:11264\n\t"
    "v_add_f32 v228, v228, v216\n\t"
    "v_add_f32 v228, v228, v217\n\t"
    "s_nop 0\n\t"
    "v_mfma_f32_32x32x16_bf16 v[160:175], a[140:143], a[44:47], v[160:175]\n\t"
    "s_add_u32 m0, s46, 36864\n\t"
    "s_nop 0\n\t"
    "buffer_load_dwordx4 v235, s[16:19], s33 offen lds\n\t"
    "v_cvt_pk_bf16_f32 v215, v218, v219\n\t"
    "v_add_f32 v229, v229, v218\n\t"
    "v_add_f32 v229, v229, v219\n\t"
    "v_mfma_f32_32x32x16_bf16 v[176:191], a[140:143], a[92:95], v[176:191]\n\t"
    "ds_read_b64_tr_b16 a[140:141], v234 offset:8704\n\t"
    "ds_read_b64_tr_b16 a[142:143], v234 offset:11776\n\t"
    "v_add_f32 v251, v228, v229\n\t"
    "v_cmp_nge_f32 vcc, s64, v251\n\t"
    "s_cbranch_vccnz .Ld_stub20\n\t"
    ".Ld_back20:\n\t"
    "s_waitcnt lgkmcnt(12)\n\t"
    "v_mfma_f32_32x32x16_bf16 v[0:15], a[96:99], v[192:195], v[0:15]\n\t"
    "buffer_load_dwordx4 v236, s[16:19], s33 offen offset:1024 lds\n\t"
    "ds_read_b128 v[252:255], v241 offset:7168\n\t"
    "v_add_f32 v226, v226, v228\n\t"
    "v_add_f32 v227, v227, v229\n\t"
    "v_mfma_f32_32x32x16_bf16 v[96:111], a[96:99], v[196:199], v[96:111]\n\t"
    "ds_read_b128 a[96:99], v230 offset:0\n\t"
    "v_mfma_f32_32x32x16_bf16 v[16:31], a[100:103], v[192:195], v[16:31]\n\t"
    "buffer_load_dwordx4 v237, s[16:19], s33 offen offset:2048 lds\n\t"
    "s_add_u32 s33, s33, 12288\n\t"
    "v_mfma_f32_32x32x16_bf16 v[112:127], a[100:103], v[196:199], v[112:127]\n\t"
    "ds_read_b128 a[100:103], v231 offset:0\n\t"
    "v_fma_f32 v216, v160, s63, v224\n\t"
    "v_fma_f32 v217, v161, s63, v224\n\t"
    "v_exp_f32 v216, v216\n\t"
    "v_mfma_f32_32x32x16_bf16 v[32:47], a[104:107], v[192:195], v[32:47]\n\t"
    "s_waitcnt lgkmcnt(2)\n\t"
    "buffer_store_dwordx4 v[252:255], v249, s[28:31], s43 offen offset:128 nt\n\t"
    "v_exp_f32 v217, v217\n\t"
    "v_fma_f32 v218, v176, s63, v225\n\t"
    "v_mfma_f32_32x32x16_bf16 a[144:159], a[104:107], v[196:199], a[144:159]\n\t"
    "ds_read_b128 a[104:107], v232 offset:0\n\t"
    "v_fma_f32 v219, v177, s63, v225\n\t"
    "v_exp_f32 v218, v218\n\t"
    "v_mfma_f32_32x32x16_bf16 v[48:63], a[108:111], v[192:195], v[48:63]\n\t"
    "v_exp_f32 v219, v219\n\t"
    "v_cvt_pk_bf16_f32 v200, v216, v217\n\t"
    "v_add_f32 v228, v216, v217\n\t"
    "v_mfma_f32_32x32x16_bf16 a[160:175], a[108:111], v[196:199], a[160:175]\n\t"
    "ds_read_b128 a[108:111], v233 offset:0\n\t"
    "v_fma_f32 v216, v162, s63, v224\n\t"
    "v_fma_f32 v217, v163, s63, v224\n\t"
    "v_exp_f32 v216, v216\n\t"
    "v_mfma_f32_32x32x16_bf16 v[64:79], a[112:115], v[192:195], v[64:79]\n\t"
    "v_exp_f32 v217, v217\n\t"
    "v_cvt_pk_bf16_f32 v204, v218, v219\n\t"
    "v_add_f32 v229, v218, v219\n\t"
    "v_mfma_f32_32x32x16_bf16 a[176:191], a[112:115], v[196:199], a[176:191]\n\t"
    "ds_read_b128 a[112:115], v230 offset:1024\n\t"
    "v_fma_f32 v218, v178, s63, v225\n\t"
    "v_fma_f32 v219, v179, s63, v225\n\t"
    "v_exp_f32 v218, v218\n\t"
    "v_mfma_f32_32x32x16_bf16 v[80:95], a[116:119], v[192:195], v[80:95]\n\t"
    "v_exp_f32 v219, v219\n\t"
    "v_cvt_pk_bf16_f32 v201, v216, v217\n\t"
    "v_mfma_f32_32x32x16_bf16 a[192:207], a[116:119], v[196:199], a[192:207]\n\t"
    "ds_read_b128 a[116:119], v231 offset:1024\n\t"
    "v_add_f32 v228, v228, v216\n\t"
    "v_add_f32 v228, v228, v217\n\t"
    "v_fma_f32 v216, v164, s63, v224\n\t"
    "v_fma_f32 v217, v165, s63, v224\n\t"
    "v_mfma_f32_32x32x16_bf16 v[0:15], a[120:123], v[208:211], v[0:15]\n\t"
    "v_exp_f32 v216, v216\n\t"
    "v_exp_f32 v217, v217\n\t"
    "v_mfma_f32_32x32x16_bf16 v[96:111], a[120:123], v[212:215], v[96:111]\n\t"
    "ds_read_b128 a[120:123], v232 offset:1024\n\t"
    "s_add_u32 m0, s61, 3968\n\t"
    "s_nop 0\n\t"
    "buffer_load_dwordx4 v248, s[24:27], s87 offen offset:128 nt lds\n\t"
    "v_mfma_f32_32x32x16_bf16 v[16:31], a[124:127], v[208:211], v[16:31]\n\t"
    "v_cvt_pk_bf16_f32 v205, v218, v219\n\t"
    "v_add_f32 v229, v229, v218\n\t"
    "v_add_f32 v229, v229, v219\n\t"
    "v_fma_f32 v218, v180, s63, v225\n\t"
    "v_fma_f32 v219, v181, s63, v225\n\t"
    "v_mfma_f32_32x32x16_bf16 v[112:127], a[124:127], v[212:215], v[112:127]\n\t"
    "ds_read_b128 a[124:127], v233 offset:1024\n\t"
    "v_exp_f32 v218, v218\n\t"
    "v_mfma_f32_32x32x16_bf16 v[32:47], a[128:131], v[208:211], v[32:47]\n\t"
    "v_exp_f32 v219, v219\n\t"
    "v_cvt_pk_bf16_f32 v202, v216, v217\n\t"
    "v_add_f32 v228, v228, v216\n\t"
    "v_add_f32 v228, v228, v217\n\t"
    "v_mfma_f32_32x32x16_bf16 a[144:159], a[128:131], v[212:215], a[144:159]\n\t"
    "ds_read_b128 a[128:131], v230 offset:2048\n\t"
    "v_fma_f32 v216, v166, s63, v224\n\t"
    "v_fma_f32 v217, v167, s63, v224\n\t"
    "v_exp_f32 v216, v216\n\t"
    "v_mfma_f32_32x32x16_bf16 v[48:63], a[132:135], v[208:211], v[48:63]\n\t"
    "v_exp_f32 v217, v217\n\t"
    "v_cvt_pk_bf16_f32 v206, v218, v219\n\t"
    "v_add_f32 v229, v229, v218\n\t"
    "v_mfma_f32_32x32x16_bf16 a[160:175], a[132:135], v[212:215], a[160:175]\n\t"
    "ds_read_b128 a[132:135], v231 offset:2048\n\t"
    "s_add_u32 m0, s61, 4992\n\t"
    "s_nop 0\n\t"
    "buffer_load_dwordx4 v249, s[24:27], s87 offen offset:128 nt lds\n\t"
    "v_mfma_f32_32x32x16_bf16 v[64:79], a[136:139], v[208:211], v[64:79]\n\t"
    "v_add_f32 v229, v229, v219\n\t"
    "v_fma_f32 v218, v182, s63, v225\n\t"
    "v_fma_f32 v219, v183, s63, v225\n\t"
    "v_exp_f32 v218, v218\n\t"
    "v_mfma_f32_32x32x16_bf16 a[176:191], a[136:139], v[212:215], a[176:191]\n\t"
    "ds_read_b128 a[136:139], v232 offset:2048\n\t"
    "v_exp_f32 v219, v219\n\t"
    "v_cvt_pk_bf16_f32 v203, v216, v217\n\t"
    "v_mfma_f32_32x32x16_bf16 v[80:95], a[140:143], v[208:211], v[80:95]\n\t"
    "v_add_f32 v228, v228, v216\n\t"
    "v_add_f32 v228, v228, v217\n\t"
    "s_nop 0\n\t"
    "v_cvt_pk_bf16_f32 v207, v218, v219\n\t"
    "v_mfma_f32_32x32x16_bf16 a[192:207], a[140:143], v[212:215], a[192:207]\n\t"
    "ds_read_b128 a[140:143], v233 offset:2048\n\t"
    "v_add_f32 v229, v229, v218\n\t"
    "v_add_f32 v229, v229, v219\n\t"
    "s_waitcnt lgkmcnt(10)\n\t"
    "v_mfma_f32_32x32x16_bf16 v[128:143], a[96:99], a[0:3], 0\n\t"
    "v_fma_f32 v216, v168, s63, v224\n\t"
    "v_fma_f32 v217, v169, s63, v224\n\t"
    "v_exp_f32 v216, v216\n\t"
    "v_exp_f32 v217, v217\n\t"
    "v_mfma_f32_32x32x16_bf16 v[144:159], a[96:99], a[48:51], 0\n\t"
    "s_add_u32 m0, s61, 6016\n\t"
    "s_nop 0\n\t"
    "buffer_load_dwordx4 v248, s[24:27], s43 offen offset:128 nt lds\n\t"
    "v_fma_f32 v218, v184, s63, v225\n\t"
    "v_fma_f32 v219, v185, s63, v225\n\t"
    "s_waitcnt lgkmcnt(9)\n\t"
    "v_mfma_f32_32x32x16_bf16 v[128:143], a[100:103], a[4:7], v[128:143]\n\t"
    "v_exp_f32 v218, v218\n\t"
    "v_exp_f32 v219, v219\n\t"
    "v_cvt_pk_bf16_f32 v208, v216, v217\n\t"
    "v_mfma_f32_32x32x16_bf16 v[144:159], a[100:103], a[52:55], v[144:159]\n\t"
    "v_add_f32 v228, v228, v216\n\t"
    "s_waitcnt vmcnt(4) lgkmcnt(0)\n\t"
    "s_barrier\n\t"
    "v_mfma_f32_32x32x16_bf16 v[128:143], a[104:107], a[8:11], v[128:143]\n\t"
    "ds_read_b64_tr_b16 a[96:97], v234 offset:24576\n\t"
    "ds_read_b64_tr_b16 a[98:99], v234 offset:27648\n\t"
    "s_mov_b32 m0, s49\n\t"
    "s_nop 0\n\t"
    "buffer_load_dwordx4 v238, s[20:23], s34 offen lds\n\t"
    "v_add_f32 v228, v228, v217\n\t"
    "v_mfma_f32_32x32x16_bf16 v[144:159], a[104:107], a[56:59], v[144:159]\n\t"
    "ds_read_b64_tr_b16 a[100:101], v234 offset:25088\n\t"
    "ds_read_b64_tr_b16 a[102:103], v234 offset:28160\n\t"
    "v_fma_f32 v216, v170, s63, v224\n\t"
    "v_fma_f32 v217, v171, s63, v224\n\t"
    "v_exp_f32 v216, v216\n\t"
    "v_mfma_f32_32x32x16_bf16 v[128:143], a[108:111], a[12:15], v[128:143]\n\t"
    "ds_read_b64_tr_b16 a[104:105], v234 offset:25600\n\t"
    "ds_read_b64_tr_b16 a[106:107], v234 offset:28672\n\t"
    "buffer_load_dwordx4 v239, s[20:23], s34 offen offset:1024 lds\n\t"
    "v_exp_f32 v217, v217\n\t"
    "v_mfma_f32_32x32x16_bf16 v[144:159], a[108:111], a[60:63], v[144:159]\n\t"
    "ds_read_b64_tr_b16 a[108:109], v234 offset:26112\n\t"
    "ds_read_b64_tr_b16 a[110:111], v234 offset:29184\n\t"
    "v_cvt_pk_bf16_f32 v212, v218, v219\n\t"
    "v_add_f32 v229, v229, v218\n\t"
    "v_add_f32 v229, v229, v219\n\t"
    "v_mfma_f32_32x32x16_bf16 v[128:143], a[112:115], a[16:19], v[128:143]\n\t"
    "buffer_load_dwordx4 v240, s[20:23], s34 offen offset:2048 lds\n\t"
    "s_add_u32 s34, s34, 12288\n\t"
    "v_fma_f32 v218, v186, s63, v225\n\t"
    "v_fma_f32 v219, v187, s63, v225\n\t"
    "v_exp_f32 v218, v218\n\t"
    "v_mfma_f32_32x32x16_bf16 v[144:159], a[112:115], a[64:67], v[144:159]\n\t"
    "ds_read_b64_tr_b16 a[112:113], v234 offset:26624\n\t"
    "ds_read_b64_tr_b16 a[114:115], v234 offset:29696\n\t"
    "v_exp_f32 v219, v219\n\t"
    "v_mfma_f32_32x32x16_bf16 v[128:143], a[116:119], a[20:23], v[128:143]\n\t"
    "s_add_u32 m0, s49, 12288\n\t"
    "s_nop 0\n\t"
    "buffer_load_dwordx4 v238, s[20:23], s35 offen lds\n\t"
    "v_cvt_pk_bf16_f32 v209, v216, v217\n\t"
    "v_add_f32 v228, v228, v216\n\t"
    "v_add_f32 v228, v228, v217\n\t"
    "v_mfma_f32_32x32x16_bf16 v[144:159], a[116:119], a[68:71], v[144:159]\n\t"
    "ds_read_b64_tr_b16 a[116:117], v234 offset:27136\n\t"
    "ds_read_b64_tr_b16 a[118:119], v234 offset:30208\n\t"
    "v_fma_f32 v216, v172, s63, v224\n\t"
    "v_fma_f32 v217, v173, s63, v224\n\t"
    "v_exp_f32 v216, v216\n\t"
    "v_mfma_f32_32x32x16_bf16 v[128:143], a[120:123], a[24:27], v[128:143]\n\t"
    "buffer_load_dwordx4 v239, s[20:23], s35 offen offset:1024 lds\n\t"
    "v_exp_f32 v217, v217\n\t"
    "v_cvt_pk_bf16_f32 v213, v218, v219\n\t"
    "v_add_f32 v229, v229, v218\n\t"
    "v_mfma_f32_32x32x16_bf16 v[144:159], a[120:123], a[72:75], v[144:159]\n\t"
    "ds_read_b64_tr_b16 a[120:121], v234 offset:30720\n\t"
    "ds_read_b64_tr_b16 a[122:123], v234 offset:33792\n\t"
    "v_add_f32 v229, v229, v219\n\t"
    "v_fma_f32 v218, v188, s63, v225\n\t"
    "v_fma_f32 v219, v189, s63, v225\n\t"
    "v_mfma_f32_32x32x16_bf16 v[128:143], a[124:127], a[28:31], v[128:143]\n\t"
    "buffer_load_dwordx4 v240, s[20:23], s35 offen offset:2048 lds\n\t"
    "s_add_u32 s35, s35, 12288\n\t"
    "v_exp_f32 v218, v218\n\t"
    "v_exp_f32 v219, v219\n\t"
    "v_mfma_f32_32x32x16_bf16 v[144:159], a[124:127], a[76:79], v[144:159]\n\t"
    "ds_read_b64_tr_b16 a[124:125], v234 offset:31232\n\t"
    "ds_read_b64_tr_b16 a[126:127], v234 offset:34304\n\t"
    "v_cvt_pk_bf16_f32 v210, v216, v217\n\t"
    "v_add_f32 v228, v228, v216\n\t"
    "v_add_f32 v228, v228, v217\n\t"
    "v_mfma_f32_32x32x16_bf16 v[128:143], a[128:131], a[32:35], v[128:143]\n\t"
    "s_mov_b32 m0, s46\n\t"
    "s_nop 0\n\t"
    "buffer_load_dwordx4 v235, s[16:19], s32 offen lds\n\t"
    "v_fma_f32 v216, v174, s63, v224\n\t"
    "v_fma_f32 v217, v175, s63, v224\n\t"
    "v_mfma_f32_32x32x16_bf16 v[144:159], a[128:131], a[80:83], v[144:159]\n\t"
    "ds_read_b64_tr_b16 a[128:129], v234 offset:31744\n\t"
    "ds_read_b64_tr_b16 a[130:131], v234 offset:34816\n\t"
    "v_exp_f32 v216, v216\n\t"
    "v_exp_f32 v217, v217\n\t"
    "v_mfma_f32_32x32x16_bf16 v[128:143], a[132:135], a[36:39], v[128:143]\n\t"
    "buffer_load_dwordx4 v236, s[16:19], s32 offen offset:1024 lds\n\t"
    "v_cvt_pk_bf16_f32 v214, v218, v219\n\t"
    "v_add_f32 v229, v229, v218\n\t"
    "v_add_f32 v229, v229, v219\n\t"
    "v_fma_f32 v218, v190, s63, v225\n\t"
    "v_mfma_f32_32x32x16_bf16 v[144:159], a[132:135], a[84:87], v[144:159]\n\t"
    "ds_read_b64_tr_b16 a[132:133], v234 offset:32256\n\t"
    "ds_read_b64_tr_b16 a[134:135], v234 offset:35328\n\t"
    "v_fma_f32 v219, v191, s63, v225\n\t"
    "v_exp_f32 v218, v218\n\t"
    "v_mfma_f32_32x32x16_bf16 v[128:143], a[136:139], a[40:43], v[128:143]\n\t"
    "buffer_load_dwordx4 v237, s[16:19], s32 offen offset:2048 lds\n\t"
    "s_add_u32 s32, s32, 12288\n\t"
    "v_exp_f32 v219, v219\n\t"
    "v_cvt_pk_bf16_f32 v211, v216, v217\n\t"
    "v_mfma_f32_32x32x16_bf16 v[144:159], a[136:139], a[88:91], v[144:159]\n\t"
    "ds_read_b64_tr_b16 a[136:137], v234 offset:32768\n\t"
    "ds_read_b64_tr_b16 a[138:139], v234 offset:35840\n\t"
    "v_add_f32 v228, v228, v216\n\t"
    "v_add_f32 v228, v228, v217\n\t"
    "s_nop 0\n\t"
    "v_cvt_pk_bf16_f32 v215, v218, v219\n\t"
    "v_mfma_f32_32x32x16_bf16 v[128:143], a[140:143], a[44:47], v[128:143]\n\t"
    "s_add_u32 m0, s46, 12288\n\t"
    "s_nop 0\n\t"
    "buffer_load_dwordx4 v235, s[16:19], s33 offen lds\n\t"
    "v_add_f32 v229, v229, v218\n\t"
    "v_add_f32 v229, v229, v219\n\t"
    "v_mfma_f32_32x32x16_bf16 v[144:159], a[140:143], a[92:95], v[144:159]\n\t"
    "ds_read_b64_tr_b16 a[140:141], v234 offset:33280\n\t"
    "ds_read_b64_tr_b16 a[142:143], v234 offset:36352\n\t"
    "v_add_f32 v251, v228, v229\n\t"
    "v_cmp_nge_f32 vcc, s64, v251\n\t"
    "s_cbranch_vccnz .Ld_stub21\n\t"
    ".Ld_back21:\n\t"
    "s_waitcnt lgkmcnt(12)\n\t"
    "v_mfma_f32_32x32x16_bf16 v[0:15], a[96:99], v[200:203], v[0:15]\n\t"
    "buffer_load_dwordx4 v236, s[16:19], s33 offen offset:1024 lds\n\t"
    "v_add_f32 v226, v226, v228\n\t"
    "v_add_f32 v227, v227, v229\n\t"
    "v_mfma_f32_32x32x16_bf16 v[96:111], a[96:99], v[204:207], v[96:111]\n\t"
    "ds_read_b128 a[96:99], v230 offset:24576\n\t"
    "v_mfma_f32_32x32x16_bf16 v[16:31], a[100:103], v[200:203], v[16:31]\n\t"
    "buffer_load_dwordx4 v237, s[16:19], s33 offen offset:2048 lds\n\t"
    "s_add_u32 s33, s33, 12288\n\t"
    "v_mfma_f32_32x32x16_bf16 v[112:127], a[100:103], v[204:207], v[112:127]\n\t"
    "ds_read_b128 a[100:103], v231 offset:24576\n\t"
    "v_fma_f32 v216, v128, s63, v224\n\t"
    "v_fma_f32 v217, v129, s63, v224\n\t"
    "v_exp_f32 v216, v216\n\t"
    "v_mfma_f32_32x32x16_bf16 v[32:47], a[104:107], v[200:203], v[32:47]\n\t"
    "v_exp_f32 v217, v217\n\t"
    "v_fma_f32 v218, v144, s63, v225\n\t"
    "v_fma_f32 v219, v145, s63, v225\n\t"
    "v_mfma_f32_32x32x16_bf16 a[144:159], a[104:107], v[204:207], a[144:159]\n\t"
    "ds_read_b128 a[104:107], v232 offset:24576\n\t"
    "v_exp_f32 v218, v218\n\t"
    "v_mfma_f32_32x32x16_bf16 v[48:63], a[108:111], v[200:203], v[48:63]\n\t"
    "v_exp_f32 v219, v219\n\t"
    "v_cvt_pk_bf16_f32 v192, v216, v217\n\t"
    "v_add_f32 v228, v216, v217\n\t"
    "v_fma_f32 v216, v130, s63, v224\n\t"
    "v_mfma_f32_32x32x16_bf16 a[160:175], a[108:111], v[204:207], a[160:175]\n\t"
    "ds_read_b128 a[108:111], v233 offset:24576\n\t"
    "s_add_u32 m0, s61, 7040\n\t"
    "s_nop 0\n\t"
    "buffer_load_dwordx4 v249, s[24:27], s43 offen offset:128 nt lds\n\t"
    "v_mfma_f32_32x32x16_bf16 v[64:79], a[112:115], v[200:203], v[64:79]\n\t"
    "v_fma_f32 v217, v131, s63, v224\n\t"
    "v_exp_f32 v216, v216\n\t"
    "v_exp_f32 v217, v217\n\t"
    "v_mfma_f32_32x32x16_bf16 a[176:191], a[112:115], v[204:207], a[176:191]\n\t"
    "ds_read_b128 a[112:115], v230 offset:25600\n\t"
    "ds_read_b128 v[220:223], v241 offset:8192\n\t"
    "v_cvt_pk_bf16_f32 v196, v218, v219\n\t"
    "v_add_f32 v229, v218, v219\n\t"
    "v_mfma_f32_32x32x16_bf16 v[80:95], a[116:119], v[200:203], v[80:95]\n\t"
    "v_fma_f32 v218, v146, s63, v225\n\t"
    "v_fma_f32 v219, v147, s63, v225\n\t"
    "v_exp_f32 v218, v218\n\t"
    "v_mfma_f32_32x32x16_bf16 a[192:207], a[116:119], v[204:207], a[192:207]\n\t"
    "ds_read_b128 a[116:119], v231 offset:25600\n\t"
    "v_exp_f32 v219, v219\n\t"
    "v_cvt_pk_bf16_f32 v193, v216, v217\n\t"
    "v_add_f32 v228, v228, v216\n\t"
    "s_waitcnt lgkmcnt(11)\n\t"
    "v_mfma_f32_32x32x16_bf16 v[0:15], a[120:123], v[208:211], v[0:15]\n\t"
    "v_add_f32 v228, v228, v217\n\t"
    "v_fma_f32 v216, v132, s63, v224\n\t"
    "v_fma_f32 v217, v133, s63, v224\n\t"
    "v_mfma_f32_32x32x16_bf16 v[96:111], a[120:123], v[212:215], v[96:111]\n\t"
    "s_waitcnt lgkmcnt(1)\n\t"
    "buffer_store_dwordx4 v[220:223], v248, s[28:31], 0 offen offset:256 nt\n\t"
    "ds_read_b128 a[120:123], v232 offset:25600\n\t"
    "v_exp_f32 v216, v216\n\t"
    "v_exp_f32 v217, v217\n\t"
    "v_mfma_f32_32x32x16_bf16 v[16:31], a[124:127], v[208:211], v[16:31]\n\t"
    "v_cvt_pk_bf16_f32 v197, v218, v219\n\t"
    "v_add_f32 v229, v229, v218\n\t"
    "v_add_f32 v229, v229, v219\n\t"
    "v_fma_f32 v218, v148, s63, v225\n\t"
    "v_mfma_f32_32x32x16_bf16 v[112:127], a[124:127], v[212:215], v[112:127]\n\t"
    "ds_read_b128 a[124:127], v233 offset:25600\n\t"
    "v_fma_f32 v219, v149, s63, v225\n\t"
    "v_exp_f32 v218, v218\n\t"
    "v_mfma_f32_32x32x16_bf16 v[32:47], a[128:131], v[208:211], v[32:47]\n\t"
    "v_exp_f32 v219, v219\n\t"
    "v_cvt_pk_bf16_f32 v194, v216, v217\n\t"
    "v_add_f32 v228, v228, v216\n\t"
    "v_mfma_f32_32x32x16_bf16 a[144:159], a[128:131], v[212:215], a[144:159]\n\t"
    "ds_read_b128 a[128:131], v230 offset:26624\n\t"
    "v_add_f32 v228, v228, v217\n\t"
    "v_fma_f32 v216, v134, s63, v224\n\t"
    "v_fma_f32 v217, v135, s63, v224\n\t"
    "v_mfma_f32_32x32x16_bf16 v[48:63], a[132:135], v[208:211], v[48:63]\n\t"
    "ds_read_b128 v[252:255], v241 offset:9216\n\t"
    "v_exp_f32 v216, v216\n\t"
    "v_exp_f32 v217, v217\n\t"
    "v_mfma_f32_32x32x16_bf16 a[160:175], a[132:135], v[212:215], a[160:175]\n\t"
    "ds_read_b128 a[132:135], v231 offset:26624\n\t"
    "v_cvt_pk_bf16_f32 v198, v218, v219\n\t"
    "v_add_f32 v229, v229, v218\n\t"
    "v_add_f32 v229, v229, v219\n\t"
    "v_mfma_f32_32x32x16_bf16 v[64:79], a[136:139], v[208:211], v[64:79]\n\t"
    "v_fma_f32 v218, v150, s63, v225\n\t"
    "v_fma_f32 v219, v151, s63, v225\n\t"
    "v_exp_f32 v218, v218\n\t"
    "v_mfma_f32_32x32x16_bf16 a[176:191], a[136:139], v[212:215], a[176:191]\n\t"
    "ds_read_b128 a[136:139], v232 offset:26624\n\t"
    "v_exp_f32 v219, v219\n\t"
    "v_cvt_pk_bf16_f32 v195, v216, v217\n\t"
    "v_add_f32 v228, v228, v216\n\t"
    "v_mfma_f32_32x32x16_bf16 v[80:95], a[140:143], v[208:211], v[80:95]\n\t"
    "s_waitcnt lgkmcnt(2)\n\t"
    "buffer_store_dwordx4 v[252:255], v249, s[28:31], 0 offen offset:256 nt\n\t"
    "v_add_f32 v228, v228, v217\n\t"
    "s_nop 0\n\t"
    "v_cvt_pk_bf16_f32 v199, v218, v219\n\t"
    "v_mfma_f32_32x32x16_bf16 a[192:207], a[140:143], v[212:215], a[192:207]\n\t"
    "ds_read_b128 a[140:143], v233 offset:26624\n\t"
    "v_add_f32 v229, v229, v218\n\t"
    "v_add_f32 v229, v229, v219\n\t"
    "v_mfma_f32_32x32x16_bf16 v[160:175], a[96:99], a[0:3], 0\n\t"
    "v_fma_f32 v216, v136, s63, v224\n\t"
    "v_fma_f32 v217, v137, s63, v224\n\t"
    "v_exp_f32 v216, v216\n\t"
    "v_exp_f32 v217, v217\n\t"
    "v_mfma_f32_32x32x16_bf16 v[176:191], a[96:99], a[48:51], 0\n\t"
    "v_fma_f32 v218, v152, s63, v225\n\t"
    "v_fma_f32 v219, v153, s63, v225\n\t"
    "v_exp_f32 v218, v218\n\t"
    "v_mfma_f32_32x32x16_bf16 v[160:175], a[100:103], a[4:7], v[160:175]\n\t"
    "v_exp_f32 v219, v219\n\t"
    "v_cvt_pk_bf16_f32 v208, v216, v217\n\t"
    "v_add_f32 v228, v228, v216\n\t"
    "v_add_f32 v228, v228, v217\n\t"
    "v_fma_f32 v216, v138, s63, v224\n\t"
    "v_mfma_f32_32x32x16_bf16 v[176:191], a[100:103], a[52:55], v[176:191]\n\t"
    "v_fma_f32 v217, v139, s63, v224\n\t"
    "s_waitcnt vmcnt(3) lgkmcnt(0)\n\t"
    "s_barrier\n\t"
    "v_mfma_f32_32x32x16_bf16 v[160:175], a[104:107], a[8:11], v[160:175]\n\t"
    "ds_read_b64_tr_b16 a[96:97], v234 offset:0\n\t"
    "ds_read_b64_tr_b16 a[98:99], v234 offset:3072\n\t"
    "s_add_u32 m0, s49, 24576\n\t"
    "s_nop 0\n\t"
    "buffer_load_dwordx4 v238, s[20:23], s34 offen lds\n\t"
    "v_mfma_f32_32x32x16_bf16 v[176:191], a[104:107], a[56:59], v[176:191]\n\t"
    "ds_read_b64_tr_b16 a[100:101], v234 offset:512\n\t"
    "ds_read_b64_tr_b16 a[102:103], v234 offset:3584\n\t"
    "v_exp_f32 v216, v216\n\t"
    "v_exp_f32 v217, v217\n\t"
    "v_mfma_f32_32x32x16_bf16 v[160:175], a[108:111], a[12:15], v[160:175]\n\t"
    "ds_read_b64_tr_b16 a[104:105], v234 offset:1024\n\t"
    "ds_read_b64_tr_b16 a[106:107], v234 offset:4096\n\t"
    "buffer_load_dwordx4 v239, s[20:23], s34 offen offset:1024 lds\n\t"
    "v_cvt_pk_bf16_f32 v212, v218, v219\n\t"
    "v_mfma_f32_32x32x16_bf16 v[176:191], a[108:111], a[60:63], v[176:191]\n\t"
    "ds_read_b64_tr_b16 a[108:109], v234 offset:1536\n\t"
    "ds_read_b64_tr_b16 a[110:111], v234 offset:4608\n\t"
    "v_add_f32 v229, v229, v218\n\t"
    "v_add_f32 v229, v229, v219\n\t"
    "v_fma_f32 v218, v154, s63, v225\n\t"
    "v_fma_f32 v219, v155, s63, v225\n\t"
    "v_mfma_f32_32x32x16_bf16 v[160:175], a[112:115], a[16:19], v[160:175]\n\t"
    "buffer_load_dwordx4 v240, s[20:23], s34 offen offset:2048 lds\n\t"
    "s_add_u32 s34, s34, 12288\n\t"
    "v_exp_f32 v218, v218\n\t"
    "v_mfma_f32_32x32x16_bf16 v[176:191], a[112:115], a[64:67], v[176:191]\n\t"
    "ds_read_b64_tr_b16 a[112:113], v234 offset:2048\n\t"
    "ds_read_b64_tr_b16 a[114:115], v234 offset:5120\n\t"
    "v_exp_f32 v219, v219\n\t"
    "v_cvt_pk_bf16_f32 v209, v216, v217\n\t"
    "v_add_f32 v228, v228, v216\n\t"
    "v_mfma_f32_32x32x16_bf16 v[160:175], a[116:119], a[20:23], v[160:175]\n\t"
    "s_add_u32 m0, s49, 36864\n\t"
    "s_nop 0\n\t"
    "buffer_load_dwordx4 v238, s[20:23], s35 offen lds\n\t"
    "v_add_f32 v228, v228, v217\n\t"
    "v_fma_f32 v216, v140, s63, v224\n\t"
    "v_mfma_f32_32x32x16_bf16 v[176:191], a[116:119], a[68:71], v[176:191]\n\t"
    "ds_read_b64_tr_b16 a[116:117], v234 offset:2560\n\t"
    "ds_read_b64_tr_b16 a[118:119], v234 offset:5632\n\t"
    "v_fma_f32 v217, v141, s63, v224\n\t"
    "v_exp_f32 v216, v216\n\t"
    "v_mfma_f32_32x32x16_bf16 v[160:175], a[120:123], a[24:27], v[160:175]\n\t"
    "buffer_load_dwordx4 v239, s[20:23], s35 offen offset:1024 lds\n\t"
    "v_exp_f32 v217, v217\n\t"
    "v_cvt_pk_bf16_f32 v213, v218, v219\n\t"
    "v_add_f32 v229, v229, v218\n\t"
    "v_add_f32 v229, v229, v219\n\t"
    "v_mfma_f32_32x32x16_bf16 v[176:191], a[120:123], a[72:75], v[176:191]\n\t"
    "ds_read_b64_tr_b16 a[120:121], v234 offset:6144\n\t"
    "ds_read_b64_tr_b16 a[122:123], v234 offset:9216\n\t"
    "v_fma_f32 v218, v156, s63, v225\n\t"
    "v_fma_f32 v219, v157, s63, v225\n\t"
    "v_mfma_f32_32x32x16_bf16 v[160:175], a[124:127], a[28:31], v[160:175]\n\t"
    "buffer_load_dwordx4 v240, s[20:23], s35 offen offset:2048 lds\n\t"
    "s_add_u32 s35, s35, 12288\n\t"
    "v_exp_f32 v218, v218\n\t"
    "v_exp_f32 v219, v219\n\t"
    "v_mfma_f32_32x32x16_bf16 v[176:191], a[124:127], a[76:79], v[176:191]\n\t"
    "ds_read_b64_tr_b16 a[124:125], v234 offset:6656\n\t"
    "ds_read_b64_tr_b16 a[126:127], v234 offset:9728\n\t"
    "v_cvt_pk_bf16_f32 v210, v216, v217\n\t"
    "v_add_f32 v228, v228, v216\n\t"
    "v_add_f32 v228, v228, v217\n\t"
    "v_mfma_f32_32x32x16_bf16 v[160:175], a[128:131], a[32:35], v[160:175]\n\t"
    "s_add_u32 m0, s46, 24576\n\t"
    "s_nop 0\n\t"
    "buffer_load_dwordx4 v235, s[16:19], s32 offen lds\n\t"
    "v_fma_f32 v216, v142, s63, v224\n\t"
    "v_fma_f32 v217, v143, s63, v224\n\t"
    "v_mfma_f32_32x32x16_bf16 v[176:191], a[128:131], a[80:83], v[176:191]\n\t"
    "ds_read_b64_tr_b16 a[128:129], v234 offset:7168\n\t"
    "ds_read_b64_tr_b16 a[130:131], v234 offset:10240\n\t"
    "v_exp_f32 v216, v216\n\t"
    "v_exp_f32 v217, v217\n\t"
    "v_mfma_f32_32x32x16_bf16 v[160:175], a[132:135], a[36:39], v[160:175]\n\t"
    "buffer_load_dwordx4 v236, s[16:19], s32 offen offset:1024 lds\n\t"
    "v_cvt_pk_bf16_f32 v214, v218, v219\n\t"
    "v_add_f32 v229, v229, v218\n\t"
    "v_add_f32 v229, v229, v219\n\t"
    "v_fma_f32 v218, v158, s63, v225\n\t"
    "v_mfma_f32_32x32x16_bf16 v[176:191], a[132:135], a[84:87], v[176:191]\n\t"
    "ds_read_b64_tr_b16 a[132:133], v234 offset:7680\n\t"
    "ds_read_b64_tr_b16 a[134:135], v234 offset:10752\n\t"
    "v_fma_f32 v219, v159, s63, v225\n\t"
    "v_exp_f32 v218, v218\n\t"
    "v_mfma_f32_32x32x16_bf16 v[160:175], a[136:139], a[40:43], v[160:175]\n\t"
    "buffer_load_dwordx4 v237, s[16:19], s32 offen offset:2048 lds\n\t"
    "s_add_u32 s32, s32, 12288\n\t"
    "v_exp_f32 v219, v219\n\t"
    "v_cvt_pk_bf16_f32 v211, v216, v217\n\t"
    "v_mfma_f32_32x32x16_bf16 v[176:191], a[136:139], a[88:91], v[176:191]\n\t"
    "ds_read_b64_tr_b16 a[136:137], v234 offset:8192\n\t"
    "ds_read_b64_tr_b16 a[138:139], v234 offset:11264\n\t"
    "v_add_f32 v228, v228, v216\n\t"
    "v_add_f32 v228, v228, v217\n\t"
    "s_nop 0\n\t"
    "v_mfma_f32_32x32x16_bf16 v[160:175], a[140:143], a[44:47], v[160:175]\n\t"
    "s_add_u32 m0, s46, 36864\n\t"
    "s_nop 0\n\t"
    "buffer_load_dwordx4 v235, s[16:19], s33 offen lds\n\t"
    "v_cvt_pk_bf16_f32 v215, v218, v219\n\t"
    "v_add_f32 v229, v229, v218\n\t"
    "v_add_f32 v229, v229, v219\n\t"
    "v_mfma_f32_32x32x16_bf16 v[176:191], a[140:143], a[92:95], v[176:191]\n\t"
    "ds_read_b64_tr_b16 a[140:141], v234 offset:8704\n\t"
    "ds_read_b64_tr_b16 a[142:143], v234 offset:11776\n\t"
    "v_add_f32 v251, v228, v229\n\t"
    "v_cmp_nge_f32 vcc, s64, v251\n\t"
    "s_cbranch_vccnz .Ld_stub22\n\t"
    ".Ld_back22:\n\t"
    "s_waitcnt lgkmcnt(12)\n\t"
    "v_mfma_f32_32x32x16_bf16 v[0:15], a[96:99], v[192:195], v[0:15]\n\t"
    "buffer_load_dwordx4 v236, s[16:19], s33 offen offset:1024 lds\n\t"
    "ds_read_b128 v[220:223], v241 offset:10240\n\t"
    "v_add_f32 v226, v226, v228\n\t"
    "v_add_f32 v227, v227, v229\n\t"
    "v_mfma_f32_32x32x16_bf16 v[96:111], a[96:99], v[196:199], v[96:111]\n\t"
    "ds_read_b128 a[96:99], v230 offset:0\n\t"
    "v_mfma_f32_32x32x16_bf16 v[16:31], a[100:103], v[192:195], v[16:31]\n\t"
    "buffer_load_dwordx4 v237, s[16:19], s33 offen offset:2048 lds\n\t"
    "s_add_u32 s33, s33, 12288\n\t"
    "v_mfma_f32_32x32x16_bf16 v[112:127], a[100:103], v[196:199], v[112:127]\n\t"
    "ds_read_b128 a[100:103], v231 offset:0\n\t"
    "v_fma_f32 v216, v160, s63, v224\n\t"
    "v_fma_f32 v217, v161, s63, v224\n\t"
    "v_exp_f32 v216, v216\n\t"
    "v_mfma_f32_32x32x16_bf16 v[32:47], a[104:107], v[192:195], v[32:47]\n\t"
    "s_waitcnt lgkmcnt(2)\n\t"
    "buffer_store_dwordx4 v[220:223], v248, s[28:31], s86 offen offset:256 nt\n\t"
    "v_exp_f32 v217, v217\n\t"
    "v_fma_f32 v218, v176, s63, v225\n\t"
    "v_fma_f32 v219, v177, s63, v225\n\t"
    "v_mfma_f32_32x32x16_bf16 a[144:159], a[104:107], v[196:199], a[144:159]\n\t"
    "ds_read_b128 a[104:107], v232 offset:0\n\t"
    "v_exp_f32 v218, v218\n\t"
    "v_exp_f32 v219, v219\n\t"
    "v_mfma_f32_32x32x16_bf16 v[48:63], a[108:111], v[192:195], v[48:63]\n\t"
    "ds_read_b128 v[252:255], v241 offset:11264\n\t"
    "v_cvt_pk_bf16_f32 v200, v216, v217\n\t"
    "v_add_f32 v228, v216, v217\n\t"
    "v_fma_f32 v216, v162, s63, v224\n\t"
    "v_mfma_f32_32x32x16_bf16 a[160:175], a[108:111], v[196:199], a[160:175]\n\t"
    "ds_read_b128 a[108:111], v233 offset:0\n\t"
    "s_waitcnt vmcnt(15)\n\t"
    "ds_read_b128 a[224:227], v244 offset:4096\n\t"
    "ds_write_b128 v244, a[240:243] offset:8192\n\t"
    "v_fma_f32 v217, v163, s63, v224\n\t"
    "v_mfma_f32_32x32x16_bf16 v[64:79], a[112:115], v[192:195], v[64:79]\n\t"
    "v_exp_f32 v216, v216\n\t"
    "v_exp_f32 v217, v217\n\t"
    "v_cvt_pk_bf16_f32 v204, v218, v219\n\t"
    "v_mfma_f32_32x32x16_bf16 a[176:191], a[112:115], v[196:199], a[176:191]\n\t"
    "ds_read_b128 a[112:115], v230 offset:1024\n\t"
    "ds_read_b128 a[228:231], v245 offset:4096\n\t"
    "ds_write_b128 v245, a[244:247] offset:8192\n\t"
    "v_add_f32 v229, v218, v219\n\t"
    "v_fma_f32 v218, v178, s63, v225\n\t"
    "v_mfma_f32_32x32x16_bf16 v[80:95], a[116:119], v[192:195], v[80:95]\n\t"
    "s_waitcnt lgkmcnt(6)\n\t"
    "buffer_store_dwordx4 v[252:255], v249, s[28:31], s86 offen offset:256 nt\n\t"
    "v_fma_f32 v219, v179, s63, v225\n\t"
    "v_exp_f32 v218, v218\n\t"
    "v_mfma_f32_32x32x16_bf16 a[192:207], a[116:119], v[196:199], a[192:207]\n\t"
    "ds_read_b128 a[116:119], v231 offset:1024\n\t"
    "ds_read_b128 a[232:235], v246 offset:4096\n\t"
    "ds_write_b128 v246, a[248:251] offset:8192\n\t"
    "v_exp_f32 v219, v219\n\t"
    "v_mfma_f32_32x32x16_bf16 v[0:15], a[120:123], v[208:211], v[0:15]\n\t"
    "v_cvt_pk_bf16_f32 v201, v216, v217\n\t"
    "v_add_f32 v228, v228, v216\n\t"
    "v_add_f32 v228, v228, v217\n\t"
    "v_fma_f32 v216, v164, s63, v224\n\t"
    "v_fma_f32 v217, v165, s63, v224\n\t"
    "v_mfma_f32_32x32x16_bf16 v[96:111], a[120:123], v[212:215], v[96:111]\n\t"
    "ds_read_b128 a[120:123], v232 offset:1024\n\t"
    "ds_read_b128 a[236:239], v247 offset:4096\n\t"
    "ds_write_b128 v247, a[252:255] offset:8192\n\t"
    "v_exp_f32 v216, v216\n\t"
    "v_mfma_f32_32x32x16_bf16 v[16:31], a[124:127], v[208:211], v[16:31]\n\t"
    "v_exp_f32 v217, v217\n\t"
    "v_cvt_pk_bf16_f32 v205, v218, v219\n\t"
    "v_add_f32 v229, v229, v218\n\t"
    "v_add_f32 v229, v229, v219\n\t"
    "v_mfma_f32_32x32x16_bf16 v[112:127], a[124:127], v[212:215], v[112:127]\n\t"
    "ds_read_b128 a[124:127], v233 offset:1024\n\t"
    "v_fma_f32 v218, v180, s63, v225\n\t"
    "v_fma_f32 v219, v181, s63, v225\n\t"
    "v_exp_f32 v218, v218\n\t"
    "v_mfma_f32_32x32x16_bf16 v[32:47], a[128:131], v[208:211], v[32:47]\n\t"
    "s_waitcnt lgkmcnt(2)\n\t"
    "s_add_u32 m0, s61, 3968\n\t"
    "s_nop 0\n\t"
    "buffer_load_dwordx4 v248, s[24:27], 0 offen offset:128 nt lds\n\t"
    "v_exp_f32 v219, v219\n\t"
    "v_mfma_f32_32x32x16_bf16 a[144:159], a[128:131], v[212:215], a[144:159]\n\t"
    "ds_read_b128 a[128:131], v230 offset:2048\n\t"
    "v_cvt_pk_bf16_f32 v202, v216, v217\n\t"
    "v_add_f32 v228, v228, v216\n\t"
    "v_add_f32 v228, v228, v217\n\t"
    "v_mfma_f32_32x32x16_bf16 v[48:63], a[132:135], v[208:211], v[48:63]\n\t"
    "v_fma_f32 v216, v166, s63, v224\n\t"
    "v_fma_f32 v217, v167, s63, v224\n\t"
    "v_exp_f32 v216, v216\n\t"
    "v_mfma_f32_32x32x16_bf16 a[160:175], a[132:135], v[212:215], a[160:175]\n\t"
    "ds_read_b128 a[132:135], v231 offset:2048\n\t"
    "v_exp_f32 v217, v217\n\t"
    "v_cvt_pk_bf16_f32 v206, v218, v219\n\t"
    "v_add_f32 v229, v229, v218\n\t"
    "v_add_f32 v229, v229, v219\n\t"
    "v_mfma_f32_32x32x16_bf16 v[64:79], a[136:139], v[208:211], v[64:79]\n\t"
    "v_fma_f32 v218, v182, s63, v225\n\t"
    "v_fma_f32 v219, v183, s63, v225\n\t"
    "v_exp_f32 v218, v218\n\t"
    "v_mfma_f32_32x32x16_bf16 a[176:191], a[136:139], v[212:215], a[176:191]\n\t"
    "ds_read_b128 a[136:139], v232 offset:2048\n\t"
    "v_exp_f32 v219, v219\n\t"
    "v_cvt_pk_bf16_f32 v203, v216, v217\n\t"
    "v_add_f32 v228, v228, v216\n\t"
    "v_mfma_f32_32x32x16_bf16 v[80:95], a[140:143], v[208:211], v[80:95]\n\t"
    "s_add_u32 m0, s61, 4992\n\t"
    "s_nop 0\n\t"
    "buffer_load_dwordx4 v249, s[24:27], 0 offen offset:128 nt lds\n\t"
    "v_add_f32 v228, v228, v217\n\t"
    "s_nop 0\n\t"
    "v_mfma_f32_32x32x16_bf16 a[192:207], a[140:143], v[212:215], a[192:207]\n\t"
    "ds_read_b128 a[140:143], v233 offset:2048\n\t"
    "v_cvt_pk_bf16_f32 v207, v218, v219\n\t"
    "v_add_f32 v229, v229, v218\n\t"
    "v_add_f32 v229, v229, v219\n\t"
    "v_mfma_f32_32x32x16_bf16 v[128:143], a[96:99], a[0:3], 0\n\t"
    "v_fma_f32 v216, v168, s63, v224\n\t"
    "v_fma_f32 v217, v169, s63, v224\n\t"
    "v_exp_f32 v216, v216\n\t"
    "v_exp_f32 v217, v217\n\t"
    "v_mfma_f32_32x32x16_bf16 v[144:159], a[96:99], a[48:51], 0\n\t"
    "v_fma_f32 v218, v184, s63, v225\n\t"
    "v_fma_f32 v219, v185, s63, v225\n\t"
    "v_exp_f32 v218, v218\n\t"
    "v_mfma_f32_32x32x16_bf16 v[128:143], a[100:103], a[4:7], v[128:143]\n\t"
    "v_exp_f32 v219, v219\n\t"
    "v_cvt_pk_bf16_f32 v208, v216, v217\n\t"
    "v_add_f32 v228, v228, v216\n\t"
    "v_add_f32 v228, v228, v217\n\t"
    "v_fma_f32 v216, v170, s63, v224\n\t"
    "v_mfma_f32_32x32x16_bf16 v[144:159], a[100:103], a[52:55], v[144:159]\n\t"
    "v_fma_f32 v217, v171, s63, v224\n\t"
    "s_waitcnt vmcnt(4) lgkmcnt(0)\n\t"
    "s_barrier\n\t"
    "v_mfma_f32_32x32x16_bf16 v[128:143], a[104:107], a[8:11], v[128:143]\n\t"
    "ds_read_b64_tr_b16 a[96:97], v234 offset:24576\n\t"
    "ds_read_b64_tr_b16 a[98:99], v234 offset:27648\n\t"
    "s_mov_b32 m0, s49\n\t"
    "s_nop 0\n\t"
    "buffer_load_dwordx4 v238, s[20:23], s34 offen lds\n\t"
    "v_mfma_f32_32x32x16_bf16 v[144:159], a[104:107], a[56:59], v[144:159]\n\t"
    "ds_read_b64_tr_b16 a[100:101], v234 offset:25088\n\t"
    "ds_read_b64_tr_b16 a[102:103], v234 offset:28160\n\t"
    "v_exp_f32 v216, v216\n\t"
    "v_exp_f32 v217, v217\n\t"
    "v_mfma_f32_32x32x16_bf16 v[128:143], a[108:111], a[12:15], v[128:143]\n\t"
    "ds_read_b64_tr_b16 a[104:105], v234 offset:25600\n\t"
    "ds_read_b64_tr_b16 a[106:107], v234 offset:28672\n\t"
    "buffer_load_dwordx4 v239, s[20:23], s34 offen offset:1024 lds\n\t"
    "v_cvt_pk_bf16_f32 v212, v218, v219\n\t"
    "v_mfma_f32_32x32x16_bf16 v[144:159], a[108:111], a[60:63], v[144:159]\n\t"
    "ds_read_b64_tr_b16 a[108:109], v234 offset:26112\n\t"
    "ds_read_b64_tr_b16 a[110:111], v234 offset:29184\n\t"
    "v_add_f32 v229, v229, v218\n\t"
    "v_add_f32 v229, v229, v219\n\t"
    "v_fma_f32 v218, v186, s63, v225\n\t"
    "v_fma_f32 v219, v187, s63, v225\n\t"
    "v_mfma_f32_32x32x16_bf16 v[128:143], a[112:115], a[16:19], v[128:143]\n\t"
    "buffer_load_dwordx4 v240, s[20:23], s34 offen offset:2048 lds\n\t"
    "s_add_u32 s34, s34, 12288\n\t"
    "v_exp_f32 v218, v218\n\t"
    "v_mfma_f32_32x32x16_bf16 v[144:159], a[112:115], a[64:67], v[144:159]\n\t"
    "ds_read_b64_tr_b16 a[112:113], v234 offset:26624\n\t"
    "ds_read_b64_tr_b16 a[114:115], v234 offset:29696\n\t"
    "v_exp_f32 v219, v219\n\t"
    "v_cvt_pk_bf16_f32 v209, v216, v217\n\t"
    "v_add_f32 v228, v228, v216\n\t"
    "v_mfma_f32_32x32x16_bf16 v[128:143], a[116:119], a[20:23], v[128:143]\n\t"
    "s_add_u32 m0, s49, 12288\n\t"
    "s_nop 0\n\t"
    "buffer_load_dwordx4 v238, s[20:23], s35 offen lds\n\t"
    "v_add_f32 v228, v228, v217\n\t"
    "v_fma_f32 v216, v172, s63, v224\n\t"
    "v_mfma_f32_32x32x16_bf16 v[144:159], a[116:119], a[68:71], v[144:159]\n\t"
    "ds_read_b64_tr_b16 a[116:117], v234 offset:27136\n\t"
    "ds_read_b64_tr_b16 a[118:119], v234 offset:30208\n\t"
    "v_fma_f32 v217, v173, s63, v224\n\t"
    "v_exp_f32 v216, v216\n\t"
    "v_mfma_f32_32x32x16_bf16 v[128:143], a[120:123], a[24:27], v[128:143]\n\t"
    "buffer_load_dwordx4 v239, s[20:23], s35 offen offset:1024 lds\n\t"
    "v_exp_f32 v217, v217\n\t"
    "v_cvt_pk_bf16_f32 v213, v218, v219\n\t"
    "v_add_f32 v229, v229, v218\n\t"
    "v_add_f32 v229, v229, v219\n\t"
    "v_mfma_f32_32x32x16_bf16 v[144:159], a[120:123], a[72:75], v[144:159]\n\t"
    "ds_read_b64_tr_b16 a[120:121], v234 offset:30720\n\t"
    "ds_read_b64_tr_b16 a[122:123], v234 offset:33792\n\t"
    "v_fma_f32 v218, v188, s63, v225\n\t"
    "v_fma_f32 v219, v189, s63, v225\n\t"
    "v_mfma_f32_32x32x16_bf16 v[128:143], a[124:127], a[28:31], v[128:143]\n\t"
    "buffer_load_dwordx4 v240, s[20:23], s35 offen offset:2048 lds\n\t"
    "s_add_u32 s35, s35, 12288\n\t"
    "v_exp_f32 v218, v218\n\t"
    "v_exp_f32 v219, v219\n\t"
    "v_mfma_f32_32x32x16_bf16 v[144:159], a[124:127], a[76:79], v[144:159]\n\t"
    "ds_read_b64_tr_b16 a[124:125], v234 offset:31232\n\t"
    "ds_read_b64_tr_b16 a[126:127], v234 offset:34304\n\t"
    "v_cvt_pk_bf16_f32 v210, v216, v217\n\t"
    "v_add_f32 v228, v228, v216\n\t"
    "v_add_f32 v228, v228, v217\n\t"
    "v_mfma_f32_32x32x16_bf16 v[128:143], a[128:131], a[32:35], v[128:143]\n\t"
    "s_mov_b32 m0, s46\n\t"
    "s_nop 0\n\t"
    "buffer_load_dwordx4 v235, s[16:19], s32 offen lds\n\t"
    "v_fma_f32 v216, v174, s63, v224\n\t"
    "v_fma_f32 v217, v175, s63, v224\n\t"
    "v_mfma_f32_32x32x16_bf16 v[144:159], a[128:131], a[80:83], v[144:159]\n\t"
    "ds_read_b64_tr_b16 a[128:129], v234 offset:31744\n\t"
    "ds_read_b64_tr_b16 a[130:131], v234 offset:34816\n\t"
    "v_exp_f32 v216, v216\n\t"
    "v_exp_f32 v217, v217\n\t"
    "v_mfma_f32_32x32x16_bf16 v[128:143], a[132:135], a[36:39], v[128:143]\n\t"
    "buffer_load_dwordx4 v236, s[16:19], s32 offen offset:1024 lds\n\t"
    "v_cvt_pk_bf16_f32 v214, v218, v219\n\t"
    "v_add_f32 v229, v229, v218\n\t"
    "v_add_f32 v229, v229, v219\n\t"
    "v_fma_f32 v218, v190, s63, v225\n\t"
    "v_mfma_f32_32x32x16_bf16 v[144:159], a[132:135], a[84:87], v[144:159]\n\t"
    "ds_read_b64_tr_b16 a[132:133], v234 offset:32256\n\t"
    "ds_read_b64_tr_b16 a[134:135], v234 offset:35328\n\t"
    "v_fma_f32 v219, v191, s63, v225\n\t"
    "v_exp_f32 v218, v218\n\t"
    "v_mfma_f32_32x32x16_bf16 v[128:143], a[136:139], a[40:43], v[128:143]\n\t"
    "buffer_load_dwordx4 v237, s[16:19], s32 offen offset:2048 lds\n\t"
    "s_add_u32 s32, s32, 12288\n\t"
    "v_exp_f32 v219, v219\n\t"
    "v_cvt_pk_bf16_f32 v211, v216, v217\n\t"
    "v_mfma_f32_32x32x16_bf16 v[144:159], a[136:139], a[88:91], v[144:159]\n\t"
    "ds_read_b64_tr_b16 a[136:137], v234 offset:32768\n\t"
    "ds_read_b64_tr_b16 a[138:139], v234 offset:35840\n\t"
    "v_add_f32 v228, v228, v216\n\t"
    "v_add_f32 v228, v228, v217\n\t"
    "s_nop 0\n\t"
    "v_mfma_f32_32x32x16_bf16 v[128:143], a[140:143], a[44:47], v[128:143]\n\t"
    "s_add_u32 m0, s46, 12288\n\t"
    "s_nop 0\n\t"
    "buffer_load_dwordx4 v235, s[16:19], s33 offen lds\n\t"
    "v_cvt_pk_bf16_f32 v215, v218, v219\n\t"
    "v_add_f32 v229, v229, v218\n\t"
    "v_add_f32 v229, v229, v219\n\t"
    "v_mfma_f32_32x32x16_bf16 v[144:159], a[140:143], a[92:95], v[144:159]\n\t"
    "ds_read_b64_tr_b16 a[140:141], v234 offset:33280\n\t"
    "ds_read_b64_tr_b16 a[142:143], v234 offset:36352\n\t"
    "v_add_f32 v251, v228, v229\n\t"
    "v_cmp_nge_f32 vcc, s64, v251\n\t"
    "s_cbranch_vccnz .Ld_stub23\n\t"
    ".Ld_back23:\n\t"
    "s_waitcnt lgkmcnt(12)\n\t"
    "v_mfma_f32_32x32x16_bf16 v[0:15], a[96:99], v[200:203], v[0:15]\n\t"
    "buffer_load_dwordx4 v236, s[16:19], s33 offen offset:1024 lds\n\t"
    "v_add_f32 v226, v226, v228\n\t"
    "v_add_f32 v227, v227, v229\n\t"
    "v_mfma_f32_32x32x16_bf16 v[96:111], a[96:99], v[204:207], v[96:111]\n\t"
    "ds_read_b128 a[96:99], v230 offset:24576\n\t"
    "v_mfma_f32_32x32x16_bf16 v[16:31], a[100:103], v[200:203], v[16:31]\n\t"
    "buffer_load_dwordx4 v237, s[16:19], s33 offen offset:2048 lds\n\t"
    "s_add_u32 s33, s33, 12288\n\t"
    "v_mfma_f32_32x32x16_bf16 v[112:127], a[100:103], v[204:207], v[112:127]\n\t"
    "ds_read_b128 a[100:103], v231 offset:24576\n\t"
    "v_fma_f32 v216, v128, s63, v224\n\t"
    "v_fma_f32 v217, v129, s63, v224\n\t"
    "v_exp_f32 v216, v216\n\t"
    "v_mfma_f32_32x32x16_bf16 v[32:47], a[104:107], v[200:203], v[32:47]\n\t"
    "s_add_u32 m0, s61, 6016\n\t"
    "s_nop 0\n\t"
    "buffer_load_dwordx4 v248, s[24:27], s86 offen offset:128 nt lds\n\t"
    "v_mfma_f32_32x32x16_bf16 a[144:159], a[104:107], v[204:207], a[144:159]\n\t"
    "ds_read_b128 a[104:107], v232 offset:24576\n\t"
    "v_exp_f32 v217, v217\n\t"
    "v_fma_f32 v218, v144, s63, v225\n\t"
    "v_fma_f32 v219, v145, s63, v225\n\t"
    "v_mfma_f32_32x32x16_bf16 v[48:63], a[108:111], v[200:203], v[48:63]\n\t"
    "v_exp_f32 v218, v218\n\t"
    "v_exp_f32 v219, v219\n\t"
    "v_cvt_pk_bf16_f32 v192, v216, v217\n\t"
    "v_mfma_f32_32x32x16_bf16 a[160:175], a[108:111], v[204:207], a[160:175]\n\t"
    "ds_read_b128 a[108:111], v233 offset:24576\n\t"
    "v_add_f32 v228, v216, v217\n\t"
    "v_fma_f32 v216, v130, s63, v224\n\t"
    "v_fma_f32 v217, v131, s63, v224\n\t"
    "v_mfma_f32_32x32x16_bf16 v[64:79], a[112:115], v[200:203], v[64:79]\n\t"
    "v_exp_f32 v216, v216\n\t"
    "v_exp_f32 v217, v217\n\t"
    "v_mfma_f32_32x32x16_bf16 a[176:191], a[112:115], v[204:207], a[176:191]\n\t"
    "ds_read_b128 a[112:115], v230 offset:25600\n\t"
    "v_cvt_pk_bf16_f32 v196, v218, v219\n\t"
    "v_add_f32 v229, v218, v219\n\t"
    "v_fma_f32 v218, v146, s63, v225\n\t"
    "v_fma_f32 v219, v147, s63, v225\n\t"
    "v_mfma_f32_32x32x16_bf16 v[80:95], a[116:119], v[200:203], v[80:95]\n\t"
    "v_exp_f32 v218, v218\n\t"
    "v_exp_f32 v219, v219\n\t"
    "v_mfma_f32_32x32x16_bf16 a[192:207], a[116:119], v[204:207], a[192:207]\n\t"
    "ds_read_b128 a[116:119], v231 offset:25600\n\t"
    "v_cvt_pk_bf16_f32 v193, v216, v217\n\t"
    "v_add_f32 v228, v228, v216\n\t"
    "v_add_f32 v228, v228, v217\n\t"
    "v_fma_f32 v216, v132, s63, v224\n\t"
    "s_waitcnt lgkmcnt(10)\n\t"
    "v_mfma_f32_32x32x16_bf16 v[0:15], a[120:123], v[208:211], v[0:15]\n\t"
    "v_fma_f32 v217, v133, s63, v224\n\t"
    "v_exp_f32 v216, v216\n\t"
    "v_mfma_f32_32x32x16_bf16 v[96:111], a[120:123], v[212:215], v[96:111]\n\t"
    "ds_read_b128 a[120:123], v232 offset:25600\n\t"
    "s_add_u32 m0, s61, 7040\n\t"
    "s_nop 0\n\t"
    "buffer_load_dwordx4 v249, s[24:27], s86 offen offset:128 nt lds\n\t"
    "v_exp_f32 v217, v217\n\t"
    "v_mfma_f32_32x32x16_bf16 v[16:31], a[124:127], v[208:211], v[16:31]\n\t"
    "v_cvt_pk_bf16_f32 v197, v218, v219\n\t"
    "v_add_f32 v229, v229, v218\n\t"
    "v_add_f32 v229, v229, v219\n\t"
    "v_fma_f32 v218, v148, s63, v225\n\t"
    "v_mfma_f32_32x32x16_bf16 v[112:127], a[124:127], v[212:215], v[112:127]\n\t"
    "ds_read_b128 a[124:127], v233 offset:25600\n\t"
    "ds_read_b128 v[220:223], v241 offset:8192\n\t"
    "v_fma_f32 v219, v149, s63, v225\n\t"
    "v_exp_f32 v218, v218\n\t"
    "v_mfma_f32_32x32x16_bf16 v[32:47], a[128:131], v[208:211], v[32:47]\n\t"
    "v_exp_f32 v219, v219\n\t"
    "v_cvt_pk_bf16_f32 v194, v216, v217\n\t"
    "v_add_f32 v228, v228, v216\n\t"
    "v_mfma_f32_32x32x16_bf16 a[144:159], a[128:131], v[212:215], a[144:159]\n\t"
    "ds_read_b128 a[128:131], v230 offset:26624\n\t"
    "v_add_f32 v228, v228, v217\n\t"
    "v_fma_f32 v216, v134, s63, v224\n\t"
    "v_fma_f32 v217, v135, s63, v224\n\t"
    "v_mfma_f32_32x32x16_bf16 v[48:63], a[132:135], v[208:211], v[48:63]\n\t"
    "v_exp_f32 v216, v216\n\t"
    "v_exp_f32 v217, v217\n\t"
    "v_mfma_f32_32x32x16_bf16 a[160:175], a[132:135], v[212:215], a[160:175]\n\t"
    "s_waitcnt lgkmcnt(1)\n\t"
    "buffer_store_dwordx4 v[220:223], v248, s[28:31], s87 offen offset:256 nt\n\t"
    "ds_read_b128 a[132:135], v231 offset:26624\n\t"
    "v_cvt_pk_bf16_f32 v198, v218, v219\n\t"
    "v_add_f32 v229, v229, v218\n\t"
    "v_add_f32 v229, v229, v219\n\t"
    "v_mfma_f32_32x32x16_bf16 v[64:79], a[136:139], v[208:211], v[64:79]\n\t"
    "v_fma_f32 v218, v150, s63, v225\n\t"
    "v_fma_f32 v219, v151, s63, v225\n\t"
    "v_exp_f32 v218, v218\n\t"
    "v_mfma_f32_32x32x16_bf16 a[176:191], a[136:139], v[212:215], a[176:191]\n\t"
    "ds_read_b128 a[136:139], v232 offset:26624\n\t"
    "ds_read_b128 v[252:255], v241 offset:9216\n\t"
    "v_exp_f32 v219, v219\n\t"
    "v_cvt_pk_bf16_f32 v195, v216, v217\n\t"
    "v_mfma_f32_32x32x16_bf16 v[80:95], a[140:143], v[208:211], v[80:95]\n\t"
    "v_add_f32 v228, v228, v216\n\t"
    "v_add_f32 v228, v228, v217\n\t"
    "s_nop 0\n\t"
    "v_cvt_pk_bf16_f32 v199, v218, v219\n\t"
    "v_mfma_f32_32x32x16_bf16 a[192:207], a[140:143], v[212:215], a[192:207]\n\t"
    "ds_read_b128 a[140:143], v233 offset:26624\n\t"
    "v_add_f32 v229, v229, v218\n\t"
    "v_add_f32 v229, v229, v219\n\t"
    "v_mfma_f32_32x32x16_bf16 v[160:175], a[96:99], a[0:3], 0\n\t"
    "v_fma_f32 v216, v136, s63, v224\n\t"
    "v_fma_f32 v217, v137, s63, v224\n\t"
    "v_exp_f32 v216, v216\n\t"
    "v_exp_f32 v217, v217\n\t"
    "v_mfma_f32_32x32x16_bf16 v[176:191], a[96:99], a[48:51], 0\n\t"
    "s_waitcnt lgkmcnt(1)\n\t"
    "buffer_store_dwordx4 v[252:255], v249, s[28:31], s87 offen offset:256 nt\n\t"
    "v_fma_f32 v218, v152, s63, v225\n\t"
    "v_fma_f32 v219, v153, s63, v225\n\t"
    "v_exp_f32 v218, v218\n\t"
    "v_mfma_f32_32x32x16_bf16 v[160:175], a[100:103], a[4:7], v[160:175]\n\t"
    "v_exp_f32 v219, v219\n\t"
    "v_cvt_pk_bf16_f32 v208, v216, v217\n\t"
    "v_add_f32 v228, v228, v216\n\t"
    "v_add_f32 v228, v228, v217\n\t"
    "v_mfma_f32_32x32x16_bf16 v[176:191], a[100:103], a[52:55], v[176:191]\n\t"
    "v_fma_f32 v216, v138, s63, v224\n\t"
    "s_waitcnt vmcnt(4) lgkmcnt(0)\n\t"
    "s_barrier\n\t"
    "v_mfma_f32_32x32x16_bf16 v[160:175], a[104:107], a[8:11], v[160:175]\n\t"
    "ds_read_b64_tr_b16 a[96:97], v234 offset:0\n\t"
    "ds_read_b64_tr_b16 a[98:99], v234 offset:3072\n\t"
    "s_add_u32 m0, s49, 24576\n\t"
    "s_nop 0\n\t"
    "buffer_load_dwordx4 v238, s[20:23], s34 offen lds\n\t"
    "v_mfma_f32_32x32x16_bf16 v[176:191], a[104:107], a[56:59], v[176:191]\n\t"
    "ds_read_b64_tr_b16 a[100:101], v234 offset:512\n\t"
    "ds_read_b64_tr_b16 a[102:103], v234 offset:3584\n\t"
    "v_fma_f32 v217, v139, s63, v224\n\t"
    "v_exp_f32 v216, v216\n\t"
    "v_mfma_f32_32x32x16_bf16 v[160:175], a[108:111], a[12:15], v[160:175]\n\t"
    "ds_read_b64_tr_b16 a[104:105], v234 offset:1024\n\t"
    "ds_read_b64_tr_b16 a[106:107], v234 offset:4096\n\t"
    "buffer_load_dwordx4 v239, s[20:23], s34 offen offset:1024 lds\n\t"
    "v_exp_f32 v217, v217\n\t"
    "v_cvt_pk_bf16_f32 v212, v218, v219\n\t"
    "v_mfma_f32_32x32x16_bf16 v[176:191], a[108:111], a[60:63], v[176:191]\n\t"
    "ds_read_b64_tr_b16 a[108:109], v234 offset:1536\n\t"
    "ds_read_b64_tr_b16 a[110:111], v234 offset:4608\n\t"
    "v_add_f32 v229, v229, v218\n\t"
    "v_add_f32 v229, v229, v219\n\t"
    "v_fma_f32 v218, v154, s63, v225\n\t"
    "v_mfma_f32_32x32x16_bf16 v[160:175], a[112:115], a[16:19], v[160:175]\n\t"
    "buffer_load_dwordx4 v240, s[20:23], s34 offen offset:2048 lds\n\t"
    "s_add_u32 s34, s34, 12288\n\t"
    "v_fma_f32 v219, v155, s63, v225\n\t"
    "v_exp_f32 v218, v218\n\t"
    "v_mfma_f32_32x32x16_bf16 v[176:191], a[112:115], a[64:67], v[176:191]\n\t"
    "ds_read_b64_tr_b16 a[112:113], v234 offset:2048\n\t"
    "ds_read_b64_tr_b16 a[114:115], v234 offset:5120\n\t"
    "v_exp_f32 v219, v219\n\t"
    "v_cvt_pk_bf16_f32 v209, v216, v217\n\t"
    "v_mfma_f32_32x32x16_bf16 v[160:175], a[116:119], a[20:23], v[160:175]\n\t"
    "s_add_u32 m0, s49, 36864\n\t"
    "s_nop 0\n\t"
    "buffer_load_dwordx4 v238, s[20:23], s35 offen lds\n\t"
    "v_add_f32 v228, v228, v216\n\t"
    "v_add_f32 v228, v228, v217\n\t"
    "v_fma_f32 v216, v140, s63, v224\n\t"
    "v_mfma_f32_32x32x16_bf16 v[176:191], a[116:119], a[68:71], v[176:191]\n\t"
    "ds_read_b64_tr_b16 a[116:117], v234 offset:2560\n\t"
    "ds_read_b64_tr_b16 a[118:119], v234 offset:5632\n\t"
    "v_fma_f32 v217, v141, s63, v224\n\t"
    "v_exp_f32 v216, v216\n\t"
    "v_mfma_f32_32x32x16_bf16 v[160:175], a[120:123], a[24:27], v[160:175]\n\t"
    "buffer_load_dwordx4 v239, s[20:23], s35 offen offset:1024 lds\n\t"
    "v_exp_f32 v217, v217\n\t"
    "v_cvt_pk_bf16_f32 v213, v218, v219\n\t"
    "v_add_f32 v229, v229, v218\n\t"
    "v_mfma_f32_32x32x16_bf16 v[176:191], a[120:123], a[72:75], v[176:191]\n\t"
    "ds_read_b64_tr_b16 a[120:121], v234 offset:6144\n\t"
    "ds_read_b64_tr_b16 a[122:123], v234 offset:9216\n\t"
    "v_add_f32 v229, v229, v219\n\t"
    "v_fma_f32 v218, v156, s63, v225\n\t"
    "v_fma_f32 v219, v157, s63, v225\n\t"
    "v_mfma_f32_32x32x16_bf16 v[160:175], a[124:127], a[28:31], v[160:175]\n\t"
    "buffer_load_dwordx4 v240, s[20:23], s35 offen offset:2048 lds\n\t"
    "s_add_u32 s35, s35, 12288\n\t"
    "v_exp_f32 v218, v218\n\t"
    "v_exp_f32 v219, v219\n\t"
    "v_mfma_f32_32x32x16_bf16 v[176:191], a[124:127], a[76:79], v[176:191]\n\t"
    "ds_read_b64_tr_b16 a[124:125], v234 offset:6656\n\t"
    "ds_read_b64_tr_b16 a[126:127], v234 offset:9728\n\t"
    "v_cvt_pk_bf16_f32 v210, v216, v217\n\t"
    "v_add_f32 v228, v228, v216\n\t"
    "v_add_f32 v228, v228, v217\n\t"
    "v_mfma_f32_32x32x16_bf16 v[160:175], a[128:131], a[32:35], v[160:175]\n\t"
    "s_add_u32 m0, s46, 24576\n\t"
    "s_nop 0\n\t"
    "buffer_load_dwordx4 v235, s[16:19], s32 offen lds\n\t"
    "v_fma_f32 v216, v142, s63, v224\n\t"
    "v_fma_f32 v217, v143, s63, v224\n\t"
    "v_mfma_f32_32x32x16_bf16 v[176:191], a[128:131], a[80:83], v[176:191]\n\t"
    "ds_read_b64_tr_b16 a[128:129], v234 offset:7168\n\t"
    "ds_read_b64_tr_b16 a[130:131], v234 offset:10240\n\t"
    "v_exp_f32 v216, v216\n\t"
    "v_exp_f32 v217, v217\n\t"
    "v_mfma_f32_32x32x16_bf16 v[160:175], a[132:135], a[36:39], v[160:175]\n\t"
    "buffer_load_dwordx4 v236, s[16:19], s32 offen offset:1024 lds\n\t"
    "v_cvt_pk_bf16_f32 v214, v218, v219\n\t"
    "v_add_f32 v229, v229, v218\n\t"
    "v_add_f32 v229, v229, v219\n\t"
    "v_fma_f32 v218, v158, s63, v225\n\t"
    "v_mfma_f32_32x32x16_bf16 v[176:191], a[132:135], a[84:87], v[176:191]\n\t"
    "ds_read_b64_tr_b16 a[132:133], v234 offset:7680\n\t"
    "ds_read_b64_tr_b16 a[134:135], v234 offset:10752\n\t"
    "v_fma_f32 v219, v159, s63, v225\n\t"
    "v_exp_f32 v218, v218\n\t"
    "v_mfma_f32_32x32x16_bf16 v[160:175], a[136:139], a[40:43], v[160:175]\n\t"
    "buffer_load_dwordx4 v237, s[16:19], s32 offen offset:2048 lds\n\t"
    "s_add_u32 s32, s32, 12288\n\t"
    "v_exp_f32 v219, v219\n\t"
    "v_cvt_pk_bf16_f32 v211, v216, v217\n\t"
    "v_mfma_f32_32x32x16_bf16 v[176:191], a[136:139], a[88:91], v[176:191]\n\t"
    "ds_read_b64_tr_b16 a[136:137], v234 offset:8192\n\t"
    "ds_read_b64_tr_b16 a[138:139], v234 offset:11264\n\t"
    "v_add_f32 v228, v228, v216\n\t"
    "v_add_f32 v228, v228, v217\n\t"
    "s_nop 0\n\t"
    "v_mfma_f32_32x32x16_bf16 v[160:175], a[140:143], a[44:47], v[160:175]\n\t"
    "s_add_u32 m0, s46, 36864\n\t"
    "s_nop 0\n\t"
    "buffer_load_dwordx4 v235, s[16:19], s33 offen lds\n\t"
    "v_cvt_pk_bf16_f32 v215, v218, v219\n\t"
    "v_add_f32 v229, v229, v218\n\t"
    "v_add_f32 v229, v229, v219\n\t"
    "v_mfma_f32_32x32x16_bf16 v[176:191], a[140:143], a[92:95], v[176:191]\n\t"
    "ds_read_b64_tr_b16 a[140:141], v234 offset:8704\n\t"
    "ds_read_b64_tr_b16 a[142:143], v234 offset:11776\n\t"
    "v_add_f32 v251, v228, v229\n\t"
    "v_cmp_nge_f32 vcc, s64, v251\n\t"
    "s_cbranch_vccnz .Ld_stub24\n\t"
    ".Ld_back24:\n\t"
    "s_waitcnt lgkmcnt(12)\n\t"
    "v_mfma_f32_32x32x16_bf16 v[0:15], a[96:99], v[192:195], v[0:15]\n\t"
    "buffer_load_dwordx4 v236, s[16:19], s33 offen offset:1024 lds\n\t"
    "v_add_f32 v226, v226, v228\n\t"
    "v_add_f32 v227, v227, v229\n\t"
    "v_mfma_f32_32x32x16_bf16 v[96:111], a[96:99], v[196:199], v[96:111]\n\t"
    "ds_read_b128 a[96:99], v230 offset:0\n\t"
    "v_mfma_f32_32x32x16_bf16 v[16:31], a[100:103], v[192:195], v[16:31]\n\t"
    "buffer_load_dwordx4 v237, s[16:19], s33 offen offset:2048 lds\n\t"
    "s_add_u32 s33, s33, 12288\n\t"
    "v_mfma_f32_32x32x16_bf16 v[112:127], a[100:103], v[196:199], v[112:127]\n\t"
    "ds_read_b128 a[100:103], v231 offset:0\n\t"
    "ds_read_b128 v[220:223], v241 offset:10240\n\t"
    "v_fma_f32 v216, v160, s63, v224\n\t"
    "v_fma_f32 v217, v161, s63, v224\n\t"
    "v_mfma_f32_32x32x16_bf16 v[32:47], a[104:107], v[192:195], v[32:47]\n\t"
    "v_exp_f32 v216, v216\n\t"
    "v_exp_f32 v217, v217\n\t"
    "v_fma_f32 v218, v176, s63, v225\n\t"
    "v_mfma_f32_32x32x16_bf16 a[144:159], a[104:107], v[196:199], a[144:159]\n\t"
    "ds_read_b128 a[104:107], v232 offset:0\n\t"
    "v_fma_f32 v219, v177, s63, v225\n\t"
    "v_exp_f32 v218, v218\n\t"
    "v_mfma_f32_32x32x16_bf16 v[48:63], a[108:111], v[192:195], v[48:63]\n\t"
    "v_exp_f32 v219, v219\n\t"
    "v_cvt_pk_bf16_f32 v200, v216, v217\n\t"
    "v_add_f32 v228, v216, v217\n\t"
    "v_mfma_f32_32x32x16_bf16 a[160:175], a[108:111], v[196:199], a[160:175]\n\t"
    "s_waitcnt lgkmcnt(1)\n\t"
    "buffer_store_dwordx4 v[220:223], v248, s[28:31], s43 offen offset:256 nt\n\t"
    "ds_read_b128 a[108:111], v233 offset:0\n\t"
    "v_fma_f32 v216, v162, s63, v224\n\t"
    "v_fma_f32 v217, v163, s63, v224\n\t"
    "v_mfma_f32_32x32x16_bf16 v[64:79], a[112:115], v[192:195], v[64:79]\n\t"
    "v_exp_f32 v216, v216\n\t"
    "v_exp_f32 v217, v217\n\t"
    "v_cvt_pk_bf16_f32 v204, v218, v219\n\t"
    "v_mfma_f32_32x32x16_bf16 a[176:191], a[112:115], v[196:199], a[176:191]\n\t"
    "ds_read_b128 a[112:115], v230 offset:1024\n\t"
    "ds_read_b128 v[252:255], v241 offset:11264\n\t"
    "v_add_f32 v229, v218, v219\n\t"
    "v_fma_f32 v218, v178, s63, v225\n\t"
    "v_mfma_f32_32x32x16_bf16 v[80:95], a[116:119], v[192:195], v[80:95]\n\t"
    "v_fma_f32 v219, v179, s63, v225\n\t"
    "v_exp_f32 v218, v218\n\t"
    "v_exp_f32 v219, v219\n\t"
    "v_mfma_f32_32x32x16_bf16 a[192:207], a[116:119], v[196:199], a[192:207]\n\t"
    "ds_read_b128 a[116:119], v231 offset:1024\n\t"
    "v_cvt_pk_bf16_f32 v201, v216, v217\n\t"
    "v_add_f32 v228, v228, v216\n\t"
    "v_add_f32 v228, v228, v217\n\t"
    "v_mfma_f32_32x32x16_bf16 v[0:15], a[120:123], v[208:211], v[0:15]\n\t"
    "v_fma_f32 v216, v164, s63, v224\n\t"
    "v_fma_f32 v217, v165, s63, v224\n\t"
    "v_exp_f32 v216, v216\n\t"
    "v_mfma_f32_32x32x16_bf16 v[96:111], a[120:123], v[212:215], v[96:111]\n\t"
    "s_waitcnt lgkmcnt(1)\n\t"
    "buffer_store_dwordx4 v[252:255], v249, s[28:31], s43 offen offset:256 nt\n\t"
    "ds_read_b128 a[120:123], v232 offset:1024\n\t"
    "v_exp_f32 v217, v217\n\t"
    "v_mfma_f32_32x32x16_bf16 v[16:31], a[124:127], v[208:211], v[16:31]\n\t"
    "v_cvt_pk_bf16_f32 v205, v218, v219\n\t"
    "v_add_f32 v229, v229, v218\n\t"
    "v_add_f32 v229, v229, v219\n\t"
    "v_fma_f32 v218, v180, s63, v225\n\t"
    "v_fma_f32 v219, v181, s63, v225\n\t"
    "v_mfma_f32_32x32x16_bf16 v[112:127], a[124:127], v[212:215], v[112:127]\n\t"
    "ds_read_b128 a[124:127], v233 offset:1024\n\t"
    "v_exp_f32 v218, v218\n\t"
    "v_mfma_f32_32x32x16_bf16 v[32:47], a[128:131], v[208:211], v[32:47]\n\t"
    "v_exp_f32 v219, v219\n\t"
    "v_cvt_pk_bf16_f32 v202, v216, v217\n\t"
    "v_add_f32 v228, v228, v216\n\t"
    "v_add_f32 v228, v228, v217\n\t"
    "v_mfma_f32_32x32x16_bf16 a[144:159], a[128:131], v[212:215], a[144:159]\n\t"
    "ds_read_b128 a[128:131], v230 offset:2048\n\t"
    "v_fma_f32 v216, v166, s63, v224\n\t"
    "v_fma_f32 v217, v167, s63, v224\n\t"
    "v_exp_f32 v216, v216\n\t"
    "v_mfma_f32_32x32x16_bf16 v[48:63], a[132:135], v[208:211], v[48:63]\n\t"
    "v_exp_f32 v217, v217\n\t"
    "v_cvt_pk_bf16_f32 v206, v218, v219\n\t"
    "v_add_f32 v229, v229, v218\n\t"
    "v_mfma_f32_32x32x16_bf16 a[160:175], a[132:135], v[212:215], a[160:175]\n\t"
    "ds_read_b128 a[132:135], v231 offset:2048\n\t"
    "s_add_u32 m0, s61, 7936\n\t"
    "s_nop 0\n\t"
    "buffer_load_dwordx4 v248, s[24:27], s87 offen offset:256 nt lds\n\t"
    "v_mfma_f32_32x32x16_bf16 v[64:79], a[136:139], v[208:211], v[64:79]\n\t"
    "v_add_f32 v229, v229, v219\n\t"
    "v_fma_f32 v218, v182, s63, v225\n\t"
    "v_fma_f32 v219, v183, s63, v225\n\t"
    "v_exp_f32 v218, v218\n\t"
    "v_mfma_f32_32x32x16_bf16 a[176:191], a[136:139], v[212:215], a[176:191]\n\t"
    "ds_read_b128 a[136:139], v232 offset:2048\n\t"
    "v_exp_f32 v219, v219\n\t"
    "v_cvt_pk_bf16_f32 v203, v216, v217\n\t"
    "v_mfma_f32_32x32x16_bf16 v[80:95], a[140:143], v[208:211], v[80:95]\n\t"
    "v_add_f32 v228, v228, v216\n\t"
    "v_add_f32 v228, v228, v217\n\t"
    "s_nop 0\n\t"
    "v_cvt_pk_bf16_f32 v207, v218, v219\n\t"
    "v_mfma_f32_32x32x16_bf16 a[192:207], a[140:143], v[212:215], a[192:207]\n\t"
    "ds_read_b128 a[140:143], v233 offset:2048\n\t"
    "v_add_f32 v229, v229, v218\n\t"
    "v_add_f32 v229, v229, v219\n\t"
    "v_mfma_f32_32x32x16_bf16 v[128:143], a[96:99], a[0:3], 0\n\t"
    "v_fma_f32 v216, v168, s63, v224\n\t"
    "v_fma_f32 v217, v169, s63, v224\n\t"
    "v_exp_f32 v216, v216\n\t"
    "v_exp_f32 v217, v217\n\t"
    "v_mfma_f32_32x32x16_bf16 v[144:159], a[96:99], a[48:51], 0\n\t"
    "v_fma_f32 v218, v184, s63, v225\n\t"
    "v_fma_f32 v219, v185, s63, v225\n\t"
    "v_exp_f32 v218, v218\n\t"
    "v_mfma_f32_32x32x16_bf16 v[128:143], a[100:103], a[4:7], v[128:143]\n\t"
    "v_exp_f32 v219, v219\n\t"
    "v_cvt_pk_bf16_f32 v208, v216, v217\n\t"
    "v_add_f32 v228, v228, v216\n\t"
    "v_add_f32 v228, v228, v217\n\t"
    "v_fma_f32 v216, v170, s63, v224\n\t"
    "v_mfma_f32_32x32x16_bf16 v[144:159], a[100:103], a[52:55], v[144:159]\n\t"
    "v_fma_f32 v217, v171, s63, v224\n\t"
    "s_waitcnt vmcnt(3) lgkmcnt(0)\n\t"
    "s_barrier\n\t"
    "v_mfma_f32_32x32x16_bf16 v[128:143], a[104:107], a[8:11], v[128:143]\n\t"
    "ds_read_b64_tr_b16 a[96:97], v234 offset:24576\n\t"
    "ds_read_b64_tr_b16 a[98:99], v234 offset:27648\n\t"
    "s_mov_b32 m0, s49\n\t"
    "s_nop 0\n\t"
    "buffer_load_dwordx4 v238, s[20:23], s34 offen lds\n\t"
    "v_mfma_f32_32x32x16_bf16 v[144:159], a[104:107], a[56:59], v[144:159]\n\t"
    "ds_read_b64_tr_b16 a[100:101], v234 offset:25088\n\t"
    "ds_read_b64_tr_b16 a[102:103], v234 offset:28160\n\t"
    "v_exp_f32 v216, v216\n\t"
    "v_exp_f32 v217, v217\n\t"
    "v_mfma_f32_32x32x16_bf16 v[128:143], a[108:111], a[12:15], v[128:143]\n\t"
    "ds_read_b64_tr_b16 a[104:105], v234 offset:25600\n\t"
    "ds_read_b64_tr_b16 a[106:107], v234 offset:28672\n\t"
    "buffer_load_dwordx4 v239, s[20:23], s34 offen offset:1024 lds\n\t"
    "v_cvt_pk_bf16_f32 v212, v218, v219\n\t"
    "v_mfma_f32_32x32x16_bf16 v[144:159], a[108:111], a[60:63], v[144:159]\n\t"
    "ds_read_b64_tr_b16 a[108:109], v234 offset:26112\n\t"
    "ds_read_b64_tr_b16 a[110:111], v234 offset:29184\n\t"
    "v_add_f32 v229, v229, v218\n\t"
    "v_add_f32 v229, v229, v219\n\t"
    "v_fma_f32 v218, v186, s63, v225\n\t"
    "v_fma_f32 v219, v187, s63, v225\n\t"
    "v_mfma_f32_32x32x16_bf16 v[128:143], a[112:115], a[16:19], v[128:143]\n\t"
    "buffer_load_dwordx4 v240, s[20:23], s34 offen offset:2048 lds\n\t"
    "s_add_u32 s34, s34, 12288\n\t"
    "v_exp_f32 v218, v218\n\t"
    "v_mfma_f32_32x32x16_bf16 v[144:159], a[112:115], a[64:67], v[144:159]\n\t"
    "ds_read_b64_tr_b16 a[112:113], v234 offset:26624\n\t"
    "ds_read_b64_tr_b16 a[114:115], v234 offset:29696\n\t"
    "v_exp_f32 v219, v219\n\t"
    "v_cvt_pk_bf16_f32 v209, v216, v217\n\t"
    "v_add_f32 v228, v228, v216\n\t"
    "v_mfma_f32_32x32x16_bf16 v[128:143], a[116:119], a[20:23], v[128:143]\n\t"
    "s_add_u32 m0, s49, 12288\n\t"
    "s_nop 0\n\t"
    "buffer_load_dwordx4 v238, s[20:23], s35 offen lds\n\t"
    "v_add_f32 v228, v228, v217\n\t"
    "v_fma_f32 v216, v172, s63, v224\n\t"
    "v_mfma_f32_32x32x16_bf16 v[144:159], a[116:119], a[68:71], v[144:159]\n\t"
    "ds_read_b64_tr_b16 a[116:117], v234 offset:27136\n\t"
    "ds_read_b64_tr_b16 a[118:119], v234 offset:30208\n\t"
    "v_fma_f32 v217, v173, s63, v224\n\t"
    "v_exp_f32 v216, v216\n\t"
    "v_mfma_f32_32x32x16_bf16 v[128:143], a[120:123], a[24:27], v[128:143]\n\t"
    "buffer_load_dwordx4 v239, s[20:23], s35 offen offset:1024 lds\n\t"
    "v_exp_f32 v217, v217\n\t"
    "v_cvt_pk_bf16_f32 v213, v218, v219\n\t"
    "v_add_f32 v229, v229, v218\n\t"
    "v_add_f32 v229, v229, v219\n\t"
    "v_mfma_f32_32x32x16_bf16 v[144:159], a[120:123], a[72:75], v[144:159]\n\t"
    "ds_read_b64_tr_b16 a[120:121], v234 offset:30720\n\t"
    "ds_read_b64_tr_b16 a[122:123], v234 offset:33792\n\t"
    "v_fma_f32 v218, v188, s63, v225\n\t"
    "v_fma_f32 v219, v189, s63, v225\n\t"
    "v_mfma_f32_32x32x16_bf16 v[128:143], a[124:127], a[28:31], v[128:143]\n\t"
    "buffer_load_dwordx4 v240, s[20:23], s35 offen offset:2048 lds\n\t"
    "s_add_u32 s35, s35, 12288\n\t"
    "v_exp_f32 v218, v218\n\t"
    "v_exp_f32 v219, v219\n\t"
    "v_mfma_f32_32x32x16_bf16 v[144:159], a[124:127], a[76:79], v[144:159]\n\t"
    "ds_read_b64_tr_b16 a[124:125], v234 offset:31232\n\t"
    "ds_read_b64_tr_b16 a[126:127], v234 offset:34304\n\t"
    "v_cvt_pk_bf16_f32 v210, v216, v217\n\t"
    "v_add_f32 v228, v228, v216\n\t"
    "v_add_f32 v228, v228, v217\n\t"
    "v_mfma_f32_32x32x16_bf16 v[128:143], a[128:131], a[32:35], v[128:143]\n\t"
    "s_mov_b32 m0, s46\n\t"
    "s_nop 0\n\t"
    "buffer_load_dwordx4 v235, s[16:19], s32 offen lds\n\t"
    "v_fma_f32 v216, v174, s63, v224\n\t"
    "v_fma_f32 v217, v175, s63, v224\n\t"
    "v_mfma_f32_32x32x16_bf16 v[144:159], a[128:131], a[80:83], v[144:159]\n\t"
    "ds_read_b64_tr_b16 a[128:129], v234 offset:31744\n\t"
    "ds_read_b64_tr_b16 a[130:131], v234 offset:34816\n\t"
    "v_exp_f32 v216, v216\n\t"
    "v_exp_f32 v217, v217\n\t"
    "v_mfma_f32_32x32x16_bf16 v[128:143], a[132:135], a[36:39], v[128:143]\n\t"
    "buffer_load_dwordx4 v236, s[16:19], s32 offen offset:1024 lds\n\t"
    "v_cvt_pk_bf16_f32 v214, v218, v219\n\t"
    "v_add_f32 v229, v229, v218\n\t"
    "v_add_f32 v229, v229, v219\n\t"
    "v_fma_f32 v218, v190, s63, v225\n\t"
    "v_mfma_f32_32x32x16_bf16 v[144:159], a[132:135], a[84:87], v[144:159]\n\t"
    "ds_read_b64_tr_b16 a[132:133], v234 offset:32256\n\t"
    "ds_read_b64_tr_b16 a[134:135], v234 offset:35328\n\t"
    "v_fma_f32 v219, v191, s63, v225\n\t"
    "v_exp_f32 v218, v218\n\t"
    "v_mfma_f32_32x32x16_bf16 v[128:143], a[136:139], a[40:43], v[128:143]\n\t"
    "buffer_load_dwordx4 v237, s[16:19], s32 offen offset:2048 lds\n\t"
    "s_add_u32 s32, s32, 12288\n\t"
    "v_exp_f32 v219, v219\n\t"
    "v_cvt_pk_bf16_f32 v211, v216, v217\n\t"
    "v_mfma_f32_32x32x16_bf16 v[144:159], a[136:139], a[88:91], v[144:159]\n\t"
    "ds_read_b64_tr_b16 a[136:137], v234 offset:32768\n\t"
    "ds_read_b64_tr_b16 a[138:139], v234 offset:35840\n\t"
    "v_add_f32 v228, v228, v216\n\t"
    "v_add_f32 v228, v228, v217\n\t"
    "s_nop 0\n\t"
    "v_mfma_f32_32x32x16_bf16 v[128:143], a[140:143], a[44:47], v[128:143]\n\t"
    "s_add_u32 m0, s46, 12288\n\t"
    "s_nop 0\n\t"
    "buffer_load_dwordx4 v235, s[16:19], s33 offen lds\n\t"
    "v_cvt_pk_bf16_f32 v215, v218, v219\n\t"
    "v_add_f32 v229, v229, v218\n\t"
    "v_add_f32 v229, v229, v219\n\t"
    "v_mfma_f32_32x32x16_bf16 v[144:159], a[140:143], a[92:95], v[144:159]\n\t"
    "ds_read_b64_tr_b16 a[140:141], v234 offset:33280\n\t"
    "ds_read_b64_tr_b16 a[142:143], v234 offset:36352\n\t"
    "v_add_f32 v251, v228, v229\n\t"
    "v_cmp_nge_f32 vcc, s64, v251\n\t"
    "s_cbranch_vccnz .Ld_stub25\n\t"
    ".Ld_back25:\n\t"
    "s_waitcnt lgkmcnt(12)\n\t"
    "v_mfma_f32_32x32x16_bf16 v[0:15], a[96:99], v[200:203], v[0:15]\n\t"
    "buffer_load_dwordx4 v236, s[16:19], s33 offen offset:1024 lds\n\t"
    "v_add_f32 v226, v226, v228\n\t"
    "v_add_f32 v227, v227, v229\n\t"
    "v_mfma_f32_32x32x16_bf16 v[96:111], a[96:99], v[204:207], v[96:111]\n\t"
    "ds_read_b128 a[96:99], v230 offset:24576\n\t"
    "v_mfma_f32_32x32x16_bf16 v[16:31], a[100:103], v[200:203], v[16:31]\n\t"
    "buffer_load_dwordx4 v237, s[16:19], s33 offen offset:2048 lds\n\t"
    "s_add_u32 s33, s33, 12288\n\t"
    "v_mfma_f32_32x32x16_bf16 v[112:127], a[100:103], v[204:207], v[112:127]\n\t"
    "ds_read_b128 a[100:103], v231 offset:24576\n\t"
    "v_fma_f32 v216, v128, s63, v224\n\t"
    "v_fma_f32 v217, v129, s63, v224\n\t"
    "v_exp_f32 v216, v216\n\t"
    "v_mfma_f32_32x32x16_bf16 v[32:47], a[104:107], v[200:203], v[32:47]\n\t"
    "s_add_u32 m0, s61, 8960\n\t"
    "s_nop 0\n\t"
    "buffer_load_dwordx4 v249, s[24:27], s87 offen offset:256 nt lds\n\t"
    "v_mfma_f32_32x32x16_bf16 a[144:159], a[104:107], v[204:207], a[144:159]\n\t"
    "ds_read_b128 a[104:107], v232 offset:24576\n\t"
    "v_exp_f32 v217, v217\n\t"
    "v_fma_f32 v218, v144, s63, v225\n\t"
    "v_fma_f32 v219, v145, s63, v225\n\t"
    "v_mfma_f32_32x32x16_bf16 v[48:63], a[108:111], v[200:203], v[48:63]\n\t"
    "v_exp_f32 v218, v218\n\t"
    "v_exp_f32 v219, v219\n\t"
    "v_cvt_pk_bf16_f32 v192, v216, v217\n\t"
    "v_mfma_f32_32x32x16_bf16 a[160:175], a[108:111], v[204:207], a[160:175]\n\t"
    "ds_read_b128 a[108:111], v233 offset:24576\n\t"
    "v_add_f32 v228, v216, v217\n\t"
    "v_fma_f32 v216, v130, s63, v224\n\t"
    "v_fma_f32 v217, v131, s63, v224\n\t"
    "v_mfma_f32_32x32x16_bf16 v[64:79], a[112:115], v[200:203], v[64:79]\n\t"
    "v_exp_f32 v216, v216\n\t"
    "v_exp_f32 v217, v217\n\t"
    "v_mfma_f32_32x32x16_bf16 a[176:191], a[112:115], v[204:207], a[176:191]\n\t"
    "ds_read_b128 a[112:115], v230 offset:25600\n\t"
    "v_cvt_pk_bf16_f32 v196, v218, v219\n\t"
    "v_add_f32 v229, v218, v219\n\t"
    "v_fma_f32 v218, v146, s63, v225\n\t"
    "v_fma_f32 v219, v147, s63, v225\n\t"
    "v_mfma_f32_32x32x16_bf16 v[80:95], a[116:119], v[200:203], v[80:95]\n\t"
    "s_add_u32 m0, s61, 9984\n\t"
    "s_nop 0\n\t"
    "buffer_load_dwordx4 v248, s[24:27], s43 offen offset:256 nt lds\n\t"
    "v_exp_f32 v218, v218\n\t"
    "v_mfma_f32_32x32x16_bf16 a[192:207], a[116:119], v[204:207], a[192:207]\n\t"
    "ds_read_b128 a[116:119], v231 offset:25600\n\t"
    "v_exp_f32 v219, v219\n\t"
    "v_cvt_pk_bf16_f32 v193, v216, v217\n\t"
    "s_waitcnt lgkmcnt(10)\n\t"
    "v_mfma_f32_32x32x16_bf16 v[0:15], a[120:123], v[208:211], v[0:15]\n\t"
    "v_add_f32 v228, v228, v216\n\t"
    "v_add_f32 v228, v228, v217\n\t"
    "v_fma_f32 v216, v132, s63, v224\n\t"
    "v_fma_f32 v217, v133, s63, v224\n\t"
    "v_mfma_f32_32x32x16_bf16 v[96:111], a[120:123], v[212:215], v[96:111]\n\t"
    "ds_read_b128 a[120:123], v232 offset:25600\n\t"
    "v_exp_f32 v216, v216\n\t"
    "v_exp_f32 v217, v217\n\t"
    "v_mfma_f32_32x32x16_bf16 v[16:31], a[124:127], v[208:211], v[16:31]\n\t"
    "v_cvt_pk_bf16_f32 v197, v218, v219\n\t"
    "v_add_f32 v229, v229, v218\n\t"
    "v_add_f32 v229, v229, v219\n\t"
    "v_fma_f32 v218, v148, s63, v225\n\t"
    "v_mfma_f32_32x32x16_bf16 v[112:127], a[124:127], v[212:215], v[112:127]\n\t"
    "ds_read_b128 a[124:127], v233 offset:25600\n\t"
    "v_fma_f32 v219, v149, s63, v225\n\t"
    "v_exp_f32 v218, v218\n\t"
    "v_mfma_f32_32x32x16_bf16 v[32:47], a[128:131], v[208:211], v[32:47]\n\t"
    "s_add_u32 m0, s61, 11008\n\t"
    "s_nop 0\n\t"
    "buffer_load_dwordx4 v249, s[24:27], s43 offen offset:256 nt lds\n\t"
    "v_exp_f32 v219, v219\n\t"
    "v_mfma_f32_32x32x16_bf16 a[144:159], a[128:131], v[212:215], a[144:159]\n\t"
    "ds_read_b128 a[128:131], v230 offset:26624\n\t"
    "v_cvt_pk_bf16_f32 v194, v216, v217\n\t"
    "v_add_f32 v228, v228, v216\n\t"
    "v_add_f32 v228, v228, v217\n\t"
    "v_mfma_f32_32x32x16_bf16 v[48:63], a[132:135], v[208:211], v[48:63]\n\t"
    "v_fma_f32 v216, v134, s63, v224\n\t"
    "v_fma_f32 v217, v135, s63, v224\n\t"
    "v_exp_f32 v216, v216\n\t"
    "v_mfma_f32_32x32x16_bf16 a[160:175], a[132:135], v[212:215], a[160:175]\n\t"
    "ds_read_b128 a[132:135], v231 offset:26624\n\t"
    "v_exp_f32 v217, v217\n\t"
    "v_cvt_pk_bf16_f32 v198, v218, v219\n\t"
    "v_add_f32 v229, v229, v218\n\t"
    "s_waitcnt lgkmcnt(10)\n\t"
    "v_mfma_f32_32x32x16_bf16 v[64:79], a[136:139], v[208:211], v[64:79]\n\t"
    "v_add_f32 v229, v229, v219\n\t"
    "v_fma_f32 v218, v150, s63, v225\n\t"
    "v_fma_f32 v219, v151, s63, v225\n\t"
    "v_exp_f32 v218, v218\n\t"
    "v_mfma_f32_32x32x16_bf16 a[176:191], a[136:139], v[212:215], a[176:191]\n\t"
    "ds_read_b128 a[136:139], v232 offset:26624\n\t"
    "v_exp_f32 v219, v219\n\t"
    "v_cvt_pk_bf16_f32 v195, v216, v217\n\t"
    "v_mfma_f32_32x32x16_bf16 v[80:95], a[140:143], v[208:211], v[80:95]\n\t"
    "v_add_f32 v228, v228, v216\n\t"
    "v_add_f32 v228, v228, v217\n\t"
    "s_nop 0\n\t"
    "v_cvt_pk_bf16_f32 v199, v218, v219\n\t"
    "v_mfma_f32_32x32x16_bf16 a[192:207], a[140:143], v[212:215], a[192:207]\n\t"
    "ds_read_b128 a[140:143], v233 offset:26624\n\t"
    "v_add_f32 v229, v229, v218\n\t"
    "v_add_f32 v229, v229, v219\n\t"
    "s_waitcnt lgkmcnt(10)\n\t"
    "v_mfma_f32_32x32x16_bf16 v[160:175], a[96:99], a[0:3], 0\n\t"
    "v_fma_f32 v216, v136, s63, v224\n\t"
    "v_fma_f32 v217, v137, s63, v224\n\t"
    "v_exp_f32 v216, v216\n\t"
    "v_exp_f32 v217, v217\n\t"
    "v_mfma_f32_32x32x16_bf16 v[176:191], a[96:99], a[48:51], 0\n\t"
    "v_fma_f32 v218, v152, s63, v225\n\t"
    "v_fma_f32 v219, v153, s63, v225\n\t"
    "v_exp_f32 v218, v218\n\t"
    "s_waitcnt lgkmcnt(9)\n\t"
    "v_mfma_f32_32x32x16_bf16 v[160:175], a[100:103], a[4:7], v[160:175]\n\t"
    "v_exp_f32 v219, v219\n\t"
    "v_cvt_pk_bf16_f32 v208, v216, v217\n\t"
    "v_add_f32 v228, v228, v216\n\t"
    "v_add_f32 v228, v228, v217\n\t"
    "v_mfma_f32_32x32x16_bf16 v[176:191], a[100:103], a[52:55], v[176:191]\n\t"
    "v_fma_f32 v216, v138, s63, v224\n\t"
    "v_fma_f32 v217, v139, s63, v224\n\t"
    "s_waitcnt vmcnt(3) lgkmcnt(0)\n\t"
    "s_barrier\n\t"
    "v_mfma_f32_32x32x16_bf16 v[160:175], a[104:107], a[8:11], v[160:175]\n\t"
    "ds_read_b64_tr_b16 a[96:97], v234 offset:0\n\t"
    "ds_read_b64_tr_b16 a[98:99], v234 offset:3072\n\t"
    "s_add_u32 m0, s49, 24576\n\t"
    "s_nop 0\n\t"
    "buffer_load_dwordx4 v238, s[20:23], s34 offen lds\n\t"
    "v_mfma_f32_32x32x16_bf16 v[176:191], a[104:107], a[56:59], v[176:191]\n\t"
    "ds_read_b64_tr_b16 a[100:101], v234 offset:512\n\t"
    "ds_read_b64_tr_b16 a[102:103], v234 offset:3584\n\t"
    "v_exp_f32 v216, v216\n\t"
    "v_mfma_f32_32x32x16_bf16 v[160:175], a[108:111], a[12:15], v[160:175]\n\t"
    "ds_read_b64_tr_b16 a[104:105], v234 offset:1024\n\t"
    "ds_read_b64_tr_b16 a[106:107], v234 offset:4096\n\t"
    "buffer_load_dwordx4 v239, s[20:23], s34 offen offset:1024 lds\n\t"
    "v_exp_f32 v217, v217\n\t"
    "v_cvt_pk_bf16_f32 v212, v218, v219\n\t"
    "v_mfma_f32_32x32x16_bf16 v[176:191], a[108:111], a[60:63], v[176:191]\n\t"
    "ds_read_b64_tr_b16 a[108:109], v234 offset:1536\n\t"
    "ds_read_b64_tr_b16 a[110:111], v234 offset:4608\n\t"
    "v_add_f32 v229, v229, v218\n\t"
    "v_add_f32 v229, v229, v219\n\t"
    "v_fma_f32 v218, v154, s63, v225\n\t"
    "v_mfma_f32_32x32x16_bf16 v[160:175], a[112:115], a[16:19], v[160:175]\n\t"
    "buffer_load_dwordx4 v240, s[20:23], s34 offen offset:2048 lds\n\t"
    "s_add_u32 s34, s34, 12288\n\t"
    "v_fma_f32 v219, v155, s63, v225\n\t"
    "v_exp_f32 v218, v218\n\t"
    "v_mfma_f32_32x32x16_bf16 v[176:191], a[112:115], a[64:67], v[176:191]\n\t"
    "ds_read_b64_tr_b16 a[112:113], v234 offset:2048\n\t"
    "ds_read_b64_tr_b16 a[114:115], v234 offset:5120\n\t"
    "v_exp_f32 v219, v219\n\t"
    "v_cvt_pk_bf16_f32 v209, v216, v217\n\t"
    "v_mfma_f32_32x32x16_bf16 v[160:175], a[116:119], a[20:23], v[160:175]\n\t"
    "s_add_u32 m0, s49, 36864\n\t"
    "s_nop 0\n\t"
    "buffer_load_dwordx4 v238, s[20:23], s35 offen lds\n\t"
    "v_add_f32 v228, v228, v216\n\t"
    "v_add_f32 v228, v228, v217\n\t"
    "v_fma_f32 v216, v140, s63, v224\n\t"
    "v_mfma_f32_32x32x16_bf16 v[176:191], a[116:119], a[68:71], v[176:191]\n\t"
    "ds_read_b64_tr_b16 a[116:117], v234 offset:2560\n\t"
    "ds_read_b64_tr_b16 a[118:119], v234 offset:5632\n\t"
    "v_fma_f32 v217, v141, s63, v224\n\t"
    "v_exp_f32 v216, v216\n\t"
    "v_mfma_f32_32x32x16_bf16 v[160:175], a[120:123], a[24:27], v[160:175]\n\t"
    "buffer_load_dwordx4 v239, s[20:23], s35 offen offset:1024 lds\n\t"
    "v_exp_f32 v217, v217\n\t"
    "v_cvt_pk_bf16_f32 v213, v218, v219\n\t"
    "v_add_f32 v229, v229, v218\n\t"
    "v_mfma_f32_32x32x16_bf16 v[176:191], a[120:123], a[72:75], v[176:191]\n\t"
    "ds_read_b64_tr_b16 a[120:121], v234 offset:6144\n\t"
    "ds_read_b64_tr_b16 a[122:123], v234 offset:9216\n\t"
    "v_add_f32 v229, v229, v219\n\t"
    "v_fma_f32 v218, v156, s63, v225\n\t"
    "v_fma_f32 v219, v157, s63, v225\n\t"
    "v_mfma_f32_32x32x16_bf16 v[160:175], a[124:127], a[28:31], v[160:175]\n\t"
    "buffer_load_dwordx4 v240, s[20:23], s35 offen offset:2048 lds\n\t"
    "s_add_u32 s35, s35, 12288\n\t"
    "v_exp_f32 v218, v218\n\t"
    "v_exp_f32 v219, v219\n\t"
    "v_mfma_f32_32x32x16_bf16 v[176:191], a[124:127], a[76:79], v[176:191]\n\t"
    "ds_read_b64_tr_b16 a[124:125], v234 offset:6656\n\t"
    "ds_read_b64_tr_b16 a[126:127], v234 offset:9728\n\t"
    "v_cvt_pk_bf16_f32 v210, v216, v217\n\t"
    "v_add_f32 v228, v228, v216\n\t"
    "v_mfma_f32_32x32x16_bf16 v[160:175], a[128:131], a[32:35], v[160:175]\n\t"
    "s_add_u32 m0, s46, 24576\n\t"
    "s_nop 0\n\t"
    "buffer_load_dwordx4 v235, s[16:19], s32 offen lds\n\t"
    "v_add_f32 v228, v228, v217\n\t"
    "v_fma_f32 v216, v142, s63, v224\n\t"
    "v_fma_f32 v217, v143, s63, v224\n\t"
    "v_mfma_f32_32x32x16_bf16 v[176:191], a[128:131], a[80:83], v[176:191]\n\t"
    "ds_read_b64_tr_b16 a[128:129], v234 offset:7168\n\t"
    "ds_read_b64_tr_b16 a[130:131], v234 offset:10240\n\t"
    "v_exp_f32 v216, v216\n\t"
    "v_mfma_f32_32x32x16_bf16 v[160:175], a[132:135], a[36:39], v[160:175]\n\t"
    "buffer_load_dwordx4 v236, s[16:19], s32 offen offset:1024 lds\n\t"
    "v_exp_f32 v217, v217\n\t"
    "v_cvt_pk_bf16_f32 v214, v218, v219\n\t"
    "v_add_f32 v229, v229, v218\n\t"
    "v_add_f32 v229, v229, v219\n\t"
    "v_mfma_f32_32x32x16_bf16 v[176:191], a[132:135], a[84:87], v[176:191]\n\t"
    "ds_read_b64_tr_b16 a[132:133], v234 offset:7680\n\t"
    "ds_read_b64_tr_b16 a[134:135], v234 offset:10752\n\t"
    "v_fma_f32 v218, v158, s63, v225\n\t"
    "v_fma_f32 v219, v159, s63, v225\n\t"
    "v_mfma_f32_32x32x16_bf16 v[160:175], a[136:139], a[40:43], v[160:175]\n\t"
    "buffer_load_dwordx4 v237, s[16:19], s32 offen offset:2048 lds\n\t"
    "v_exp_f32 v218, v218\n\t"
    "v_exp_f32 v219, v219\n\t"
    "v_cvt_pk_bf16_f32 v211, v216, v217\n\t"
    "v_mfma_f32_32x32x16_bf16 v[176:191], a[136:139], a[88:91], v[176:191]\n\t"
    "ds_read_b64_tr_b16 a[136:137], v234 offset:8192\n\t"
    "ds_read_b64_tr_b16 a[138:139], v234 offset:11264\n\t"
    "v_add_f32 v228, v228, v216\n\t"
    "v_add_f32 v228, v228, v217\n\t"
    "s_nop 0\n\t"
    "v_mfma_f32_32x32x16_bf16 v[160:175], a[140:143], a[44:47], v[160:175]\n\t"
    "s_add_u32 m0, s46, 36864\n\t"
    "s_nop 0\n\t"
    "buffer_load_dwordx4 v235, s[16:19], s33 offen lds\n\t"
    "v_cvt_pk_bf16_f32 v215, v218, v219\n\t"
    "v_add_f32 v229, v229, v218\n\t"
    "v_mfma_f32_32x32x16_bf16 v[176:191], a[140:143], a[92:95], v[176:191]\n\t"
    "ds_read_b64_tr_b16 a[140:141], v234 offset:8704\n\t"
    "ds_read_b64_tr_b16 a[142:143], v234 offset:11776\n\t"
    "v_add_f32 v229, v229, v219\n\t"
    "v_add_f32 v251, v228, v229\n\t"
    "v_cmp_nge_f32 vcc, s64, v251\n\t"
    "s_cbranch_vccnz .Ld_stub26\n\t"
    ".Ld_back26:\n\t"
    "s_waitcnt lgkmcnt(12)\n\t"
    "v_mfma_f32_32x32x16_bf16 v[0:15], a[96:99], v[192:195], v[0:15]\n\t"
    "buffer_load_dwordx4 v236, s[16:19], s33 offen offset:1024 lds\n\t"
    "v_add_f32 v226, v226, v228\n\t"
    "v_add_f32 v227, v227, v229\n\t"
    "v_mfma_f32_32x32x16_bf16 v[96:111], a[96:99], v[196:199], v[96:111]\n\t"
    "ds_read_b128 a[96:99], v230 offset:0\n\t"
    "v_mfma_f32_32x32x16_bf16 v[16:31], a[100:103], v[192:195], v[16:31]\n\t"
    "buffer_load_dwordx4 v237, s[16:19], s33 offen offset:2048 lds\n\t"
    "v_mfma_f32_32x32x16_bf16 v[112:127], a[100:103], v[196:199], v[112:127]\n\t"
    "ds_read_b128 a[100:103], v231 offset:0\n\t"
    "v_fma_f32 v216, v160, s63, v224\n\t"
    "v_fma_f32 v217, v161, s63, v224\n\t"
    "v_exp_f32 v216, v216\n\t"
    "v_mfma_f32_32x32x16_bf16 v[32:47], a[104:107], v[192:195], v[32:47]\n\t"
    "v_exp_f32 v217, v217\n\t"
    "v_fma_f32 v218, v176, s63, v225\n\t"
    "v_mfma_f32_32x32x16_bf16 a[144:159], a[104:107], v[196:199], a[144:159]\n\t"
    "ds_read_b128 a[104:107], v232 offset:0\n\t"
    "v_fma_f32 v219, v177, s63, v225\n\t"
    "v_exp_f32 v218, v218\n\t"
    "v_mfma_f32_32x32x16_bf16 v[48:63], a[108:111], v[192:195], v[48:63]\n\t"
    "v_exp_f32 v219, v219\n\t"
    "v_cvt_pk_bf16_f32 v200, v216, v217\n\t"
    "v_add_f32 v228, v216, v217\n\t"
    "v_fma_f32 v216, v162, s63, v224\n\t"
    "v_mfma_f32_32x32x16_bf16 a[160:175], a[108:111], v[196:199], a[160:175]\n\t"
    "ds_read_b128 a[108:111], v233 offset:0\n\t"
    "v_fma_f32 v217, v163, s63, v224\n\t"
    "v_exp_f32 v216, v216\n\t"
    "v_mfma_f32_32x32x16_bf16 v[64:79], a[112:115], v[192:195], v[64:79]\n\t"
    "v_exp_f32 v217, v217\n\t"
    "v_cvt_pk_bf16_f32 v204, v218, v219\n\t"
    "v_add_f32 v229, v218, v219\n\t"
    "v_mfma_f32_32x32x16_bf16 a[176:191], a[112:115], v[196:199], a[176:191]\n\t"
    "ds_read_b128 a[112:115], v230 offset:1024\n\t"
    "v_fma_f32 v218, v178, s63, v225\n\t"
    "v_fma_f32 v219, v179, s63, v225\n\t"
    "v_exp_f32 v218, v218\n\t"
    "v_mfma_f32_32x32x16_bf16 v[80:95], a[116:119], v[192:195], v[80:95]\n\t"
    "v_exp_f32 v219, v219\n\t"
    "v_cvt_pk_bf16_f32 v201, v216, v217\n\t"
    "v_add_f32 v228, v228, v216\n\t"
    "v_mfma_f32_32x32x16_bf16 a[192:207], a[116:119], v[196:199], a[192:207]\n\t"
    "ds_read_b128 a[116:119], v231 offset:1024\n\t"
    "v_add_f32 v228, v228, v217\n\t"
    "v_fma_f32 v216, v164, s63, v224\n\t"
    "v_fma_f32 v217, v165, s63, v224\n\t"
    "s_waitcnt lgkmcnt(10)\n\t"
    "v_mfma_f32_32x32x16_bf16 v[0:15], a[120:123], v[208:211], v[0:15]\n\t"
    "v_exp_f32 v216, v216\n\t"
    "v_exp_f32 v217, v217\n\t"
    "v_mfma_f32_32x32x16_bf16 v[96:111], a[120:123], v[212:215], v[96:111]\n\t"
    "ds_read_b128 a[120:123], v232 offset:1024\n\t"
    "v_cvt_pk_bf16_f32 v205, v218, v219\n\t"
    "v_add_f32 v229, v229, v218\n\t"
    "v_add_f32 v229, v229, v219\n\t"
    "v_mfma_f32_32x32x16_bf16 v[16:31], a[124:127], v[208:211], v[16:31]\n\t"
    "v_fma_f32 v218, v180, s63, v225\n\t"
    "v_fma_f32 v219, v181, s63, v225\n\t"
    "v_exp_f32 v218, v218\n\t"
    "v_mfma_f32_32x32x16_bf16 v[112:127], a[124:127], v[212:215], v[112:127]\n\t"
    "ds_read_b128 a[124:127], v233 offset:1024\n\t"
    "v_exp_f32 v219, v219\n\t"
    "v_cvt_pk_bf16_f32 v202, v216, v217\n\t"
    "v_add_f32 v228, v228, v216\n\t"
    "v_mfma_f32_32x32x16_bf16 v[32:47], a[128:131], v[208:211], v[32:47]\n\t"
    "s_waitcnt vmcnt(12)\n\t"
    "ds_read_b128 a[240:243], v244 offset:8192\n\t"
    "v_add_f32 v228, v228, v217\n\t"
    "v_fma_f32 v216, v166, s63, v224\n\t"
    "v_mfma_f32_32x32x16_bf16 a[144:159], a[128:131], v[212:215], a[144:159]\n\t"
    "ds_read_b128 a[128:131], v230 offset:2048\n\t"
    "v_fma_f32 v217, v167, s63, v224\n\t"
    "v_exp_f32 v216, v216\n\t"
    "v_mfma_f32_32x32x16_bf16 v[48:63], a[132:135], v[208:211], v[48:63]\n\t"
    "ds_read_b128 a[244:247], v245 offset:8192\n\t"
    "v_exp_f32 v217, v217\n\t"
    "v_cvt_pk_bf16_f32 v206, v218, v219\n\t"
    "v_mfma_f32_32x32x16_bf16 a[160:175], a[132:135], v[212:215], a[160:175]\n\t"
    "ds_read_b128 a[132:135], v231 offset:2048\n\t"
    "v_add_f32 v229, v229, v218\n\t"
    "v_add_f32 v229, v229, v219\n\t"
    "v_fma_f32 v218, v182, s63, v225\n\t"
    "s_waitcnt lgkmcnt(12)\n\t"
    "v_mfma_f32_32x32x16_bf16 v[64:79], a[136:139], v[208:211], v[64:79]\n\t"
    "ds_read_b128 a[248:251], v246 offset:8192\n\t"
    "v_fma_f32 v219, v183, s63, v225\n\t"
    "v_exp_f32 v218, v218\n\t"
    "v_mfma_f32_32x32x16_bf16 a[176:191], a[136:139], v[212:215], a[176:191]\n\t"
    "ds_read_b128 a[136:139], v232 offset:2048\n\t"
    "v_exp_f32 v219, v219\n\t"
    "v_cvt_pk_bf16_f32 v203, v216, v217\n\t"
    "v_add_f32 v228, v228, v216\n\t"
    "v_mfma_f32_32x32x16_bf16 v[80:95], a[140:143], v[208:211], v[80:95]\n\t"
    "ds_read_b128 a[252:255], v247 offset:8192\n\t"
    "v_add_f32 v228, v228, v217\n\t"
    "s_nop 0\n\t"
    "v_cvt_pk_bf16_f32 v207, v218, v219\n\t"
    "v_mfma_f32_32x32x16_bf16 a[192:207], a[140:143], v[212:215], a[192:207]\n\t"
    "ds_read_b128 a[140:143], v233 offset:2048\n\t"
    "v_add_f32 v229, v229, v218\n\t"
    "v_add_f32 v229, v229, v219\n\t"
    "s_waitcnt lgkmcnt(14)\n\t"
    "v_mfma_f32_32x32x16_bf16 v[128:143], a[96:99], a[0:3], 0\n\t"
    "v_fma_f32 v216, v168, s63, v224\n\t"
    "v_fma_f32 v217, v169, s63, v224\n\t"
    "v_exp_f32 v216, v216\n\t"
    "v_exp_f32 v217, v217\n\t"
    "v_mfma_f32_32x32x16_bf16 v[144:159], a[96:99], a[48:51], 0\n\t"
    "s_waitcnt lgkmcnt(1)\n\t"
    "s_add_u32 m0, s61, 7936\n\t"
    "s_nop 0\n\t"
    "buffer_load_dwordx4 v248, s[24:27], 0 offen offset:256 nt lds\n\t"
    "v_fma_f32 v218, v184, s63, v225\n\t"
    "v_fma_f32 v219, v185, s63, v225\n\t"
    "v_mfma_f32_32x32x16_bf16 v[128:143], a[100:103], a[4:7], v[128:143]\n\t"
    "v_exp_f32 v218, v218\n\t"
    "v_exp_f32 v219, v219\n\t"
    "v_cvt_pk_bf16_f32 v208, v216, v217\n\t"
    "v_mfma_f32_32x32x16_bf16 v[144:159], a[100:103], a[52:55], v[144:159]\n\t"
    "v_add_f32 v228, v228, v216\n\t"
    "s_waitcnt vmcnt(1) lgkmcnt(0)\n\t"
    "s_barrier\n\t"
    "v_mfma_f32_32x32x16_bf16 v[128:143], a[104:107], a[8:11], v[128:143]\n\t"
    "ds_read_b64_tr_b16 a[96:97], v234 offset:24576\n\t"
    "ds_read_b64_tr_b16 a[98:99], v234 offset:27648\n\t"
    "s_mov_b32 m0, s49\n\t"
    "s_nop 0\n\t"
    "buffer_load_dwordx4 v238, s[20:23], s34 offen lds\n\t"
    "v_mfma_f32_32x32x16_bf16 v[144:159], a[104:107], a[56:59], v[144:159]\n\t"
    "ds_read_b64_tr_b16 a[100:101], v234 offset:25088\n\t"
    "ds_read_b64_tr_b16 a[102:103], v234 offset:28160\n\t"
    "v_add_f32 v228, v228, v217\n\t"
    "v_fma_f32 v216, v170, s63, v224\n\t"
    "v_fma_f32 v217, v171, s63, v224\n\t"
    "v_mfma_f32_32x32x16_bf16 v[128:143], a[108:111], a[12:15], v[128:143]\n\t"
    "ds_read_b64_tr_b16 a[104:105], v234 offset:25600\n\t"
    "ds_read_b64_tr_b16 a[106:107], v234 offset:28672\n\t"
    "buffer_load_dwordx4 v239, s[20:23], s34 offen offset:1024 lds\n\t"
    "v_exp_f32 v216, v216\n\t"
    "v_exp_f32 v217, v217\n\t"
    "v_mfma_f32_32x32x16_bf16 v[144:159], a[108:111], a[60:63], v[144:159]\n\t"
    "ds_read_b64_tr_b16 a[108:109], v234 offset:26112\n\t"
    "ds_read_b64_tr_b16 a[110:111], v234 offset:29184\n\t"
    "v_cvt_pk_bf16_f32 v212, v218, v219\n\t"
    "v_add_f32 v229, v229, v218\n\t"
    "v_mfma_f32_32x32x16_bf16 v[128:143], a[112:115], a[16:19], v[128:143]\n\t"
    "buffer_load_dwordx4 v240, s[20:23], s34 offen offset:2048 lds\n\t"
    "s_add_u32 s34, s34, 12288\n\t"
    "v_add_f32 v229, v229, v219\n\t"
    "v_fma_f32 v218, v186, s63, v225\n\t"
    "v_fma_f32 v219, v187, s63, v225\n\t"
    "v_mfma_f32_32x32x16_bf16 v[144:159], a[112:115], a[64:67], v[144:159]\n\t"
    "ds_read_b64_tr_b16 a[112:113], v234 offset:26624\n\t"
    "ds_read_b64_tr_b16 a[114:115], v234 offset:29696\n\t"
    "v_exp_f32 v218, v218\n\t"
    "v_exp_f32 v219, v219\n\t"
    "v_mfma_f32_32x32x16_bf16 v[128:143], a[116:119], a[20:23], v[128:143]\n\t"
    "s_add_u32 m0, s49, 12288\n\t"
    "s_nop 0\n\t"
    "buffer_load_dwordx4 v238, s[20:23], s35 offen lds\n\t"
    "v_cvt_pk_bf16_f32 v209, v216, v217\n\t"
    "v_add_f32 v228, v228, v216\n\t"
    "v_mfma_f32_32x32x16_bf16 v[144:159], a[116:119], a[68:71], v[144:159]\n\t"
    "ds_read_b64_tr_b16 a[116:117], v234 offset:27136\n\t"
    "ds_read_b64_tr_b16 a[118:119], v234 offset:30208\n\t"
    "v_add_f32 v228, v228, v217\n\t"
    "v_fma_f32 v216, v172, s63, v224\n\t"
    "v_fma_f32 v217, v173, s63, v224\n\t"
    "v_mfma_f32_32x32x16_bf16 v[128:143], a[120:123], a[24:27], v[128:143]\n\t"
    "buffer_load_dwordx4 v239, s[20:23], s35 offen offset:1024 lds\n\t"
    "v_exp_f32 v216, v216\n\t"
    "v_exp_f32 v217, v217\n\t"
    "v_cvt_pk_bf16_f32 v213, v218, v219\n\t"
    "v_mfma_f32_32x32x16_bf16 v[144:159], a[120:123], a[72:75], v[144:159]\n\t"
    "ds_read_b64_tr_b16 a[120:121], v234 offset:30720\n\t"
    "ds_read_b64_tr_b16 a[122:123], v234 offset:33792\n\t"
    "v_add_f32 v229, v229, v218\n\t"
    "v_add_f32 v229, v229, v219\n\t"
    "v_fma_f32 v218, v188, s63, v225\n\t"
    "v_mfma_f32_32x32x16_bf16 v[128:143], a[124:127], a[28:31], v[128:143]\n\t"
    "buffer_load_dwordx4 v240, s[20:23], s35 offen offset:2048 lds\n\t"
    "s_add_u32 s35, s35, 12288\n\t"
    "v_fma_f32 v219, v189, s63, v225\n\t"
    "v_exp_f32 v218, v218\n\t"
    "v_mfma_f32_32x32x16_bf16 v[144:159], a[124:127], a[76:79], v[144:159]\n\t"
    "ds_read_b64_tr_b16 a[124:125], v234 offset:31232\n\t"
    "ds_read_b64_tr_b16 a[126:127], v234 offset:34304\n\t"
    "v_exp_f32 v219, v219\n\t"
    "v_cvt_pk_bf16_f32 v210, v216, v217\n\t"
    "v_add_f32 v228, v228, v216\n\t"
    "v_mfma_f32_32x32x16_bf16 v[128:143], a[128:131], a[32:35], v[128:143]\n\t"
    "s_mov_b32 m0, s46\n\t"
    "s_mov_b32 s18, s42\n\t"
    "buffer_load_dwordx4 v235, s[16:19], s36 offen lds\n\t"
    "v_add_f32 v228, v228, v217\n\t"
    "v_fma_f32 v216, v174, s63, v224\n\t"
    "v_mfma_f32_32x32x16_bf16 v[144:159], a[128:131], a[80:83], v[144:159]\n\t"
    "ds_read_b64_tr_b16 a[128:129], v234 offset:31744\n\t"
    "ds_read_b64_tr_b16 a[130:131], v234 offset:34816\n\t"
    "v_fma_f32 v217, v175, s63, v224\n\t"
    "v_exp_f32 v216, v216\n\t"
    "v_mfma_f32_32x32x16_bf16 v[128:143], a[132:135], a[36:39], v[128:143]\n\t"
    "buffer_load_dwordx4 v236, s[16:19], s36 offen offset:1024 lds\n\t"
    "v_exp_f32 v217, v217\n\t"
    "v_cvt_pk_bf16_f32 v214, v218, v219\n\t"
    "v_add_f32 v229, v229, v218\n\t"
    "v_add_f32 v229, v229, v219\n\t"
    "v_mfma_f32_32x32x16_bf16 v[144:159], a[132:135], a[84:87], v[144:159]\n\t"
    "ds_read_b64_tr_b16 a[132:133], v234 offset:32256\n\t"
    "ds_read_b64_tr_b16 a[134:135], v234 offset:35328\n\t"
    "v_fma_f32 v218, v190, s63, v225\n\t"
    "v_fma_f32 v219, v191, s63, v225\n\t"
    "v_mfma_f32_32x32x16_bf16 v[128:143], a[136:139], a[40:43], v[128:143]\n\t"
    "buffer_load_dwordx4 v237, s[16:19], s36 offen offset:2048 lds\n\t"
    "v_exp_f32 v218, v218\n\t"
    "v_exp_f32 v219, v219\n\t"
    "v_cvt_pk_bf16_f32 v211, v216, v217\n\t"
    "v_mfma_f32_32x32x16_bf16 v[144:159], a[136:139], a[88:91], v[144:159]\n\t"
    "ds_read_b64_tr_b16 a[136:137], v234 offset:32768\n\t"
    "ds_read_b64_tr_b16 a[138:139], v234 offset:35840\n\t"
    "v_add_f32 v228, v228, v216\n\t"
    "v_add_f32 v228, v228, v217\n\t"
    "s_nop 0\n\t"
    "v_cvt_pk_bf16_f32 v215, v218, v219\n\t"
    "v_mfma_f32_32x32x16_bf16 v[128:143], a[140:143], a[44:47], v[128:143]\n\t"
    "s_add_u32 m0, s46, 12288\n\t"
    "s_nop 0\n\t"
    "buffer_load_dwordx4 v235, s[16:19], s37 offen lds\n\t"
    "v_add_f32 v229, v229, v218\n\t"
    "v_add_f32 v229, v229, v219\n\t"
    "v_mfma_f32_32x32x16_bf16 v[144:159], a[140:143], a[92:95], v[144:159]\n\t"
    "ds_read_b64_tr_b16 a[140:141], v234 offset:33280\n\t"
    "ds_read_b64_tr_b16 a[142:143], v234 offset:36352\n\t"
    "v_add_f32 v251, v228, v229\n\t"
    "v_cmp_nge_f32 vcc, s64, v251\n\t"
    "s_cbranch_vccnz .Ld_stub27\n\t"
    ".Ld_back27:\n\t"
    "s_waitcnt lgkmcnt(12)\n\t"
    "v_mfma_f32_32x32x16_bf16 v[0:15], a[96:99], v[200:203], v[0:15]\n\t"
    "buffer_load_dwordx4 v236, s[16:19], s37 offen offset:1024 lds\n\t"
    "v_add_f32 v226, v226, v228\n\t"
    "v_add_f32 v227, v227, v229\n\t"
    "v_mfma_f32_32x32x16_bf16 v[96:111], a[96:99], v[204:207], v[96:111]\n\t"
    "ds_read_b128 a[96:99], v230 offset:24576\n\t"
    "v_mfma_f32_32x32x16_bf16 v[16:31], a[100:103], v[200:203], v[16:31]\n\t"
    "buffer_load_dwordx4 v237, s[16:19], s37 offen offset:2048 lds\n\t"
    "v_mfma_f32_32x32x16_bf16 v[112:127], a[100:103], v[204:207], v[112:127]\n\t"
    "ds_read_b128 a[100:103], v231 offset:24576\n\t"
    "v_fma_f32 v216, v128, s63, v224\n\t"
    "v_fma_f32 v217, v129, s63, v224\n\t"
    "v_exp_f32 v216, v216\n\t"
    "v_mfma_f32_32x32x16_bf16 v[32:47], a[104:107], v[200:203], v[32:47]\n\t"
    "v_exp_f32 v217, v217\n\t"
    "v_fma_f32 v218, v144, s63, v225\n\t"
    "v_fma_f32 v219, v145, s63, v225\n\t"
    "v_mfma_f32_32x32x16_bf16 a[144:159], a[104:107], v[204:207], a[144:159]\n\t"
    "ds_read_b128 a[104:107], v232 offset:24576\n\t"
    "v_exp_f32 v218, v218\n\t"
    "v_exp_f32 v219, v219\n\t"
    "v_mfma_f32_32x32x16_bf16 v[48:63], a[108:111], v[200:203], v[48:63]\n\t"
    "v_cvt_pk_bf16_f32 v192, v216, v217\n\t"
    "v_add_f32 v228, v216, v217\n\t"
    "v_fma_f32 v216, v130, s63, v224\n\t"
    "v_fma_f32 v217, v131, s63, v224\n\t"
    "v_mfma_f32_32x32x16_bf16 a[160:175], a[108:111], v[204:207], a[160:175]\n\t"
    "ds_read_b128 a[108:111], v233 offset:24576\n\t"
    "s_add_u32 m0, s61, 8960\n\t"
    "s_nop 0\n\t"
    "buffer_load_dwordx4 v249, s[24:27], 0 offen offset:256 nt lds\n\t"
    "v_mfma_f32_32x32x16_bf16 v[64:79], a[112:115], v[200:203], v[64:79]\n\t"
    "v_exp_f32 v216, v216\n\t"
    "v_exp_f32 v217, v217\n\t"
    "v_mfma_f32_32x32x16_bf16 a[176:191], a[112:115], v[204:207], a[176:191]\n\t"
    "ds_read_b128 a[112:115], v230 offset:25600\n\t"
    "v_cvt_pk_bf16_f32 v196, v218, v219\n\t"
    "v_add_f32 v229, v218, v219\n\t"
    "v_fma_f32 v218, v146, s63, v225\n\t"
    "v_fma_f32 v219, v147, s63, v225\n\t"
    "v_mfma_f32_32x32x16_bf16 v[80:95], a[116:119], v[200:203], v[80:95]\n\t"
    "v_exp_f32 v218, v218\n\t"
    "v_exp_f32 v219, v219\n\t"
    "v_mfma_f32_32x32x16_bf16 a[192:207], a[116:119], v[204:207], a[192:207]\n\t"
    "ds_read_b128 a[116:119], v231 offset:25600\n\t"
    "v_cvt_pk_bf16_f32 v193, v216, v217\n\t"
    "v_add_f32 v228, v228, v216\n\t"
    "v_add_f32 v228, v228, v217\n\t"
    "v_fma_f32 v216, v132, s63, v224\n\t"
    "s_waitcnt lgkmcnt(10)\n\t"
    "v_mfma_f32_32x32x16_bf16 v[0:15], a[120:123], v[208:211], v[0:15]\n\t"
    "v_fma_f32 v217, v133, s63, v224\n\t"
    "v_exp_f32 v216, v216\n\t"
    "v_mfma_f32_32x32x16_bf16 v[96:111], a[120:123], v[212:215], v[96:111]\n\t"
    "ds_read_b128 a[120:123], v232 offset:25600\n\t"
    "s_add_u32 m0, s61, 9984\n\t"
    "s_nop 0\n\t"
    "buffer_load_dwordx4 v248, s[24:27], s86 offen offset:256 nt lds\n\t"
    "v_exp_f32 v217, v217\n\t"
    "v_mfma_f32_32x32x16_bf16 v[16:31], a[124:127], v[208:211], v[16:31]\n\t"
    "v_cvt_pk_bf16_f32 v197, v218, v219\n\t"
    "v_add_f32 v229, v229, v218\n\t"
    "v_add_f32 v229, v229, v219\n\t"
    "v_fma_f32 v218, v148, s63, v225\n\t"
    "v_mfma_f32_32x32x16_bf16 v[112:127], a[124:127], v[212:215], v[112:127]\n\t"
    "ds_read_b128 a[124:127], v233 offset:25600\n\t"
    "v_fma_f32 v219, v149, s63, v225\n\t"
    "v_exp_f32 v218, v218\n\t"
    "v_mfma_f32_32x32x16_bf16 v[32:47], a[128:131], v[208:211], v[32:47]\n\t"
    "v_exp_f32 v219, v219\n\t"
    "v_cvt_pk_bf16_f32 v194, v216, v217\n\t"
    "v_add_f32 v228, v228, v216\n\t"
    "v_add_f32 v228, v228, v217\n\t"
    "v_mfma_f32_32x32x16_bf16 a[144:159], a[128:131], v[212:215], a[144:159]\n\t"
    "ds_read_b128 a[128:131], v230 offset:26624\n\t"
    "v_fma_f32 v216, v134, s63, v224\n\t"
    "v_fma_f32 v217, v135, s63, v224\n\t"
    "v_mfma_f32_32x32x16_bf16 v[48:63], a[132:135], v[208:211], v[48:63]\n\t"
    "v_exp_f32 v216, v216\n\t"
    "v_exp_f32 v217, v217\n\t"
    "v_cvt_pk_bf16_f32 v198, v218, v219\n\t"
    "v_mfma_f32_32x32x16_bf16 a[160:175], a[132:135], v[212:215], a[160:175]\n\t"
    "ds_read_b128 a[132:135], v231 offset:26624\n\t"
    "s_add_u32 m0, s61, 11008\n\t"
    "s_nop 0\n\t"
    "buffer_load_dwordx4 v249, s[24:27], s86 offen offset:256 nt lds\n\t"
    "v_add_f32 v229, v229, v218\n\t"
    "s_waitcnt lgkmcnt(10)\n\t"
    "v_mfma_f32_32x32x16_bf16 v[64:79], a[136:139], v[208:211], v[64:79]\n\t"
    "v_add_f32 v229, v229, v219\n\t"
    "v_fma_f32 v218, v150, s63, v225\n\t"
    "v_fma_f32 v219, v151, s63, v225\n\t"
    "v_exp_f32 v218, v218\n\t"
    "v_mfma_f32_32x32x16_bf16 a[176:191], a[136:139], v[212:215], a[176:191]\n\t"
    "ds_read_b128 a[136:139], v232 offset:26624\n\t"
    "v_exp_f32 v219, v219\n\t"
    "v_cvt_pk_bf16_f32 v195, v216, v217\n\t"
    "v_mfma_f32_32x32x16_bf16 v[80:95], a[140:143], v[208:211], v[80:95]\n\t"
    "v_add_f32 v228, v228, v216\n\t"
    "v_add_f32 v228, v228, v217\n\t"
    "s_nop 0\n\t"
    "v_cvt_pk_bf16_f32 v199, v218, v219\n\t"
    "v_mfma_f32_32x32x16_bf16 a[192:207], a[140:143], v[212:215], a[192:207]\n\t"
    "ds_read_b128 a[140:143], v233 offset:26624\n\t"
    "v_add_f32 v229, v229, v218\n\t"
    "v_add_f32 v229, v229, v219\n\t"
    "s_waitcnt lgkmcnt(10)\n\t"
    "v_mfma_f32_32x32x16_bf16 v[160:175], a[96:99], a[0:3], 0\n\t"
    "v_fma_f32 v216, v136, s63, v224\n\t"
    "v_fma_f32 v217, v137, s63, v224\n\t"
    "v_exp_f32 v216, v216\n\t"
    "v_exp_f32 v217, v217\n\t"
    "v_mfma_f32_32x32x16_bf16 v[176:191], a[96:99], a[48:51], 0\n\t"
    "v_fma_f32 v218, v152, s63, v225\n\t"
    "v_fma_f32 v219, v153, s63, v225\n\t"
    "v_exp_f32 v218, v218\n\t"
    "s_waitcnt lgkmcnt(9)\n\t"
    "v_mfma_f32_32x32x16_bf16 v[160:175], a[100:103], a[4:7], v[160:175]\n\t"
    "v_exp_f32 v219, v219\n\t"
    "v_cvt_pk_bf16_f32 v208, v216, v217\n\t"
    "v_add_f32 v228, v228, v216\n\t"
    "v_add_f32 v228, v228, v217\n\t"
    "v_mfma_f32_32x32x16_bf16 v[176:191], a[100:103], a[52:55], v[176:191]\n\t"
    "v_fma_f32 v216, v138, s63, v224\n\t"
    "s_waitcnt vmcnt(9) lgkmcnt(0)\n\t"
    "s_barrier\n\t"
    "v_mfma_f32_32x32x16_bf16 v[160:175], a[104:107], a[8:11], v[160:175]\n\t"
    "ds_read_b64_tr_b16 a[96:97], v234 offset:0\n\t"
    "ds_read_b64_tr_b16 a[98:99], v234 offset:3072\n\t"
    "s_add_u32 m0, s49, 24576\n\t"
    "s_nop 0\n\t"
    "buffer_load_dwordx4 v238, s[20:23], s34 offen lds\n\t"
    "v_mfma_f32_32x32x16_bf16 v[176:191], a[104:107], a[56:59], v[176:191]\n\t"
    "ds_read_b64_tr_b16 a[100:101], v234 offset:512\n\t"
    "ds_read_b64_tr_b16 a[102:103], v234 offset:3584\n\t"
    "v_fma_f32 v217, v139, s63, v224\n\t"
    "v_exp_f32 v216, v216\n\t"
    "v_mfma_f32_32x32x16_bf16 v[160:175], a[108:111], a[12:15], v[160:175]\n\t"
    "ds_read_b64_tr_b16 a[104:105], v234 offset:1024\n\t"
    "ds_read_b64_tr_b16 a[106:107], v234 offset:4096\n\t"
    "buffer_load_dwordx4 v239, s[20:23], s34 offen offset:1024 lds\n\t"
    "v_exp_f32 v217, v217\n\t"
    "v_cvt_pk_bf16_f32 v212, v218, v219\n\t"
    "v_mfma_f32_32x32x16_bf16 v[176:191], a[108:111], a[60:63], v[176:191]\n\t"
    "ds_read_b64_tr_b16 a[108:109], v234 offset:1536\n\t"
    "ds_read_b64_tr_b16 a[110:111], v234 offset:4608\n\t"
    "v_add_f32 v229, v229, v218\n\t"
    "v_add_f32 v229, v229, v219\n\t"
    "v_fma_f32 v218, v154, s63, v225\n\t"
    "v_mfma_f32_32x32x16_bf16 v[160:175], a[112:115], a[16:19], v[160:175]\n\t"
    "buffer_load_dwordx4 v240, s[20:23], s34 offen offset:2048 lds\n\t"
    "v_fma_f32 v219, v155, s63, v225\n\t"
    "v_exp_f32 v218, v218\n\t"
    "v_mfma_f32_32x32x16_bf16 v[176:191], a[112:115], a[64:67], v[176:191]\n\t"
    "ds_read_b64_tr_b16 a[112:113], v234 offset:2048\n\t"
    "ds_read_b64_tr_b16 a[114:115], v234 offset:5120\n\t"
    "v_exp_f32 v219, v219\n\t"
    "v_cvt_pk_bf16_f32 v209, v216, v217\n\t"
    "v_add_f32 v228, v228, v216\n\t"
    "v_mfma_f32_32x32x16_bf16 v[160:175], a[116:119], a[20:23], v[160:175]\n\t"
    "s_add_u32 m0, s49, 36864\n\t"
    "s_nop 0\n\t"
    "buffer_load_dwordx4 v238, s[20:23], s35 offen lds\n\t"
    "v_add_f32 v228, v228, v217\n\t"
    "v_fma_f32 v216, v140, s63, v224\n\t"
    "v_mfma_f32_32x32x16_bf16 v[176:191], a[116:119], a[68:71], v[176:191]\n\t"
    "ds_read_b64_tr_b16 a[116:117], v234 offset:2560\n\t"
    "ds_read_b64_tr_b16 a[118:119], v234 offset:5632\n\t"
    "v_fma_f32 v217, v141, s63, v224\n\t"
    "v_exp_f32 v216, v216\n\t"
    "v_mfma_f32_32x32x16_bf16 v[160:175], a[120:123], a[24:27], v[160:175]\n\t"
    "buffer_load_dwordx4 v239, s[20:23], s35 offen offset:1024 lds\n\t"
    "v_exp_f32 v217, v217\n\t"
    "v_cvt_pk_bf16_f32 v213, v218, v219\n\t"
    "v_add_f32 v229, v229, v218\n\t"
    "v_mfma_f32_32x32x16_bf16 v[176:191], a[120:123], a[72:75], v[176:191]\n\t"
    "ds_read_b64_tr_b16 a[120:121], v234 offset:6144\n\t"
    "ds_read_b64_tr_b16 a[122:123], v234 offset:9216\n\t"
    "v_add_f32 v229, v229, v219\n\t"
    "v_fma_f32 v218, v156, s63, v225\n\t"
    "v_fma_f32 v219, v157, s63, v225\n\t"
    "v_mfma_f32_32x32x16_bf16 v[160:175], a[124:127], a[28:31], v[160:175]\n\t"
    "buffer_load_dwordx4 v240, s[20:23], s35 offen offset:2048 lds\n\t"
    "v_exp_f32 v218, v218\n\t"
    "v_exp_f32 v219, v219\n\t"
    "v_mfma_f32_32x32x16_bf16 v[176:191], a[124:127], a[76:79], v[176:191]\n\t"
    "ds_read_b64_tr_b16 a[124:125], v234 offset:6656\n\t"
    "ds_read_b64_tr_b16 a[126:127], v234 offset:9728\n\t"
    "v_cvt_pk_bf16_f32 v210, v216, v217\n\t"
    "v_add_f32 v228, v228, v216\n\t"
    "v_add_f32 v228, v228, v217\n\t"
    "v_fma_f32 v216, v142, s63, v224\n\t"
    "v_mfma_f32_32x32x16_bf16 v[160:175], a[128:131], a[32:35], v[160:175]\n\t"
    "s_add_u32 m0, s46, 24576\n\t"
    "s_nop 0\n\t"
    "buffer_load_dwordx4 v235, s[16:19], s38 offen lds\n\t"
    "v_fma_f32 v217, v143, s63, v224\n\t"
    "v_mfma_f32_32x32x16_bf16 v[176:191], a[128:131], a[80:83], v[176:191]\n\t"
    "ds_read_b64_tr_b16 a[128:129], v234 offset:7168\n\t"
    "ds_read_b64_tr_b16 a[130:131], v234 offset:10240\n\t"
    "v_exp_f32 v216, v216\n\t"
    "v_exp_f32 v217, v217\n\t"
    "v_mfma_f32_32x32x16_bf16 v[160:175], a[132:135], a[36:39], v[160:175]\n\t"
    "buffer_load_dwordx4 v236, s[16:19], s38 offen offset:1024 lds\n\t"
    "v_cvt_pk_bf16_f32 v214, v218, v219\n\t"
    "v_add_f32 v229, v229, v218\n\t"
    "v_add_f32 v229, v229, v219\n\t"
    "v_fma_f32 v218, v158, s63, v225\n\t"
    "v_mfma_f32_32x32x16_bf16 v[176:191], a[132:135], a[84:87], v[176:191]\n\t"
    "ds_read_b64_tr_b16 a[132:133], v234 offset:7680\n\t"
    "ds_read_b64_tr_b16 a[134:135], v234 offset:10752\n\t"
    "v_fma_f32 v219, v159, s63, v225\n\t"
    "v_exp_f32 v218, v218\n\t"
    "v_mfma_f32_32x32x16_bf16 v[160:175], a[136:139], a[40:43], v[160:175]\n\t"
    "buffer_load_dwordx4 v237, s[16:19], s38 offen offset:2048 lds\n\t"
    "v_exp_f32 v219, v219\n\t"
    "v_cvt_pk_bf16_f32 v211, v216, v217\n\t"
    "v_add_f32 v228, v228, v216\n\t"
    "v_mfma_f32_32x32x16_bf16 v[176:191], a[136:139], a[88:91], v[176:191]\n\t"
    "ds_read_b64_tr_b16 a[136:137], v234 offset:8192\n\t"
    "ds_read_b64_tr_b16 a[138:139], v234 offset:11264\n\t"
    "v_add_f32 v228, v228, v217\n\t"
    "s_nop 0\n\t"
    "v_cvt_pk_bf16_f32 v215, v218, v219\n\t"
    "v_mfma_f32_32x32x16_bf16 v[160:175], a[140:143], a[44:47], v[160:175]\n\t"
    "s_add_u32 m0, s46, 36864\n\t"
    "s_nop 0\n\t"
    "buffer_load_dwordx4 v235, s[16:19], s39 offen lds\n\t"
    "v_add_f32 v229, v229, v218\n\t"
    "v_add_f32 v229, v229, v219\n\t"
    "v_mfma_f32_32x32x16_bf16 v[176:191], a[140:143], a[92:95], v[176:191]\n\t"
    "ds_read_b64_tr_b16 a[140:141], v234 offset:8704\n\t"
    "ds_read_b64_tr_b16 a[142:143], v234 offset:11776\n\t"
    "v_add_f32 v251, v228, v229\n\t"
    "v_cmp_nge_f32 vcc, s64, v251\n\t"
    "s_cbranch_vccnz .Ld_stub28\n\t"
    ".Ld_back28:\n\t"
    "s_waitcnt lgkmcnt(12)\n\t"
    "v_mfma_f32_32x32x16_bf16 v[0:15], a[96:99], v[192:195], v[0:15]\n\t"
    "buffer_load_dwordx4 v236, s[16:19], s39 offen offset:1024 lds\n\t"
    "v_add_f32 v226, v226, v228\n\t"
    "v_add_f32 v227, v227, v229\n\t"
    "v_mfma_f32_32x32x16_bf16 v[96:111], a[96:99], v[196:199], v[96:111]\n\t"
    "v_mfma_f32_32x32x16_bf16 v[16:31], a[100:103], v[192:195], v[16:31]\n\t"
    "buffer_load_dwordx4 v237, s[16:19], s39 offen offset:2048 lds\n\t"
    "v_mfma_f32_32x32x16_bf16 v[112:127], a[100:103], v[196:199], v[112:127]\n\t"
    "v_fma_f32 v216, v160, s63, v224\n\t"
    "v_fma_f32 v217, v161, s63, v224\n\t"
    "v_exp_f32 v216, v216\n\t"
    "s_waitcnt vmcnt(6) lgkmcnt(0)\n\t"
    "s_barrier\n\t"
    "v_mfma_f32_32x32x16_bf16 v[32:47], a[104:107], v[192:195], v[32:47]\n\t"
    "ds_read_b64_tr_b16 a[96:97], v234 offset:24576\n\t"
    "ds_read_b64_tr_b16 a[98:99], v234 offset:27648\n\t"
    "v_exp_f32 v217, v217\n\t"
    "v_fma_f32 v218, v176, s63, v225\n\t"
    "v_fma_f32 v219, v177, s63, v225\n\t"
    "v_exp_f32 v218, v218\n\t"
    "v_exp_f32 v219, v219\n\t"
    "v_mfma_f32_32x32x16_bf16 a[144:159], a[104:107], v[196:199], a[144:159]\n\t"
    "ds_read_b64_tr_b16 a[100:101], v234 offset:25088\n\t"
    "ds_read_b64_tr_b16 a[102:103], v234 offset:28160\n\t"
    "v_cvt_pk_bf16_f32 v200, v216, v217\n\t"
    "v_add_f32 v228, v216, v217\n\t"
    "v_fma_f32 v216, v162, s63, v224\n\t"
    "v_fma_f32 v217, v163, s63, v224\n\t"
    "v_exp_f32 v216, v216\n\t"
    "v_mfma_f32_32x32x16_bf16 v[48:63], a[108:111], v[192:195], v[48:63]\n\t"
    "ds_read_b64_tr_b16 a[104:105], v234 offset:25600\n\t"
    "ds_read_b64_tr_b16 a[106:107], v234 offset:28672\n\t"
    "s_mov_b32 m0, s49\n\t"
    "s_mov_b32 s22, s42\n\t"
    "buffer_load_dwordx4 v238, s[20:23], s36 offen lds\n\t"
    "v_exp_f32 v217, v217\n\t"
    "v_cvt_pk_bf16_f32 v204, v218, v219\n\t"
    "v_add_f32 v229, v218, v219\n\t"
    "v_mfma_f32_32x32x16_bf16 a[160:175], a[108:111], v[196:199], a[160:175]\n\t"
    "ds_read_b64_tr_b16 a[108:109], v234 offset:26112\n\t"
    "ds_read_b64_tr_b16 a[110:111], v234 offset:29184\n\t"
    "v_fma_f32 v218, v178, s63, v225\n\t"
    "v_fma_f32 v219, v179, s63, v225\n\t"
    "v_exp_f32 v218, v218\n\t"
    "v_exp_f32 v219, v219\n\t"
    "v_cvt_pk_bf16_f32 v201, v216, v217\n\t"
    "v_mfma_f32_32x32x16_bf16 v[64:79], a[112:115], v[192:195], v[64:79]\n\t"
    "buffer_load_dwordx4 v239, s[20:23], s36 offen offset:1024 lds\n\t"
    "v_add_f32 v228, v228, v216\n\t"
    "v_add_f32 v228, v228, v217\n\t"
    "v_fma_f32 v216, v164, s63, v224\n\t"
    "v_fma_f32 v217, v165, s63, v224\n\t"
    "v_exp_f32 v216, v216\n\t"
    "v_exp_f32 v217, v217\n\t"
    "v_mfma_f32_32x32x16_bf16 a[176:191], a[112:115], v[196:199], a[176:191]\n\t"
    "ds_read_b64_tr_b16 a[112:113], v234 offset:26624\n\t"
    "ds_read_b64_tr_b16 a[114:115], v234 offset:29696\n\t"
    "v_cvt_pk_bf16_f32 v205, v218, v219\n\t"
    "v_add_f32 v229, v229, v218\n\t"
    "v_add_f32 v229, v229, v219\n\t"
    "v_fma_f32 v218, v180, s63, v225\n\t"
    "v_fma_f32 v219, v181, s63, v225\n\t"
    "v_exp_f32 v218, v218\n\t"
    "v_mfma_f32_32x32x16_bf16 v[80:95], a[116:119], v[192:195], v[80:95]\n\t"
    "buffer_load_dwordx4 v240, s[20:23], s36 offen offset:2048 lds\n\t"
    "v_exp_f32 v219, v219\n\t"
    "v_cvt_pk_bf16_f32 v202, v216, v217\n\t"
    "v_add_f32 v228, v228, v216\n\t"
    "v_add_f32 v228, v228, v217\n\t"
    "v_fma_f32 v216, v166, s63, v224\n\t"
    "v_fma_f32 v217, v167, s63, v224\n\t"
    "v_mfma_f32_32x32x16_bf16 a[192:207], a[116:119], v[196:199], a[192:207]\n\t"
    "ds_read_b64_tr_b16 a[116:117], v234 offset:27136\n\t"
    "ds_read_b64_tr_b16 a[118:119], v234 offset:30208\n\t"
    "v_exp_f32 v216, v216\n\t"
    "v_exp_f32 v217, v217\n\t"
    "v_cvt_pk_bf16_f32 v206, v218, v219\n\t"
    "v_add_f32 v229, v229, v218\n\t"
    "v_add_f32 v229, v229, v219\n\t"
    "v_mfma_f32_32x32x16_bf16 v[0:15], a[120:123], v[208:211], v[0:15]\n\t"
    "s_add_u32 m0, s49, 12288\n\t"
    "s_nop 0\n\t"
    "buffer_load_dwordx4 v238, s[20:23], s37 offen lds\n\t"
    "v_fma_f32 v218, v182, s63, v225\n\t"
    "v_fma_f32 v219, v183, s63, v225\n\t"
    "v_exp_f32 v218, v218\n\t"
    "v_exp_f32 v219, v219\n\t"
    "v_mfma_f32_32x32x16_bf16 v[96:111], a[120:123], v[212:215], v[96:111]\n\t"
    "ds_read_b64_tr_b16 a[120:121], v234 offset:30720\n\t"
    "ds_read_b64_tr_b16 a[122:123], v234 offset:33792\n\t"
    "v_cvt_pk_bf16_f32 v203, v216, v217\n\t"
    "v_add_f32 v228, v228, v216\n\t"
    "v_add_f32 v228, v228, v217\n\t"
    "s_nop 0\n\t"
    "v_cvt_pk_bf16_f32 v207, v218, v219\n\t"
    "v_add_f32 v229, v229, v218\n\t"
    "v_add_f32 v229, v229, v219\n\t"
    "v_mfma_f32_32x32x16_bf16 v[16:31], a[124:127], v[208:211], v[16:31]\n\t"
    "buffer_load_dwordx4 v239, s[20:23], s37 offen offset:1024 lds\n\t"
    "v_fma_f32 v216, v168, s63, v224\n\t"
    "v_fma_f32 v217, v169, s63, v224\n\t"
    "v_exp_f32 v216, v216\n\t"
    "v_exp_f32 v217, v217\n\t"
    "v_fma_f32 v218, v184, s63, v225\n\t"
    "v_fma_f32 v219, v185, s63, v225\n\t"
    "v_mfma_f32_32x32x16_bf16 v[112:127], a[124:127], v[212:215], v[112:127]\n\t"
    "ds_read_b64_tr_b16 a[124:125], v234 offset:31232\n\t"
    "ds_read_b64_tr_b16 a[126:127], v234 offset:34304\n\t"
    "v_exp_f32 v218, v218\n\t"
    "v_exp_f32 v219, v219\n\t"
    "v_cvt_pk_bf16_f32 v192, v216, v217\n\t"
    "v_add_f32 v228, v228, v216\n\t"
    "v_mfma_f32_32x32x16_bf16 v[32:47], a[128:131], v[208:211], v[32:47]\n\t"
    "buffer_load_dwordx4 v240, s[20:23], s37 offen offset:2048 lds\n\t"
    "v_add_f32 v228, v228, v217\n\t"
    "v_fma_f32 v216, v170, s63, v224\n\t"
    "v_fma_f32 v217, v171, s63, v224\n\t"
    "v_exp_f32 v216, v216\n\t"
    "v_exp_f32 v217, v217\n\t"
    "v_cvt_pk_bf16_f32 v196, v218, v219\n\t"
    "v_mfma_f32_32x32x16_bf16 a[144:159], a[128:131], v[212:215], a[144:159]\n\t"
    "ds_read_b64_tr_b16 a[128:129], v234 offset:31744\n\t"
    "ds_read_b64_tr_b16 a[130:131], v234 offset:34816\n\t"
    "v_add_f32 v229, v229, v218\n\t"
    "v_add_f32 v229, v229, v219\n\t"
    "v_fma_f32 v218, v186, s63, v225\n\t"
    "v_fma_f32 v219, v187, s63, v225\n\t"
    "v_exp_f32 v218, v218\n\t"
    "v_mfma_f32_32x32x16_bf16 v[48:63], a[132:135], v[208:211], v[48:63]\n\t"
    "v_exp_f32 v219, v219\n\t"
    "v_cvt_pk_bf16_f32 v193, v216, v217\n\t"
    "v_add_f32 v228, v228, v216\n\t"
    "v_add_f32 v228, v228, v217\n\t"
    "v_fma_f32 v216, v172, s63, v224\n\t"
    "v_fma_f32 v217, v173, s63, v224\n\t"
    "v_exp_f32 v216, v216\n\t"
    "v_mfma_f32_32x32x16_bf16 a[160:175], a[132:135], v[212:215], a[160:175]\n\t"
    "ds_read_b64_tr_b16 a[132:133], v234 offset:32256\n\t"
    "ds_read_b64_tr_b16 a[134:135], v234 offset:35328\n\t"
    "v_exp_f32 v217, v217\n\t"
    "v_cvt_pk_bf16_f32 v197, v218, v219\n\t"
    "v_add_f32 v229, v229, v218\n\t"
    "v_add_f32 v229, v229, v219\n\t"
    "v_fma_f32 v218, v188, s63, v225\n\t"
    "v_fma_f32 v219, v189, s63, v225\n\t"
    "v_mfma_f32_32x32x16_bf16 v[64:79], a[136:139], v[208:211], v[64:79]\n\t"
    "v_exp_f32 v218, v218\n\t"
    "v_exp_f32 v219, v219\n\t"
    "v_cvt_pk_bf16_f32 v194, v216, v217\n\t"
    "v_add_f32 v228, v228, v216\n\t"
    "v_add_f32 v228, v228, v217\n\t"
    "v_fma_f32 v216, v174, s63, v224\n\t"
    "v_fma_f32 v217, v175, s63, v224\n\t"
    "v_mfma_f32_32x32x16_bf16 a[176:191], a[136:139], v[212:215], a[176:191]\n\t"
    "ds_read_b64_tr_b16 a[136:137], v234 offset:32768\n\t"
    "ds_read_b64_tr_b16 a[138:139], v234 offset:35840\n\t"
    "v_exp_f32 v216, v216\n\t"
    "v_exp_f32 v217, v217\n\t"
    "v_cvt_pk_bf16_f32 v198, v218, v219\n\t"
    "v_add_f32 v229, v229, v218\n\t"
    "v_add_f32 v229, v229, v219\n\t"
    "v_mfma_f32_32x32x16_bf16 v[80:95], a[140:143], v[208:211], v[80:95]\n\t"
    "v_fma_f32 v218, v190, s63, v225\n\t"
    "v_fma_f32 v219, v191, s63, v225\n\t"
    "v_exp_f32 v218, v218\n\t"
    "v_exp_f32 v219, v219\n\t"
    "v_cvt_pk_bf16_f32 v195, v216, v217\n\t"
    "v_add_f32 v228, v228, v216\n\t"
    "v_add_f32 v228, v228, v217\n\t"
    "v_mfma_f32_32x32x16_bf16 a[192:207], a[140:143], v[212:215], a[192:207]\n\t"
    "ds_read_b64_tr_b16 a[140:141], v234 offset:33280\n\t"
    "ds_read_b64_tr_b16 a[142:143], v234 offset:36352\n\t"
    "s_nop 0\n\t"
    "v_cvt_pk_bf16_f32 v199, v218, v219\n\t"
    "v_add_f32 v229, v229, v218\n\t"
    "v_add_f32 v229, v229, v219\n\t"
    "v_add_f32 v251, v228, v229\n\t"
    "v_cmp_nge_f32 vcc, s64, v251\n\t"
    "s_cbranch_vccnz .Ld_stub29\n\t"
    ".Ld_back29:\n\t"
    "s_waitcnt lgkmcnt(14)\n\t"
    "v_mfma_f32_32x32x16_bf16 v[0:15], a[96:99], v[200:203], v[0:15]\n\t"
    "s_waitcnt vmcnt(18)\n\t"
    "ds_read_b128 a[0:3], v244 offset:0\n\t"
    "v_add_f32 v226, v226, v228\n\t"
    "v_add_f32 v227, v227, v229\n\t"
    "v_add_u32 v230, s54, v230\n\t"
    "v_add_u32 v231, s54, v231\n\t"
    "v_add_u32 v232, s54, v232\n\t"
    "v_mfma_f32_32x32x16_bf16 v[16:31], a[100:103], v[200:203], v[16:31]\n\t"
    "ds_read_b128 a[4:7], v245 offset:0\n\t"
    "v_add_u32 v233, s54, v233\n\t"
    "v_add_u32 v234, s54, v234\n\t"
    "v_mov_b32 v250, v226\n\t"
    "s_nop 1\n\t"
    "v_permlane32_swap_b32 v226, v250\n\t"
    "v_mfma_f32_32x32x16_bf16 v[32:47], a[104:107], v[200:203], v[32:47]\n\t"
    "ds_read_b128 a[8:11], v246 offset:0\n\t"
    "v_add_f32 v226, v226, v250\n\t"
    "v_rcp_f32 v242, v226\n\t"
    "v_mov_b32 v250, v227\n\t"
    "s_nop 1\n\t"
    "v_permlane32_swap_b32 v227, v250\n\t"
    "v_mfma_f32_32x32x16_bf16 v[48:63], a[108:111], v[200:203], v[48:63]\n\t"
    "ds_read_b128 a[12:15], v247 offset:0\n\t"
    "v_add_f32 v227, v227, v250\n\t"
    "v_rcp_f32 v243, v227\n\t"
    "v_accvgpr_mov_b32 a48, a208\n\t"
    "v_accvgpr_mov_b32 a49, a209\n\t"
    "v_accvgpr_mov_b32 a50, a210\n\t"
    "s_waitcnt lgkmcnt(14)\n\t"
    "v_mfma_f32_32x32x16_bf16 v[64:79], a[112:115], v[200:203], v[64:79]\n\t"
    "ds_read_b128 a[16:19], v244 offset:4096\n\t"
    "v_accvgpr_mov_b32 a51, a211\n\t"
    "v_accvgpr_mov_b32 a52, a212\n\t"
    "v_accvgpr_mov_b32 a53, a213\n\t"
    "v_accvgpr_mov_b32 a54, a214\n\t"
    "v_accvgpr_mov_b32 a55, a215\n\t"
    "v_accvgpr_mov_b32 a56, a216\n\t"
    "v_mfma_f32_32x32x16_bf16 v[80:95], a[116:119], v[200:203], v[80:95]\n\t"
    "ds_read_b128 a[20:23], v245 offset:4096\n\t"
    "v_accvgpr_mov_b32 a57, a217\n\t"
    "s_waitcnt vmcnt(24) lgkmcnt(0)\n\t"
    "s_barrier\n\t"
    "v_mfma_f32_32x32x16_bf16 v[0:15], a[120:123], v[192:195], v[0:15]\n\t"
    "s_add_u32 m0, s49, 24576\n\t"
    "s_nop 0\n\t"
    "buffer_load_dwordx4 v238, s[20:23], s38 offen lds\n\t"
    "ds_read_b128 a[24:27], v246 offset:4096\n\t"
    "v_accvgpr_mov_b32 a58, a218\n\t"
    "v_accvgpr_mov_b32 a59, a219\n\t"
    "v_accvgpr_mov_b32 a60, a220\n\t"
    "v_mfma_f32_32x32x16_bf16 v[16:31], a[124:127], v[192:195], v[16:31]\n\t"
    "ds_read_b128 a[28:31], v247 offset:4096\n\t"
    "v_accvgpr_mov_b32 a61, a221\n\t"
    "v_accvgpr_mov_b32 a62, a222\n\t"
    "v_accvgpr_mov_b32 a63, a223\n\t"
    "v_accvgpr_mov_b32 a64, a224\n\t"
    "v_accvgpr_mov_b32 a65, a225\n\t"
    "v_accvgpr_mov_b32 a66, a226\n\t"
    "v_mfma_f32_32x32x16_bf16 v[32:47], a[128:131], v[192:195], v[32:47]\n\t"
    "buffer_load_dwordx4 v239, s[20:23], s38 offen offset:1024 lds\n\t"
    "ds_read_b128 a[32:35], v244 offset:8192\n\t"
    "v_accvgpr_mov_b32 a67, a227\n\t"
    "v_accvgpr_mov_b32 a68, a228\n\t"
    "v_accvgpr_mov_b32 a69, a229\n\t"
    "v_accvgpr_mov_b32 a70, a230\n\t"
    "v_accvgpr_mov_b32 a71, a231\n\t"
    "v_mfma_f32_32x32x16_bf16 v[48:63], a[132:135], v[192:195], v[48:63]\n\t"
    "ds_read_b128 a[36:39], v245 offset:8192\n\t"
    "v_accvgpr_mov_b32 a72, a232\n\t"
    "v_accvgpr_mov_b32 a73, a233\n\t"
    "v_accvgpr_mov_b32 a74, a234\n\t"
    "v_accvgpr_mov_b32 a75, a235\n\t"
    "v_accvgpr_mov_b32 a76, a236\n\t"
    "v_mfma_f32_32x32x16_bf16 v[64:79], a[136:139], v[192:195], v[64:79]\n\t"
    "buffer_load_dwordx4 v240, s[20:23], s38 offen offset:2048 lds\n\t"
    "ds_read_b128 a[40:43], v246 offset:8192\n\t"
    "v_accvgpr_mov_b32 a77, a237\n\t"
    "v_accvgpr_mov_b32 a78, a238\n\t"
    "v_accvgpr_mov_b32 a79, a239\n\t"
    "v_accvgpr_mov_b32 a80, a240\n\t"
    "v_accvgpr_mov_b32 a81, a241\n\t"
    "v_mfma_f32_32x32x16_bf16 v[80:95], a[140:143], v[192:195], v[80:95]\n\t"
    "ds_read_b128 a[44:47], v247 offset:8192\n\t"
    "v_accvgpr_mov_b32 a82, a242\n\t"
    "v_accvgpr_mov_b32 a83, a243\n\t"
    "v_accvgpr_mov_b32 a84, a244\n\t"
    "v_accvgpr_mov_b32 a85, a245\n\t"
    "v_accvgpr_mov_b32 a86, a246\n\t"
    "v_accvgpr_mov_b32 a87, a247\n\t"
    "v_mfma_f32_32x32x16_bf16 v[96:111], a[96:99], v[204:207], v[96:111]\n\t"
    "ds_read_b128 a[96:99], v230 offset:0\n\t"
    "s_add_u32 m0, s49, 36864\n\t"
    "s_nop 0\n\t"
    "buffer_load_dwordx4 v238, s[20:23], s39 offen lds\n\t"
    "v_accvgpr_mov_b32 a88, a248\n\t"
    "v_accvgpr_mov_b32 a89, a249\n\t"
    "v_accvgpr_mov_b32 a90, a250\n\t"
    "v_mfma_f32_32x32x16_bf16 v[112:127], a[100:103], v[204:207], v[112:127]\n\t"
    "ds_read_b128 a[100:103], v231 offset:0\n\t"
    "v_accvgpr_mov_b32 a91, a251\n\t"
    "v_accvgpr_mov_b32 a92, a252\n\t"
    "v_accvgpr_mov_b32 a93, a253\n\t"
    "v_accvgpr_mov_b32 a94, a254\n\t"
    "v_accvgpr_mov_b32 a95, a255\n\t"
    "v_mfma_f32_32x32x16_bf16 a[144:159], a[104:107], v[204:207], a[144:159]\n\t"
    "ds_read_b128 a[104:107], v232 offset:0\n\t"
    "buffer_load_dwordx4 v239, s[20:23], s39 offen offset:1024 lds\n\t"
    "s_add_u32 s28, s84, s75\n\t"
    "s_addc_u32 s29, s85, 0\n\t"
    "s_mov_b32 s30, s78\n\t"
    "v_mul_f32 v0, v242, v0\n\t"
    "v_mul_f32 v1, v242, v1\n\t"
    "v_mfma_f32_32x32x16_bf16 a[160:175], a[108:111], v[204:207], a[160:175]\n\t"
    "ds_read_b128 a[108:111], v233 offset:0\n\t"
    "v_mul_f32 v2, v242, v2\n\t"
    "v_mul_f32 v3, v242, v3\n\t"
    "v_mul_f32 v4, v242, v4\n\t"
    "v_mul_f32 v5, v242, v5\n\t"
    "v_mul_f32 v6, v242, v6\n\t"
    "v_mul_f32 v7, v242, v7\n\t"
    "v_mfma_f32_32x32x16_bf16 a[176:191], a[112:115], v[204:207], a[176:191]\n\t"
    "ds_read_b128 a[112:115], v230 offset:1024\n\t"
    "buffer_load_dwordx4 v240, s[20:23], s39 offen offset:2048 lds\n\t"
    "v_cvt_pk_bf16_f32 v0, v0, v1\n\t"
    "v_cvt_pk_bf16_f32 v1, v2, v3\n\t"
    "v_cvt_pk_bf16_f32 v2, v4, v5\n\t"
    "v_cvt_pk_bf16_f32 v3, v6, v7\n\t"
    "ds_write_b128 v244, v[0:3] offset:0\n\t"
    "v_mfma_f32_32x32x16_bf16 a[192:207], a[116:119], v[204:207], a[192:207]\n\t"
    "ds_read_b128 a[116:119], v231 offset:1024\n\t"
    "v_mul_f32 v8, v242, v8\n\t"
    "v_mul_f32 v9, v242, v9\n\t"
    "v_mul_f32 v10, v242, v10\n\t"
    "v_mul_f32 v11, v242, v11\n\t"
    "v_mul_f32 v12, v242, v12\n\t"
    "v_mfma_f32_32x32x16_bf16 v[96:111], a[120:123], v[196:199], v[96:111]\n\t"
    "ds_read_b128 a[120:123], v232 offset:1024\n\t"
    "v_mul_f32 v13, v242, v13\n\t"
    "v_mul_f32 v14, v242, v14\n\t"
    "v_mul_f32 v15, v242, v15\n\t"
    "v_cvt_pk_bf16_f32 v8, v8, v9\n\t"
    "v_cvt_pk_bf16_f32 v9, v10, v11\n\t"
    "v_cvt_pk_bf16_f32 v10, v12, v13\n\t"
    "v_mfma_f32_32x32x16_bf16 v[112:127], a[124:127], v[196:199], v[112:127]\n\t"
    "ds_read_b128 a[124:127], v233 offset:1024\n\t"
    "v_cvt_pk_bf16_f32 v11, v14, v15\n\t"
    "ds_write_b128 v245, v[8:11] offset:0\n\t"
    "v_mul_f32 v16, v242, v16\n\t"
    "v_mul_f32 v17, v242, v17\n\t"
    "v_mul_f32 v18, v242, v18\n\t"
    "v_mul_f32 v19, v242, v19\n\t"
    "v_mfma_f32_32x32x16_bf16 a[144:159], a[128:131], v[196:199], a[144:159]\n\t"
    "ds_read_b128 a[128:131], v230 offset:2048\n\t"
    "v_mul_f32 v20, v242, v20\n\t"
    "v_mul_f32 v21, v242, v21\n\t"
    "v_mul_f32 v22, v242, v22\n\t"
    "v_mul_f32 v23, v242, v23\n\t"
    "v_cvt_pk_bf16_f32 v16, v16, v17\n\t"
    "v_cvt_pk_bf16_f32 v17, v18, v19\n\t"
    "v_mfma_f32_32x32x16_bf16 a[160:175], a[132:135], v[196:199], a[160:175]\n\t"
    "ds_read_b128 a[132:135], v231 offset:2048\n\t"
    "v_cvt_pk_bf16_f32 v18, v20, v21\n\t"
    "v_cvt_pk_bf16_f32 v19, v22, v23\n\t"
    "ds_write_b128 v246, v[16:19] offset:0\n\t"
    "v_mul_f32 v24, v242, v24\n\t"
    "v_mul_f32 v25, v242, v25\n\t"
    "v_mfma_f32_32x32x16_bf16 a[176:191], a[136:139], v[196:199], a[176:191]\n\t"
    "ds_read_b128 a[136:139], v232 offset:2048\n\t"
    "v_mul_f32 v26, v242, v26\n\t"
    "v_mul_f32 v27, v242, v27\n\t"
    "v_mul_f32 v28, v242, v28\n\t"
    "v_mul_f32 v29, v242, v29\n\t"
    "v_mul_f32 v30, v242, v30\n\t"
    "v_mul_f32 v31, v242, v31\n\t"
    "v_mfma_f32_32x32x16_bf16 a[192:207], a[140:143], v[196:199], a[192:207]\n\t"
    "ds_read_b128 a[140:143], v233 offset:2048\n\t"
    "v_cvt_pk_bf16_f32 v24, v24, v25\n\t"
    "v_cvt_pk_bf16_f32 v25, v26, v27\n\t"
    "v_cvt_pk_bf16_f32 v26, v28, v29\n\t"
    "v_cvt_pk_bf16_f32 v27, v30, v31\n\t"
    "ds_write_b128 v247, v[24:27] offset:0\n\t"
    "s_mov_b32 s66, s67\n\t"
    "s_mov_b32 s67, s68\n\t"
    "s_mov_b32 s68, 0\n\t"
    "s_mov_b32 s69, s70\n\t"
    "s_mov_b32 s70, s71\n\t"
    "s_mov_b32 s71, 0\n\t"
    "s_mov_b32 s72, s73\n\t"
    "s_mov_b32 s73, s74\n\t"
    "s_mov_b32 s74, 0\n\t"
    "s_mov_b32 s75, s76\n\t"
    "s_mov_b32 s76, s77\n\t"
    "s_mov_b32 s77, 0\n\t"
    "s_mov_b32 s78, s79\n\t"
    "s_mov_b32 s79, s80\n\t"
    "s_mov_b32 s80, 0\n\t"
    "s_cmp_eq_u32 s72, 24576\n\t"
    "s_cbranch_scc1 .Ld_u0\n\t"
    "s_cmp_eq_u32 s72, 12288\n\t"
    "s_cbranch_scc1 .Ld_u1\n\t"
    ".Ld_final:\n\t"
    "s_nop 7\n\t"
    "s_nop 3\n\t"
    "ds_read_b128 v[128:131], v241 offset:0\n\t"
    "ds_read_b128 v[132:135], v241 offset:1024\n\t"
    "ds_read_b128 v[136:139], v241 offset:2048\n\t"
    "ds_read_b128 v[140:143], v241 offset:3072\n\t"
    "v_pk_mul_f32 v[32:33], v[242:243], v[32:33] op_sel_hi:[0,1]\n\t"
    "v_pk_mul_f32 v[34:35], v[242:243], v[34:35] op_sel_hi:[0,1]\n\t"
    "v_pk_mul_f32 v[36:37], v[242:243], v[36:37] op_sel_hi:[0,1]\n\t"
    "v_pk_mul_f32 v[38:39], v[242:243], v[38:39] op_sel_hi:[0,1]\n\t"
    "v_cvt_pk_bf16_f32 v32, v32, v33\n\t"
    "v_cvt_pk_bf16_f32 v33, v34, v35\n\t"
    "v_cvt_pk_bf16_f32 v34, v36, v37\n\t"
    "s_waitcnt lgkmcnt(3)\n\t"
    "buffer_store_dwordx4 v[128:131], v248, s[28:31], 0 offen nt\n\t"
    "v_cvt_pk_bf16_f32 v35, v38, v39\n\t"
    "ds_write_b128 v244, v[32:35] offset:4096\n\t"
    "v_pk_mul_f32 v[40:41], v[242:243], v[40:41] op_sel_hi:[0,1]\n\t"
    "v_pk_mul_f32 v[42:43], v[242:243], v[42:43] op_sel_hi:[0,1]\n\t"
    "v_pk_mul_f32 v[44:45], v[242:243], v[44:45] op_sel_hi:[0,1]\n\t"
    "v_pk_mul_f32 v[46:47], v[242:243], v[46:47] op_sel_hi:[0,1]\n\t"
    "v_cvt_pk_bf16_f32 v40, v40, v41\n\t"
    "s_waitcnt lgkmcnt(3)\n\t"
    "buffer_store_dwordx4 v[132:135], v249, s[28:31], 0 offen nt\n\t"
    "v_cvt_pk_bf16_f32 v41, v42, v43\n\t"
    "v_cvt_pk_bf16_f32 v42, v44, v45\n\t"
    "v_cvt_pk_bf16_f32 v43, v46, v47\n\t"
    "ds_write_b128 v245, v[40:43] offset:4096\n\t"
    "v_pk_mul_f32 v[48:49], v[242:243], v[48:49] op_sel_hi:[0,1]\n\t"
    "v_pk_mul_f32 v[50:51], v[242:243], v[50:51] op_sel_hi:[0,1]\n\t"
    "v_pk_mul_f32 v[52:53], v[242:243], v[52:53] op_sel_hi:[0,1]\n\t"
    "s_waitcnt lgkmcnt(3)\n\t"
    "buffer_store_dwordx4 v[136:139], v248, s[28:31], s86 offen nt\n\t"
    "v_pk_mul_f32 v[54:55], v[242:243], v[54:55] op_sel_hi:[0,1]\n\t"
    "v_cvt_pk_bf16_f32 v48, v48, v49\n\t"
    "v_cvt_pk_bf16_f32 v49, v50, v51\n\t"
    "v_cvt_pk_bf16_f32 v50, v52, v53\n\t"
    "v_cvt_pk_bf16_f32 v51, v54, v55\n\t"
    "ds_write_b128 v246, v[48:51] offset:4096\n\t"
    "v_pk_mul_f32 v[56:57], v[242:243], v[56:57] op_sel_hi:[0,1]\n\t"
    "s_waitcnt lgkmcnt(3)\n\t"
    "buffer_store_dwordx4 v[140:143], v249, s[28:31], s86 offen nt\n\t"
    "v_pk_mul_f32 v[58:59], v[242:243], v[58:59] op_sel_hi:[0,1]\n\t"
    "v_pk_mul_f32 v[60:61], v[242:243], v[60:61] op_sel_hi:[0,1]\n\t"
    "v_pk_mul_f32 v[62:63], v[242:243], v[62:63] op_sel_hi:[0,1]\n\t"
    "v_cvt_pk_bf16_f32 v56, v56, v57\n\t"
    "v_cvt_pk_bf16_f32 v57, v58, v59\n\t"
    "v_cvt_pk_bf16_f32 v58, v60, v61\n\t"
    "v_cvt_pk_bf16_f32 v59, v62, v63\n\t"
    "ds_write_b128 v247, v[56:59] offset:4096\n\t"
    "ds_read_b128 v[144:147], v241 offset:4096\n\t"
    "ds_read_b128 v[148:151], v241 offset:5120\n\t"
    "ds_read_b128 v[152:155], v241 offset:6144\n\t"
    "ds_read_b128 v[156:159], v241 offset:7168\n\t"
    "v_pk_mul_f32 v[64:65], v[242:243], v[64:65] op_sel_hi:[0,1]\n\t"
    "v_pk_mul_f32 v[66:67], v[242:243], v[66:67] op_sel_hi:[0,1]\n\t"
    "v_pk_mul_f32 v[68:69], v[242:243], v[68:69] op_sel_hi:[0,1]\n\t"
    "v_pk_mul_f32 v[70:71], v[242:243], v[70:71] op_sel_hi:[0,1]\n\t"
    "v_cvt_pk_bf16_f32 v64, v64, v65\n\t"
    "v_cvt_pk_bf16_f32 v65, v66, v67\n\t"
    "v_cvt_pk_bf16_f32 v66, v68, v69\n\t"
    "s_waitcnt lgkmcnt(3)\n\t"
    "buffer_store_dwordx4 v[144:147], v248, s[28:31], 0 offen offset:128 nt\n\t"
    "v_cvt_pk_bf16_f32 v67, v70, v71\n\t"
    "ds_write_b128 v244, v[64:67] offset:8192\n\t"
    "v_pk_mul_f32 v[72:73], v[242:243], v[72:73] op_sel_hi:[0,1]\n\t"
    "v_pk_mul_f32 v[74:75], v[242:243], v[74:75] op_sel_hi:[0,1]\n\t"
    "v_pk_mul_f32 v[76:77], v[242:243], v[76:77] op_sel_hi:[0,1]\n\t"
    "v_pk_mul_f32 v[78:79], v[242:243], v[78:79] op_sel_hi:[0,1]\n\t"
    "v_cvt_pk_bf16_f32 v72, v72, v73\n\t"
    "s_waitcnt lgkmcnt(3)\n\t"
    "buffer_store_dwordx4 v[148:151], v249, s[28:31], 0 offen offset:128 nt\n\t"
    "v_cvt_pk_bf16_f32 v73, v74, v75\n\t"
    "v_cvt_pk_bf16_f32 v74, v76, v77\n\t"
    "v_cvt_pk_bf16_f32 v75, v78, v79\n\t"
    "ds_write_b128 v245, v[72:75] offset:8192\n\t"
    "v_pk_mul_f32 v[80:81], v[242:243], v[80:81] op_sel_hi:[0,1]\n\t"
    "v_pk_mul_f32 v[82:83], v[242:243], v[82:83] op_sel_hi:[0,1]\n\t"
    "v_pk_mul_f32 v[84:85], v[242:243], v[84:85] op_sel_hi:[0,1]\n\t"
    "s_waitcnt lgkmcnt(3)\n\t"
    "buffer_store_dwordx4 v[152:155], v248, s[28:31], s86 offen offset:128 nt\n\t"
    "v_pk_mul_f32 v[86:87], v[242:243], v[86:87] op_sel_hi:[0,1]\n\t"
    "v_cvt_pk_bf16_f32 v80, v80, v81\n\t"
    "v_cvt_pk_bf16_f32 v81, v82, v83\n\t"
    "v_cvt_pk_bf16_f32 v82, v84, v85\n\t"
    "v_cvt_pk_bf16_f32 v83, v86, v87\n\t"
    "ds_write_b128 v246, v[80:83] offset:8192\n\t"
    "v_pk_mul_f32 v[88:89], v[242:243], v[88:89] op_sel_hi:[0,1]\n\t"
    "s_waitcnt lgkmcnt(3)\n\t"
    "buffer_store_dwordx4 v[156:159], v249, s[28:31], s86 offen offset:128 nt\n\t"
    "v_pk_mul_f32 v[90:91], v[242:243], v[90:91] op_sel_hi:[0,1]\n\t"
    "v_pk_mul_f32 v[92:93], v[242:243], v[92:93] op_sel_hi:[0,1]\n\t"
    "v_pk_mul_f32 v[94:95], v[242:243], v[94:95] op_sel_hi:[0,1]\n\t"
    "v_cvt_pk_bf16_f32 v88, v88, v89\n\t"
    "v_cvt_pk_bf16_f32 v89, v90, v91\n\t"
    "v_cvt_pk_bf16_f32 v90, v92, v93\n\t"
    "v_cvt_pk_bf16_f32 v91, v94, v95\n\t"
    "ds_write_b128 v247, v[88:91] offset:8192\n\t"
    "ds_read_b128 v[160:163], v241 offset:8192\n\t"
    "ds_read_b128 v[164:167], v241 offset:9216\n\t"
    "ds_read_b128 v[168:171], v241 offset:10240\n\t"
    "ds_read_b128 v[172:175], v241 offset:11264\n\t"
    "v_pk_mul_f32 v[96:97], v[242:243], v[96:97] op_sel:[1,0] op_sel_hi:[1,1]\n\t"
    "v_pk_mul_f32 v[98:99], v[242:243], v[98:99] op_sel:[1,0] op_sel_hi:[1,1]\n\t"
    "v_pk_mul_f32 v[100:101], v[242:243], v[100:101] op_sel:[1,0] op_sel_hi:[1,1]\n\t"
    "v_pk_mul_f32 v[102:103], v[242:243], v[102:103] op_sel:[1,0] op_sel_hi:[1,1]\n\t"
    "v_cvt_pk_bf16_f32 v96, v96, v97\n\t"
    "v_cvt_pk_bf16_f32 v97, v98, v99\n\t"
    "v_cvt_pk_bf16_f32 v98, v100, v101\n\t"
    "s_waitcnt lgkmcnt(3)\n\t"
    "buffer_store_dwordx4 v[160:163], v248, s[28:31], 0 offen offset:256 nt\n\t"
    "v_cvt_pk_bf16_f32 v99, v102, v103\n\t"
    "ds_write_b128 v244, v[96:99] offset:0\n\t"
    "v_pk_mul_f32 v[104:105], v[242:243], v[104:105] op_sel:[1,0] op_sel_hi:[1,1]\n\t"
    "v_pk_mul_f32 v[106:107], v[242:243], v[106:107] op_sel:[1,0] op_sel_hi:[1,1]\n\t"
    "v_pk_mul_f32 v[108:109], v[242:243], v[108:109] op_sel:[1,0] op_sel_hi:[1,1]\n\t"
    "v_pk_mul_f32 v[110:111], v[242:243], v[110:111] op_sel:[1,0] op_sel_hi:[1,1]\n\t"
    "v_cvt_pk_bf16_f32 v104, v104, v105\n\t"
    "s_waitcnt lgkmcnt(3)\n\t"
    "buffer_store_dwordx4 v[164:167], v249, s[28:31], 0 offen offset:256 nt\n\t"
    "v_cvt_pk_bf16_f32 v105, v106, v107\n\t"
    "v_cvt_pk_bf16_f32 v106, v108, v109\n\t"
    "v_cvt_pk_bf16_f32 v107, v110, v111\n\t"
    "ds_write_b128 v245, v[104:107] offset:0\n\t"
    "v_pk_mul_f32 v[112:113], v[242:243], v[112:113] op_sel:[1,0] op_sel_hi:[1,1]\n\t"
    "v_pk_mul_f32 v[114:115], v[242:243], v[114:115] op_sel:[1,0] op_sel_hi:[1,1]\n\t"
    "v_pk_mul_f32 v[116:117], v[242:243], v[116:117] op_sel:[1,0] op_sel_hi:[1,1]\n\t"
    "s_waitcnt lgkmcnt(3)\n\t"
    "buffer_store_dwordx4 v[168:171], v248, s[28:31], s86 offen offset:256 nt\n\t"
    "v_pk_mul_f32 v[118:119], v[242:243], v[118:119] op_sel:[1,0] op_sel_hi:[1,1]\n\t"
    "v_cvt_pk_bf16_f32 v112, v112, v113\n\t"
    "v_cvt_pk_bf16_f32 v113, v114, v115\n\t"
    "v_cvt_pk_bf16_f32 v114, v116, v117\n\t"
    "v_cvt_pk_bf16_f32 v115, v118, v119\n\t"
    "ds_write_b128 v246, v[112:115] offset:0\n\t"
    "v_pk_mul_f32 v[120:121], v[242:243], v[120:121] op_sel:[1,0] op_sel_hi:[1,1]\n\t"
    "s_waitcnt lgkmcnt(3)\n\t"
    "buffer_store_dwordx4 v[172:175], v249, s[28:31], s86 offen offset:256 nt\n\t"
    "v_pk_mul_f32 v[122:123], v[242:243], v[122:123] op_sel:[1,0] op_sel_hi:[1,1]\n\t"
    "v_pk_mul_f32 v[124:125], v[242:243], v[124:125] op_sel:[1,0] op_sel_hi:[1,1]\n\t"
    "v_pk_mul_f32 v[126:127], v[242:243], v[126:127] op_sel:[1,0] op_sel_hi:[1,1]\n\t"
    "v_cvt_pk_bf16_f32 v120, v120, v121\n\t"
    "v_cvt_pk_bf16_f32 v121, v122, v123\n\t"
    "v_cvt_pk_bf16_f32 v122, v124, v125\n\t"
    "v_cvt_pk_bf16_f32 v123, v126, v127\n\t"
    "ds_write_b128 v247, v[120:123] offset:0\n\t"
    "ds_read_b128 v[176:179], v241 offset:0\n\t"
    "ds_read_b128 v[180:183], v241 offset:1024\n\t"
    "ds_read_b128 v[184:187], v241 offset:2048\n\t"
    "ds_read_b128 v[188:191], v241 offset:3072\n\t"
    "v_accvgpr_read_b32 v0, a144\n\t"
    "v_accvgpr_read_b32 v1, a145\n\t"
    "v_accvgpr_read_b32 v2, a146\n\t"
    "v_accvgpr_read_b32 v3, a147\n\t"
    "v_accvgpr_read_b32 v4, a148\n\t"
    "v_accvgpr_read_b32 v5, a149\n\t"
    "v_accvgpr_read_b32 v6, a150\n\t"
    "v_accvgpr_read_b32 v7, a151\n\t"
    "v_pk_mul_f32 v[0:1], v[242:243], v[0:1] op_sel:[1,0] op_sel_hi:[1,1]\n\t"
    "v_pk_mul_f32 v[2:3], v[242:243], v[2:3] op_sel:[1,0] op_sel_hi:[1,1]\n\t"
    "v_pk_mul_f32 v[4:5], v[242:243], v[4:5] op_sel:[1,0] op_sel_hi:[1,1]\n\t"
    "v_pk_mul_f32 v[6:7], v[242:243], v[6:7] op_sel:[1,0] op_sel_hi:[1,1]\n\t"
    "v_cvt_pk_bf16_f32 v0, v0, v1\n\t"
    "s_waitcnt lgkmcnt(3)\n\t"
    "buffer_store_dwordx4 v[176:179], v248, s[28:31], s87 offen nt\n\t"
    "v_cvt_pk_bf16_f32 v1, v2, v3\n\t"
    "v_cvt_pk_bf16_f32 v2, v4, v5\n\t"
    "v_cvt_pk_bf16_f32 v3, v6, v7\n\t"
    "ds_write_b128 v244, v[0:3] offset:4096\n\t"
    "v_accvgpr_read_b32 v8, a152\n\t"
    "v_accvgpr_read_b32 v9, a153\n\t"
    "v_accvgpr_read_b32 v10, a154\n\t"
    "v_accvgpr_read_b32 v11, a155\n\t"
    "v_accvgpr_read_b32 v12, a156\n\t"
    "v_accvgpr_read_b32 v13, a157\n\t"
    "v_accvgpr_read_b32 v14, a158\n\t"
    "v_accvgpr_read_b32 v15, a159\n\t"
    "v_pk_mul_f32 v[8:9], v[242:243], v[8:9] op_sel:[1,0] op_sel_hi:[1,1]\n\t"
    "s_waitcnt lgkmcnt(3)\n\t"
    "buffer_store_dwordx4 v[180:183], v249, s[28:31], s87 offen nt\n\t"
    "v_pk_mul_f32 v[10:11], v[242:243], v[10:11] op_sel:[1,0] op_sel_hi:[1,1]\n\t"
    "v_pk_mul_f32 v[12:13], v[242:243], v[12:13] op_sel:[1,0] op_sel_hi:[1,1]\n\t"
    "v_pk_mul_f32 v[14:15], v[242:243], v[14:15] op_sel:[1,0] op_sel_hi:[1,1]\n\t"
    "v_cvt_pk_bf16_f32 v8, v8, v9\n\t"
    "v_cvt_pk_bf16_f32 v9, v10, v11\n\t"
    "v_cvt_pk_bf16_f32 v10, v12, v13\n\t"
    "v_cvt_pk_bf16_f32 v11, v14, v15\n\t"
    "ds_write_b128 v245, v[8:11] offset:4096\n\t"
    "v_accvgpr_read_b32 v16, a160\n\t"
    "v_accvgpr_read_b32 v17, a161\n\t"
    "v_accvgpr_read_b32 v18, a162\n\t"
    "v_accvgpr_read_b32 v19, a163\n\t"
    "v_accvgpr_read_b32 v20, a164\n\t"
    "s_waitcnt lgkmcnt(3)\n\t"
    "buffer_store_dwordx4 v[184:187], v248, s[28:31], s43 offen nt\n\t"
    "v_accvgpr_read_b32 v21, a165\n\t"
    "v_accvgpr_read_b32 v22, a166\n\t"
    "v_accvgpr_read_b32 v23, a167\n\t"
    "v_pk_mul_f32 v[16:17], v[242:243], v[16:17] op_sel:[1,0] op_sel_hi:[1,1]\n\t"
    "v_pk_mul_f32 v[18:19], v[242:243], v[18:19] op_sel:[1,0] op_sel_hi:[1,1]\n\t"
    "v_pk_mul_f32 v[20:21], v[242:243], v[20:21] op_sel:[1,0] op_sel_hi:[1,1]\n\t"
    "v_pk_mul_f32 v[22:23], v[242:243], v[22:23] op_sel:[1,0] op_sel_hi:[1,1]\n\t"
    "v_cvt_pk_bf16_f32 v16, v16, v17\n\t"
    "v_cvt_pk_bf16_f32 v17, v18, v19\n\t"
    "v_cvt_pk_bf16_f32 v18, v20, v21\n\t"
    "v_cvt_pk_bf16_f32 v19, v22, v23\n\t"
    "ds_write_b128 v246, v[16:19] offset:4096\n\t"
    "v_accvgpr_read_b32 v24, a168\n\t"
    "s_waitcnt lgkmcnt(3)\n\t"
    "buffer_store_dwordx4 v[188:191], v249, s[28:31], s43 offen nt\n\t"
    "v_accvgpr_read_b32 v25, a169\n\t"
    "v_accvgpr_read_b32 v26, a170\n\t"
    "v_accvgpr_read_b32 v27, a171\n\t"
    "v_accvgpr_read_b32 v28, a172\n\t"
    "v_accvgpr_read_b32 v29, a173\n\t"
    "v_accvgpr_read_b32 v30, a174\n\t"
    "v_accvgpr_read_b32 v31, a175\n\t"
    "v_pk_mul_f32 v[24:25], v[242:243], v[24:25] op_sel:[1,0] op_sel_hi:[1,1]\n\t"
    "v_pk_mul_f32 v[26:27], v[242:243], v[26:27] op_sel:[1,0] op_sel_hi:[1,1]\n\t"
    "v_pk_mul_f32 v[28:29], v[242:243], v[28:29] op_sel:[1,0] op_sel_hi:[1,1]\n\t"
    "v_pk_mul_f32 v[30:31], v[242:243], v[30:31] op_sel:[1,0] op_sel_hi:[1,1]\n\t"
    "v_cvt_pk_bf16_f32 v24, v24, v25\n\t"
    "v_cvt_pk_bf16_f32 v25, v26, v27\n\t"
    "v_cvt_pk_bf16_f32 v26, v28, v29\n\t"
    "v_cvt_pk_bf16_f32 v27, v30, v31\n\t"
    "ds_write_b128 v247, v[24:27] offset:4096\n\t"
    "ds_read_b128 v[192:195], v241 offset:4096\n\t"
    "ds_read_b128 v[196:199], v241 offset:5120\n\t"
    "ds_read_b128 v[200:203], v241 offset:6144\n\t"
    "ds_read_b128 v[204:207], v241 offset:7168\n\t"
    "v_accvgpr_read_b32 v32, a176\n\t"
    "v_accvgpr_read_b32 v33, a177\n\t"
    "v_accvgpr_read_b32 v34, a178\n\t"
    "v_accvgpr_read_b32 v35, a179\n\t"
    "v_accvgpr_read_b32 v36, a180\n\t"
    "v_accvgpr_read_b32 v37, a181\n\t"
    "v_accvgpr_read_b32 v38, a182\n\t"
    "v_accvgpr_read_b32 v39, a183\n\t"
    "v_pk_mul_f32 v[32:33], v[242:243], v[32:33] op_sel:[1,0] op_sel_hi:[1,1]\n\t"
    "v_pk_mul_f32 v[34:35], v[242:243], v[34:35] op_sel:[1,0] op_sel_hi:[1,1]\n\t"
    "v_pk_mul_f32 v[36:37], v[242:243], v[36:37] op_sel:[1,0] op_sel_hi:[1,1]\n\t"
    "v_pk_mul_f32 v[38:39], v[242:243], v[38:39] op_sel:[1,0] op_sel_hi:[1,1]\n\t"
    "v_cvt_pk_bf16_f32 v32, v32, v33\n\t"
    "s_waitcnt lgkmcnt(3)\n\t"
    "buffer_store_dwordx4 v[192:195], v248, s[28:31], s87 offen offset:128 nt\n\t"
    "v_cvt_pk_bf16_f32 v33, v34, v35\n\t"
    "v_cvt_pk_bf16_f32 v34, v36, v37\n\t"
    "v_cvt_pk_bf16_f32 v35, v38, v39\n\t"
    "ds_write_b128 v244, v[32:35] offset:8192\n\t"
    "v_accvgpr_read_b32 v40, a184\n\t"
    "v_accvgpr_read_b32 v41, a185\n\t"
    "v_accvgpr_read_b32 v42, a186\n\t"
    "v_accvgpr_read_b32 v43, a187\n\t"
    "v_accvgpr_read_b32 v44, a188\n\t"
    "v_accvgpr_read_b32 v45, a189\n\t"
    "v_accvgpr_read_b32 v46, a190\n\t"
    "v_accvgpr_read_b32 v47, a191\n\t"
    "v_pk_mul_f32 v[40:41], v[242:243], v[40:41] op_sel:[1,0] op_sel_hi:[1,1]\n\t"
    "s_waitcnt lgkmcnt(3)\n\t"
    "buffer_store_dwordx4 v[196:199], v249, s[28:31], s87 offen offset:128 nt\n\t"
    "v_pk_mul_f32 v[42:43], v[242:243], v[42:43] op_sel:[1,0] op_sel_hi:[1,1]\n\t"
    "v_pk_mul_f32 v[44:45], v[242:243], v[44:45] op_sel:[1,0] op_sel_hi:[1,1]\n\t"
    "v_pk_mul_f32 v[46:47], v[242:243], v[46:47] op_sel:[1,0] op_sel_hi:[1,1]\n\t"
    "v_cvt_pk_bf16_f32 v40, v40, v41\n\t"
    "v_cvt_pk_bf16_f32 v41, v42, v43\n\t"
    "v_cvt_pk_bf16_f32 v42, v44, v45\n\t"
    "v_cvt_pk_bf16_f32 v43, v46, v47\n\t"
    "ds_write_b128 v245, v[40:43] offset:8192\n\t"
    "v_accvgpr_read_b32 v48, a192\n\t"
    "v_accvgpr_read_b32 v49, a193\n\t"
    "v_accvgpr_read_b32 v50, a194\n\t"
    "v_accvgpr_read_b32 v51, a195\n\t"
    "v_accvgpr_read_b32 v52, a196\n\t"
    "s_waitcnt lgkmcnt(3)\n\t"
    "buffer_store_dwordx4 v[200:203], v248, s[28:31], s43 offen offset:128 nt\n\t"
    "v_accvgpr_read_b32 v53, a197\n\t"
    "v_accvgpr_read_b32 v54, a198\n\t"
    "v_accvgpr_read_b32 v55, a199\n\t"
    "v_pk_mul_f32 v[48:49], v[242:243], v[48:49] op_sel:[1,0] op_sel_hi:[1,1]\n\t"
    "v_pk_mul_f32 v[50:51], v[242:243], v[50:51] op_sel:[1,0] op_sel_hi:[1,1]\n\t"
    "v_pk_mul_f32 v[52:53], v[242:243], v[52:53] op_sel:[1,0] op_sel_hi:[1,1]\n\t"
    "v_pk_mul_f32 v[54:55], v[242:243], v[54:55] op_sel:[1,0] op_sel_hi:[1,1]\n\t"
    "v_cvt_pk_bf16_f32 v48, v48, v49\n\t"
    "v_cvt_pk_bf16_f32 v49, v50, v51\n\t"
    "v_cvt_pk_bf16_f32 v50, v52, v53\n\t"
    "v_cvt_pk_bf16_f32 v51, v54, v55\n\t"
    "ds_write_b128 v246, v[48:51] offset:8192\n\t"
    "v_accvgpr_read_b32 v56, a200\n\t"
    "s_waitcnt lgkmcnt(3)\n\t"
    "buffer_store_dwordx4 v[204:207], v249, s[28:31], s43 offen offset:128 nt\n\t"
    "v_accvgpr_read_b32 v57, a201\n\t"
    "v_accvgpr_read_b32 v58, a202\n\t"
    "v_accvgpr_read_b32 v59, a203\n\t"
    "v_accvgpr_read_b32 v60, a204\n\t"
    "v_accvgpr_read_b32 v61, a205\n\t"
    "v_accvgpr_read_b32 v62, a206\n\t"
    "v_accvgpr_read_b32 v63, a207\n\t"
    "v_pk_mul_f32 v[56:57], v[242:243], v[56:57] op_sel:[1,0] op_sel_hi:[1,1]\n\t"
    "v_pk_mul_f32 v[58:59], v[242:243], v[58:59] op_sel:[1,0] op_sel_hi:[1,1]\n\t"
    "v_pk_mul_f32 v[60:61], v[242:243], v[60:61] op_sel:[1,0] op_sel_hi:[1,1]\n\t"
    "v_pk_mul_f32 v[62:63], v[242:243], v[62:63] op_sel:[1,0] op_sel_hi:[1,1]\n\t"
    "v_cvt_pk_bf16_f32 v56, v56, v57\n\t"
    "v_cvt_pk_bf16_f32 v57, v58, v59\n\t"
    "v_cvt_pk_bf16_f32 v58, v60, v61\n\t"
    "v_cvt_pk_bf16_f32 v59, v62, v63\n\t"
    "ds_write_b128 v247, v[56:59] offset:8192\n\t"
    "ds_read_b128 v[208:211], v241 offset:8192\n\t"
    "ds_read_b128 v[212:215], v241 offset:9216\n\t"
    "ds_read_b128 v[216:219], v241 offset:10240\n\t"
    "ds_read_b128 v[220:223], v241 offset:11264\n\t"
    "s_waitcnt lgkmcnt(3)\n\t"
    "buffer_store_dwordx4 v[208:211], v248, s[28:31], s87 offen offset:256 nt\n\t"
    "s_waitcnt lgkmcnt(2)\n\t"
    "buffer_store_dwordx4 v[212:215], v249, s[28:31], s87 offen offset:256 nt\n\t"
    "s_waitcnt lgkmcnt(1)\n\t"
    "buffer_store_dwordx4 v[216:219], v248, s[28:31], s43 offen offset:256 nt\n\t"
    "s_waitcnt lgkmcnt(0)\n\t"
    "buffer_store_dwordx4 v[220:223], v249, s[28:31], s43 offen offset:256 nt\n\t"
    "s_waitcnt vmcnt(0) lgkmcnt(0)\n\t"
    "s_branch .Ld_end\n\t"
    ".Ld_rare:\n\t"
    ".Ld_stub0:\n\t"
    "s_mov_b32 s65, 0\n\t"
    "s_branch .Ld_rare1\n\t"
    ".Ld_stub1:\n\t"
    "s_mov_b32 s65, 1\n\t"
    "s_branch .Ld_rare0\n\t"
    ".Ld_stub2:\n\t"
    "s_mov_b32 s65, 2\n\t"
    "s_branch .Ld_rare1\n\t"
    ".Ld_stub3:\n\t"
    "s_mov_b32 s65, 3\n\t"
    "s_branch .Ld_rare0\n\t"
    ".Ld_stub4:\n\t"
    "s_mov_b32 s65, 4\n\t"
    "s_branch .Ld_rare1\n\t"
    ".Ld_stub5:\n\t"
    "s_mov_b32 s65, 5\n\t"
    "s_branch .Ld_rare0\n\t"
    ".Ld_stub6:\n\t"
    "s_mov_b32 s65, 6\n\t"
    "s_branch .Ld_rare1\n\t"
    ".Ld_stub7:\n\t"
    "s_mov_b32 s65, 7\n\t"
    "s_branch .Ld_rare0\n\t"
    ".Ld_stub8:\n\t"
    "s_mov_b32 s65, 8\n\t"
    "s_branch .Ld_rare1\n\t"
    ".Ld_stub9:\n\t"
    "s_mov_b32 s65, 9\n\t"
    "s_branch .Ld_rare0\n\t"
    ".Ld_stub10:\n\t"
    "s_mov_b32 s65, 10\n\t"
    "s_branch .Ld_rare1\n\t"
    ".Ld_stub11:\n\t"
    "s_mov_b32 s65, 11\n\t"
    "s_branch .Ld_rare0\n\t"
    ".Ld_stub12:\n\t"
    "s_mov_b32 s65, 12\n\t"
    "s_branch .Ld_rare1\n\t"
    ".Ld_stub13:\n\t"
    "s_mov_b32 s65, 13\n\t"
    "s_branch .Ld_rare0\n\t"
    ".Ld_stub14:\n\t"
    "s_mov_b32 s65, 14\n\t"
    "s_branch .Ld_rare2\n\t"
    ".Ld_stub15:\n\t"
    "s_mov_b32 s65, 15\n\t"
    "s_branch .Ld_rare1\n\t"
    ".Ld_stub16:\n\t"
    "s_mov_b32 s65, 16\n\t"
    "s_branch .Ld_rare0\n\t"
    ".Ld_stub17:\n\t"
    "s_mov_b32 s65, 17\n\t"
    "s_branch .Ld_rare1\n\t"
    ".Ld_stub18:\n\t"
    "s_mov_b32 s65, 18\n\t"
    "s_branch .Ld_rare0\n\t"
    ".Ld_stub19:\n\t"
    "s_mov_b32 s65, 19\n\t"
    "s_branch .Ld_rare1\n\t"
    ".Ld_stub20:\n\t"
    "s_mov_b32 s65, 20\n\t"
    "s_branch .Ld_rare0\n\t"
    ".Ld_stub21:\n\t"
    "s_mov_b32 s65, 21\n\t"
    "s_branch .Ld_rare1\n\t"
    ".Ld_stub22:\n\t"
    "s_mov_b32 s65, 22\n\t"
    "s_branch .Ld_rare0\n\t"
    ".Ld_stub23:\n\t"
    "s_mov_b32 s65, 23\n\t"
    "s_branch .Ld_rare1\n\t"
    ".Ld_stub24:\n\t"
    "s_mov_b32 s65, 24\n\t"
    "s_branch .Ld_rare0\n\t"
    ".Ld_stub25:\n\t"
    "s_mov_b32 s65, 25\n\t"
    "s_branch .Ld_rare1\n\t"
    ".Ld_stub26:\n\t"
    "s_mov_b32 s65, 26\n\t"
    "s_branch .Ld_rare0\n\t"
    ".Ld_stub27:\n\t"
    "s_mov_b32 s65, 27\n\t"
    "s_branch .Ld_rare1\n\t"
    ".Ld_stub28:\n\t"
    "s_mov_b32 s65, 28\n\t"
    "s_branch .Ld_rare0\n\t"
    ".Ld_stub29:\n\t"
    "s_mov_b32 s65, 29\n\t"
    "s_branch .Ld_rare2\n\t"
    ".Ld_rare0:\n\t"
    "s_nop 7\n\t"
    "s_nop 7\n\t"
    "v_max3_f32 v250, v128, v129, v130\n\t"
    "v_max3_f32 v251, v131, v132, v133\n\t"
    "v_max3_f32 v250, v250, v134, v135\n\t"
    "v_max3_f32 v251, v251, v136, v137\n\t"
    "v_max3_f32 v250, v250, v138, v139\n\t"
    "v_max3_f32 v251, v251, v140, v141\n\t"
    "v_max3_f32 v250, v250, v142, v143\n\t"
    "v_max_f32 v250, v250, v251\n\t"
    "v_mov_b32 v251, v250\n\t"
    "s_nop 1\n\t"
    "v_permlane32_swap_b32 v250, v251\n\t"
    "v_max_f32 v250, v250, v251\n\t"
    "v_mul_f32 v250, s63, v250\n\t"
    "v_max_f32_e64 v250, v250, -v224\n\t"
    "v_add_f32 v216, v224, v250\n\t"
    "v_exp_f32_e64 v216, -v216\n\t"
    "v_sub_f32 v224, 0, v250\n\t"
    "v_mul_f32 v226, v216, v226\n\t"
    "v_pk_mul_f32 v[0:1], v[216:217], v[0:1] op_sel_hi:[0,1]\n\t"
    "v_pk_mul_f32 v[2:3], v[216:217], v[2:3] op_sel_hi:[0,1]\n\t"
    "v_pk_mul_f32 v[4:5], v[216:217], v[4:5] op_sel_hi:[0,1]\n\t"
    "v_pk_mul_f32 v[6:7], v[216:217], v[6:7] op_sel_hi:[0,1]\n\t"
    "v_pk_mul_f32 v[8:9], v[216:217], v[8:9] op_sel_hi:[0,1]\n\t"
    "v_pk_mul_f32 v[10:11], v[216:217], v[10:11] op_sel_hi:[0,1]\n\t"
    "v_pk_mul_f32 v[12:13], v[216:217], v[12:13] op_sel_hi:[0,1]\n\t"
    "v_pk_mul_f32 v[14:15], v[216:217], v[14:15] op_sel_hi:[0,1]\n\t"
    "v_pk_mul_f32 v[16:17], v[216:217], v[16:17] op_sel_hi:[0,1]\n\t"
    "v_pk_mul_f32 v[18:19], v[216:217], v[18:19] op_sel_hi:[0,1]\n\t"
    "v_pk_mul_f32 v[20:21], v[216:217], v[20:21] op_sel_hi:[0,1]\n\t"
    "v_pk_mul_f32 v[22:23], v[216:217], v[22:23] op_sel_hi:[0,1]\n\t"
    "v_pk_mul_f32 v[24:25], v[216:217], v[24:25] op_sel_hi:[0,1]\n\t"
    "v_pk_mul_f32 v[26:27], v[216:217], v[26:27] op_sel_hi:[0,1]\n\t"
    "v_pk_mul_f32 v[28:29], v[216:217], v[28:29] op_sel_hi:[0,1]\n\t"
    "v_pk_mul_f32 v[30:31], v[216:217], v[30:31] op_sel_hi:[0,1]\n\t"
    "v_pk_mul_f32 v[32:33], v[216:217], v[32:33] op_sel_hi:[0,1]\n\t"
    "v_pk_mul_f32 v[34:35], v[216:217], v[34:35] op_sel_hi:[0,1]\n\t"
    "v_pk_mul_f32 v[36:37], v[216:217], v[36:37] op_sel_hi:[0,1]\n\t"
    "v_pk_mul_f32 v[38:39], v[216:217], v[38:39] op_sel_hi:[0,1]\n\t"
    "v_pk_mul_f32 v[40:41], v[216:217], v[40:41] op_sel_hi:[0,1]\n\t"
    "v_pk_mul_f32 v[42:43], v[216:217], v[42:43] op_sel_hi:[0,1]\n\t"
    "v_pk_mul_f32 v[44:45], v[216:217], v[44:45] op_sel_hi:[0,1]\n\t"
    "v_pk_mul_f32 v[46:47], v[216:217], v[46:47] op_sel_hi:[0,1]\n\t"
    "v_pk_mul_f32 v[48:49], v[216:217], v[48:49] op_sel_hi:[0,1]\n\t"
    "v_pk_mul_f32 v[50:51], v[216:217], v[50:51] op_sel_hi:[0,1]\n\t"
    "v_pk_mul_f32 v[52:53], v[216:217], v[52:53] op_sel_hi:[0,1]\n\t"
    "v_pk_mul_f32 v[54:55], v[216:217], v[54:55] op_sel_hi:[0,1]\n\t"
    "v_pk_mul_f32 v[56:57], v[216:217], v[56:57] op_sel_hi:[0,1]\n\t"
    "v_pk_mul_f32 v[58:59], v[216:217], v[58:59] op_sel_hi:[0,1]\n\t"
    "v_pk_mul_f32 v[60:61], v[216:217], v[60:61] op_sel_hi:[0,1]\n\t"
    "v_pk_mul_f32 v[62:63], v[216:217], v[62:63] op_sel_hi:[0,1]\n\t"
    "v_pk_mul_f32 v[64:65], v[216:217], v[64:65] op_sel_hi:[0,1]\n\t"
    "v_pk_mul_f32 v[66:67], v[216:217], v[66:67] op_sel_hi:[0,1]\n\t"
    "v_pk_mul_f32 v[68:69], v[216:217], v[68:69] op_sel_hi:[0,1]\n\t"
    "v_pk_mul_f32 v[70:71], v[216:217], v[70:71] op_sel_hi:[0,1]\n\t"
    "v_pk_mul_f32 v[72:73], v[216:217], v[72:73] op_sel_hi:[0,1]\n\t"
    "v_pk_mul_f32 v[74:75], v[216:217], v[74:75] op_sel_hi:[0,1]\n\t"
    "v_pk_mul_f32 v[76:77], v[216:217], v[76:77] op_sel_hi:[0,1]\n\t"
    "v_pk_mul_f32 v[78:79], v[216:217], v[78:79] op_sel_hi:[0,1]\n\t"
    "v_pk_mul_f32 v[80:81], v[216:217], v[80:81] op_sel_hi:[0,1]\n\t"
    "v_pk_mul_f32 v[82:83], v[216:217], v[82:83] op_sel_hi:[0,1]\n\t"
    "v_pk_mul_f32 v[84:85], v[216:217], v[84:85] op_sel_hi:[0,1]\n\t"
    "v_pk_mul_f32 v[86:87], v[216:217], v[86:87] op_sel_hi:[0,1]\n\t"
    "v_pk_mul_f32 v[88:89], v[216:217], v[88:89] op_sel_hi:[0,1]\n\t"
    "v_pk_mul_f32 v[90:91], v[216:217], v[90:91] op_sel_hi:[0,1]\n\t"
    "v_pk_mul_f32 v[92:93], v[216:217], v[92:93] op_sel_hi:[0,1]\n\t"
    "v_pk_mul_f32 v[94:95], v[216:217], v[94:95] op_sel_hi:[0,1]\n\t"
    "v_max3_f32 v250, v144, v145, v146\n\t"
    "v_max3_f32 v251, v147, v148, v149\n\t"
    "v_max3_f32 v250, v250, v150, v151\n\t"
    "v_max3_f32 v251, v251, v152, v153\n\t"
    "v_max3_f32 v250, v250, v154, v155\n\t"
    "v_max3_f32 v251, v251, v156, v157\n\t"
    "v_max3_f32 v250, v250, v158, v159\n\t"
    "v_max_f32 v250, v250, v251\n\t"
    "v_mov_b32 v251, v250\n\t"
    "s_nop 1\n\t"
    "v_permlane32_swap_b32 v250, v251\n\t"
    "v_max_f32 v250, v250, v251\n\t"
    "v_mul_f32 v250, s63, v250\n\t"
    "v_max_f32_e64 v250, v250, -v225\n\t"
    "v_add_f32 v216, v225, v250\n\t"
    "v_exp_f32_e64 v216, -v216\n\t"
    "v_sub_f32 v225, 0, v250\n\t"
    "v_mul_f32 v227, v216, v227\n\t"
    "v_pk_mul_f32 v[96:97], v[216:217], v[96:97] op_sel_hi:[0,1]\n\t"
    "v_pk_mul_f32 v[98:99], v[216:217], v[98:99] op_sel_hi:[0,1]\n\t"
    "v_pk_mul_f32 v[100:101], v[216:217], v[100:101] op_sel_hi:[0,1]\n\t"
    "v_pk_mul_f32 v[102:103], v[216:217], v[102:103] op_sel_hi:[0,1]\n\t"
    "v_pk_mul_f32 v[104:105], v[216:217], v[104:105] op_sel_hi:[0,1]\n\t"
    "v_pk_mul_f32 v[106:107], v[216:217], v[106:107] op_sel_hi:[0,1]\n\t"
    "v_pk_mul_f32 v[108:109], v[216:217], v[108:109] op_sel_hi:[0,1]\n\t"
    "v_pk_mul_f32 v[110:111], v[216:217], v[110:111] op_sel_hi:[0,1]\n\t"
    "v_pk_mul_f32 v[112:113], v[216:217], v[112:113] op_sel_hi:[0,1]\n\t"
    "v_pk_mul_f32 v[114:115], v[216:217], v[114:115] op_sel_hi:[0,1]\n\t"
    "v_pk_mul_f32 v[116:117], v[216:217], v[116:117] op_sel_hi:[0,1]\n\t"
    "v_pk_mul_f32 v[118:119], v[216:217], v[118:119] op_sel_hi:[0,1]\n\t"
    "v_pk_mul_f32 v[120:121], v[216:217], v[120:121] op_sel_hi:[0,1]\n\t"
    "v_pk_mul_f32 v[122:123], v[216:217], v[122:123] op_sel_hi:[0,1]\n\t"
    "v_pk_mul_f32 v[124:125], v[216:217], v[124:125] op_sel_hi:[0,1]\n\t"
    "v_pk_mul_f32 v[126:127], v[216:217], v[126:127] op_sel_hi:[0,1]\n\t"
    "v_accvgpr_read_b32 v250, a144\n\t"
    "v_mul_f32 v250, v216, v250\n\t"
    "v_accvgpr_write_b32 a144, v250\n\t"
    "v_accvgpr_read_b32 v250, a145\n\t"
    "v_mul_f32 v250, v216, v250\n\t"
    "v_accvgpr_write_b32 a145, v250\n\t"
    "v_accvgpr_read_b32 v250, a146\n\t"
    "v_mul_f32 v250, v216, v250\n\t"
    "v_accvgpr_write_b32 a146, v250\n\t"
    "v_accvgpr_read_b32 v250, a147\n\t"
    "v_mul_f32 v250, v216, v250\n\t"
    "v_accvgpr_write_b32 a147, v250\n\t"
    "v_accvgpr_read_b32 v250, a148\n\t"
    "v_mul_f32 v250, v216, v250\n\t"
    "v_accvgpr_write_b32 a148, v250\n\t"
    "v_accvgpr_read_b32 v250, a149\n\t"
    "v_mul_f32 v250, v216, v250\n\t"
    "v_accvgpr_write_b32 a149, v250\n\t"
    "v_accvgpr_read_b32 v250, a150\n\t"
    "v_mul_f32 v250, v216, v250\n\t"
    "v_accvgpr_write_b32 a150, v250\n\t"
    "v_accvgpr_read_b32 v250, a151\n\t"
    "v_mul_f32 v250, v216, v250\n\t"
    "v_accvgpr_write_b32 a151, v250\n\t"
    "v_accvgpr_read_b32 v250, a152\n\t"
    "v_mul_f32 v250, v216, v250\n\t"
    "v_accvgpr_write_b32 a152, v250\n\t"
    "v_accvgpr_read_b32 v250, a153\n\t"
    "v_mul_f32 v250, v216, v250\n\t"
    "v_accvgpr_write_b32 a153, v250\n\t"
    "v_accvgpr_read_b32 v250, a154\n\t"
    "v_mul_f32 v250, v216, v250\n\t"
    "v_accvgpr_write_b32 a154, v250\n\t"
    "v_accvgpr_read_b32 v250, a155\n\t"
    "v_mul_f32 v250, v216, v250\n\t"
    "v_accvgpr_write_b32 a155, v250\n\t"
    "v_accvgpr_read_b32 v250, a156\n\t"
    "v_mul_f32 v250, v216, v250\n\t"
    "v_accvgpr_write_b32 a156, v250\n\t"
    "v_accvgpr_read_b32 v250, a157\n\t"
    "v_mul_f32 v250, v216, v250\n\t"
    "v_accvgpr_write_b32 a157, v250\n\t"
    "v_accvgpr_read_b32 v250, a158\n\t"
    "v_mul_f32 v250, v216, v250\n\t"
    "v_accvgpr_write_b32 a158, v250\n\t"
    "v_accvgpr_read_b32 v250, a159\n\t"
    "v_mul_f32 v250, v216, v250\n\t"
    "v_accvgpr_write_b32 a159, v250\n\t"
    "v_accvgpr_read_b32 v250, a160\n\t"
    "v_mul_f32 v250, v216, v250\n\t"
    "v_accvgpr_write_b32 a160, v250\n\t"
    "v_accvgpr_read_b32 v250, a161\n\t"
    "v_mul_f32 v250, v216, v250\n\t"
    "v_accvgpr_write_b32 a161, v250\n\t"
    "v_accvgpr_read_b32 v250, a162\n\t"
    "v_mul_f32 v250, v216, v250\n\t"
    "v_accvgpr_write_b32 a162, v250\n\t"
    "v_accvgpr_read_b32 v250, a163\n\t"
    "v_mul_f32 v250, v216, v250\n\t"
    "v_accvgpr_write_b32 a163, v250\n\t"
    "v_accvgpr_read_b32 v250, a164\n\t"
    "v_mul_f32 v250, v216, v250\n\t"
    "v_accvgpr_write_b32 a164, v250\n\t"
    "v_accvgpr_read_b32 v250, a165\n\t"
    "v_mul_f32 v250, v216, v250\n\t"
    "v_accvgpr_write_b32 a165, v250\n\t"
    "v_accvgpr_read_b32 v250, a166\n\t"
    "v_mul_f32 v250, v216, v250\n\t"
    "v_accvgpr_write_b32 a166, v250\n\t"
    "v_accvgpr_read_b32 v250, a167\n\t"
    "v_mul_f32 v250, v216, v250\n\t"
    "v_accvgpr_write_b32 a167, v250\n\t"
    "v_accvgpr_read_b32 v250, a168\n\t"
    "v_mul_f32 v250, v216, v250\n\t"
    "v_accvgpr_write_b32 a168, v250\n\t"
    "v_accvgpr_read_b32 v250, a169\n\t"
    "v_mul_f32 v250, v216, v250\n\t"
    "v_accvgpr_write_b32 a169, v250\n\t"
    "v_accvgpr_read_b32 v250, a170\n\t"
    "v_mul_f32 v250, v216, v250\n\t"
    "v_accvgpr_write_b32 a170, v250\n\t"
    "v_accvgpr_read_b32 v250, a171\n\t"
    "v_mul_f32 v250, v216, v250\n\t"
    "v_accvgpr_write_b32 a171, v250\n\t"
    "v_accvgpr_read_b32 v250, a172\n\t"
    "v_mul_f32 v250, v216, v250\n\t"
    "v_accvgpr_write_b32 a172, v250\n\t"
    "v_accvgpr_read_b32 v250, a173\n\t"
    "v_mul_f32 v250, v216, v250\n\t"
    "v_accvgpr_write_b32 a173, v250\n\t"
    "v_accvgpr_read_b32 v250, a174\n\t"
    "v_mul_f32 v250, v216, v250\n\t"
    "v_accvgpr_write_b32 a174, v250\n\t"
    "v_accvgpr_read_b32 v250, a175\n\t"
    "v_mul_f32 v250, v216, v250\n\t"
    "v_accvgpr_write_b32 a175, v250\n\t"
    "v_accvgpr_read_b32 v250, a176\n\t"
    "v_mul_f32 v250, v216, v250\n\t"
    "v_accvgpr_write_b32 a176, v250\n\t"
    "v_accvgpr_read_b32 v250, a177\n\t"
    "v_mul_f32 v250, v216, v250\n\t"
    "v_accvgpr_write_b32 a177, v250\n\t"
    "v_accvgpr_read_b32 v250, a178\n\t"
    "v_mul_f32 v250, v216, v250\n\t"
    "v_accvgpr_write_b32 a178, v250\n\t"
    "v_accvgpr_read_b32 v250, a179\n\t"
    "v_mul_f32 v250, v216, v250\n\t"
    "v_accvgpr_write_b32 a179, v250\n\t"
    "v_accvgpr_read_b32 v250, a180\n\t"
    "v_mul_f32 v250, v216, v250\n\t"
    "v_accvgpr_write_b32 a180, v250\n\t"
    "v_accvgpr_read_b32 v250, a181\n\t"
    "v_mul_f32 v250, v216, v250\n\t"
    "v_accvgpr_write_b32 a181, v250\n\t"
    "v_accvgpr_read_b32 v250, a182\n\t"
    "v_mul_f32 v250, v216, v250\n\t"
    "v_accvgpr_write_b32 a182, v250\n\t"
    "v_accvgpr_read_b32 v250, a183\n\t"
    "v_mul_f32 v250, v216, v250\n\t"
    "v_accvgpr_write_b32 a183, v250\n\t"
    "v_accvgpr_read_b32 v250, a184\n\t"
    "v_mul_f32 v250, v216, v250\n\t"
    "v_accvgpr_write_b32 a184, v250\n\t"
    "v_accvgpr_read_b32 v250, a185\n\t"
    "v_mul_f32 v250, v216, v250\n\t"
    "v_accvgpr_write_b32 a185, v250\n\t"
    "v_accvgpr_read_b32 v250, a186\n\t"
    "v_mul_f32 v250, v216, v250\n\t"
    "v_accvgpr_write_b32 a186, v250\n\t"
    "v_accvgpr_read_b32 v250, a187\n\t"
    "v_mul_f32 v250, v216, v250\n\t"
    "v_accvgpr_write_b32 a187, v250\n\t"
    "v_accvgpr_read_b32 v250, a188\n\t"
    "v_mul_f32 v250, v216, v250\n\t"
    "v_accvgpr_write_b32 a188, v250\n\t"
    "v_accvgpr_read_b32 v250, a189\n\t"
    "v_mul_f32 v250, v216, v250\n\t"
    "v_accvgpr_write_b32 a189, v250\n\t"
    "v_accvgpr_read_b32 v250, a190\n\t"
    "v_mul_f32 v250, v216, v250\n\t"
    "v_accvgpr_write_b32 a190, v250\n\t"
    "v_accvgpr_read_b32 v250, a191\n\t"
    "v_mul_f32 v250, v216, v250\n\t"
    "v_accvgpr_write_b32 a191, v250\n\t"
    "v_accvgpr_read_b32 v250, a192\n\t"
    "v_mul_f32 v250, v216, v250\n\t"
    "v_accvgpr_write_b32 a192, v250\n\t"
    "v_accvgpr_read_b32 v250, a193\n\t"
    "v_mul_f32 v250, v216, v250\n\t"
    "v_accvgpr_write_b32 a193, v250\n\t"
    "v_accvgpr_read_b32 v250, a194\n\t"
    "v_mul_f32 v250, v216, v250\n\t"
    "v_accvgpr_write_b32 a194, v250\n\t"
    "v_accvgpr_read_b32 v250, a195\n\t"
    "v_mul_f32 v250, v216, v250\n\t"
    "v_accvgpr_write_b32 a195, v250\n\t"
    "v_accvgpr_read_b32 v250, a196\n\t"
    "v_mul_f32 v250, v216, v250\n\t"
    "v_accvgpr_write_b32 a196, v250\n\t"
    "v_accvgpr_read_b32 v250, a197\n\t"
    "v_mul_f32 v250, v216, v250\n\t"
    "v_accvgpr_write_b32 a197, v250\n\t"
    "v_accvgpr_read_b32 v250, a198\n\t"
    "v_mul_f32 v250, v216, v250\n\t"
    "v_accvgpr_write_b32 a198, v250\n\t"
    "v_accvgpr_read_b32 v250, a199\n\t"
    "v_mul_f32 v250, v216, v250\n\t"
    "v_accvgpr_write_b32 a199, v250\n\t"
    "v_accvgpr_read_b32 v250, a200\n\t"
    "v_mul_f32 v250, v216, v250\n\t"
    "v_accvgpr_write_b32 a200, v250\n\t"
    "v_accvgpr_read_b32 v250, a201\n\t"
    "v_mul_f32 v250, v216, v250\n\t"
    "v_accvgpr_write_b32 a201, v250\n\t"
    "v_accvgpr_read_b32 v250, a202\n\t"
    "v_mul_f32 v250, v216, v250\n\t"
    "v_accvgpr_write_b32 a202, v250\n\t"
    "v_accvgpr_read_b32 v250, a203\n\t"
    "v_mul_f32 v250, v216, v250\n\t"
    "v_accvgpr_write_b32 a203, v250\n\t"
    "v_accvgpr_read_b32 v250, a204\n\t"
    "v_mul_f32 v250, v216, v250\n\t"
    "v_accvgpr_write_b32 a204, v250\n\t"
    "v_accvgpr_read_b32 v250, a205\n\t"
    "v_mul_f32 v250, v216, v250\n\t"
    "v_accvgpr_write_b32 a205, v250\n\t"
    "v_accvgpr_read_b32 v250, a206\n\t"
    "v_mul_f32 v250, v216, v250\n\t"
    "v_accvgpr_write_b32 a206, v250\n\t"
    "v_accvgpr_read_b32 v250, a207\n\t"
    "v_mul_f32 v250, v216, v250\n\t"
    "v_accvgpr_write_b32 a207, v250\n\t"
    "v_fma_f32 v216, v128, s63, v224\n\t"
    "v_fma_f32 v217, v129, s63, v224\n\t"
    "v_exp_f32 v216, v216\n\t"
    "v_exp_f32 v217, v217\n\t"
    "v_fma_f32 v218, v144, s63, v225\n\t"
    "v_fma_f32 v219, v145, s63, v225\n\t"
    "v_exp_f32 v218, v218\n\t"
    "v_exp_f32 v219, v219\n\t"
    "v_cvt_pk_bf16_f32 v192, v216, v217\n\t"
    "v_add_f32 v228, v216, v217\n\t"
    "v_fma_f32 v216, v130, s63, v224\n\t"
    "v_fma_f32 v217, v131, s63, v224\n\t"
    "v_exp_f32 v216, v216\n\t"
    "v_exp_f32 v217, v217\n\t"
    "v_cvt_pk_bf16_f32 v196, v218, v219\n\t"
    "v_add_f32 v229, v218, v219\n\t"
    "v_fma_f32 v218, v146, s63, v225\n\t"
    "v_fma_f32 v219, v147, s63, v225\n\t"
    "v_exp_f32 v218, v218\n\t"
    "v_exp_f32 v219, v219\n\t"
    "v_cvt_pk_bf16_f32 v193, v216, v217\n\t"
    "v_add_f32 v228, v228, v216\n\t"
    "v_add_f32 v228, v228, v217\n\t"
    "v_fma_f32 v216, v132, s63, v224\n\t"
    "v_fma_f32 v217, v133, s63, v224\n\t"
    "v_exp_f32 v216, v216\n\t"
    "v_exp_f32 v217, v217\n\t"
    "v_cvt_pk_bf16_f32 v197, v218, v219\n\t"
    "v_add_f32 v229, v229, v218\n\t"
    "v_add_f32 v229, v229, v219\n\t"
    "v_fma_f32 v218, v148, s63, v225\n\t"
    "v_fma_f32 v219, v149, s63, v225\n\t"
    "v_exp_f32 v218, v218\n\t"
    "v_exp_f32 v219, v219\n\t"
    "v_cvt_pk_bf16_f32 v194, v216, v217\n\t"
    "v_add_f32 v228, v228, v216\n\t"
    "v_add_f32 v228, v228, v217\n\t"
    "v_fma_f32 v216, v134, s63, v224\n\t"
    "v_fma_f32 v217, v135, s63, v224\n\t"
    "v_exp_f32 v216, v216\n\t"
    "v_exp_f32 v217, v217\n\t"
    "v_cvt_pk_bf16_f32 v198, v218, v219\n\t"
    "v_add_f32 v229, v229, v218\n\t"
    "v_add_f32 v229, v229, v219\n\t"
    "v_fma_f32 v218, v150, s63, v225\n\t"
    "v_fma_f32 v219, v151, s63, v225\n\t"
    "v_exp_f32 v218, v218\n\t"
    "v_exp_f32 v219, v219\n\t"
    "v_cvt_pk_bf16_f32 v195, v216, v217\n\t"
    "v_add_f32 v228, v228, v216\n\t"
    "v_add_f32 v228, v228, v217\n\t"
    "v_fma_f32 v216, v136, s63, v224\n\t"
    "v_fma_f32 v217, v137, s63, v224\n\t"
    "v_exp_f32 v216, v216\n\t"
    "v_exp_f32 v217, v217\n\t"
    "v_cvt_pk_bf16_f32 v199, v218, v219\n\t"
    "v_add_f32 v229, v229, v218\n\t"
    "v_add_f32 v229, v229, v219\n\t"
    "v_fma_f32 v218, v152, s63, v225\n\t"
    "v_fma_f32 v219, v153, s63, v225\n\t"
    "v_exp_f32 v218, v218\n\t"
    "v_exp_f32 v219, v219\n\t"
    "v_cvt_pk_bf16_f32 v208, v216, v217\n\t"
    "v_add_f32 v228, v228, v216\n\t"
    "v_add_f32 v228, v228, v217\n\t"
    "v_fma_f32 v216, v138, s63, v224\n\t"
    "v_fma_f32 v217, v139, s63, v224\n\t"
    "v_exp_f32 v216, v216\n\t"
    "v_exp_f32 v217, v217\n\t"
    "v_cvt_pk_bf16_f32 v212, v218, v219\n\t"
    "v_add_f32 v229, v229, v218\n\t"
    "v_add_f32 v229, v229, v219\n\t"
    "v_fma_f32 v218, v154, s63, v225\n\t"
    "v_fma_f32 v219, v155, s63, v225\n\t"
    "v_exp_f32 v218, v218\n\t"
    "v_exp_f32 v219, v219\n\t"
    "v_cvt_pk_bf16_f32 v209, v216, v217\n\t"
    "v_add_f32 v228, v228, v216\n\t"
    "v_add_f32 v228, v228, v217\n\t"
    "v_fma_f32 v216, v140, s63, v224\n\t"
    "v_fma_f32 v217, v141, s63, v224\n\t"
    "v_exp_f32 v216, v216\n\t"
    "v_exp_f32 v217, v217\n\t"
    "v_cvt_pk_bf16_f32 v213, v218, v219\n\t"
    "v_add_f32 v229, v229, v218\n\t"
    "v_add_f32 v229, v229, v219\n\t"
    "v_fma_f32 v218, v156, s63, v225\n\t"
    "v_fma_f32 v219, v157, s63, v225\n\t"
    "v_exp_f32 v218, v218\n\t"
    "v_exp_f32 v219, v219\n\t"
    "v_cvt_pk_bf16_f32 v210, v216, v217\n\t"
    "v_add_f32 v228, v228, v216\n\t"
    "v_add_f32 v228, v228, v217\n\t"
    "v_fma_f32 v216, v142, s63, v224\n\t"
    "v_fma_f32 v217, v143, s63, v224\n\t"
    "v_exp_f32 v216, v216\n\t"
    "v_exp_f32 v217, v217\n\t"
    "v_cvt_pk_bf16_f32 v214, v218, v219\n\t"
    "v_add_f32 v229, v229, v218\n\t"
    "v_add_f32 v229, v229, v219\n\t"
    "v_fma_f32 v218, v158, s63, v225\n\t"
    "v_fma_f32 v219, v159, s63, v225\n\t"
    "v_exp_f32 v218, v218\n\t"
    "v_exp_f32 v219, v219\n\t"
    "v_cvt_pk_bf16_f32 v211, v216, v217\n\t"
    "v_add_f32 v228, v228, v216\n\t"
    "v_add_f32 v228, v228, v217\n\t"
    "s_nop 0\n\t"
    "v_cvt_pk_bf16_f32 v215, v218, v219\n\t"
    "v_add_f32 v229, v229, v218\n\t"
    "v_add_f32 v229, v229, v219\n\t"
    "s_nop 1\n\t"
    "s_cmp_eq_u32 s65, 1\n\t"
    "s_cbranch_scc1 .Ld_back1\n\t"
    "s_cmp_eq_u32 s65, 3\n\t"
    "s_cbranch_scc1 .Ld_back3\n\t"
    "s_cmp_eq_u32 s65, 5\n\t"
    "s_cbranch_scc1 .Ld_back5\n\t"
    "s_cmp_eq_u32 s65, 7\n\t"
    "s_cbranch_scc1 .Ld_back7\n\t"
    "s_cmp_eq_u32 s65, 9\n\t"
    "s_cbranch_scc1 .Ld_back9\n\t"
    "s_cmp_eq_u32 s65, 11\n\t"
    "s_cbranch_scc1 .Ld_back11\n\t"
    "s_cmp_eq_u32 s65, 13\n\t"
    "s_cbranch_scc1 .Ld_back13\n\t"
    "s_cmp_eq_u32 s65, 16\n\t"
    "s_cbranch_scc1 .Ld_back16\n\t"
    "s_cmp_eq_u32 s65, 18\n\t"
    "s_cbranch_scc1 .Ld_back18\n\t"
    "s_cmp_eq_u32 s65, 20\n\t"
    "s_cbranch_scc1 .Ld_back20\n\t"
    "s_cmp_eq_u32 s65, 22\n\t"
    "s_cbranch_scc1 .Ld_back22\n\t"
    "s_cmp_eq_u32 s65, 24\n\t"
    "s_cbranch_scc1 .Ld_back24\n\t"
    "s_cmp_eq_u32 s65, 26\n\t"
    "s_cbranch_scc1 .Ld_back26\n\t"
    "s_branch .Ld_back28\n\t"
    ".Ld_rare1:\n\t"
    "s_nop 7\n\t"
    "s_nop 7\n\t"
    "v_max3_f32 v250, v160, v161, v162\n\t"
    "v_max3_f32 v251, v163, v164, v165\n\t"
    "v_max3_f32 v250, v250, v166, v167\n\t"
    "v_max3_f32 v251, v251, v168, v169\n\t"
    "v_max3_f32 v250, v250, v170, v171\n\t"
    "v_max3_f32 v251, v251, v172, v173\n\t"
    "v_max3_f32 v250, v250, v174, v175\n\t"
    "v_max_f32 v250, v250, v251\n\t"
    "v_mov_b32 v251, v250\n\t"
    "s_nop 1\n\t"
    "v_permlane32_swap_b32 v250, v251\n\t"
    "v_max_f32 v250, v250, v251\n\t"
    "v_mul_f32 v250, s63, v250\n\t"
    "v_max_f32_e64 v250, v250, -v224\n\t"
    "v_add_f32 v216, v224, v250\n\t"
    "v_exp_f32_e64 v216, -v216\n\t"
    "v_sub_f32 v224, 0, v250\n\t"
    "v_mul_f32 v226, v216, v226\n\t"
    "v_pk_mul_f32 v[0:1], v[216:217], v[0:1] op_sel_hi:[0,1]\n\t"
    "v_pk_mul_f32 v[2:3], v[216:217], v[2:3] op_sel_hi:[0,1]\n\t"
    "v_pk_mul_f32 v[4:5], v[216:217], v[4:5] op_sel_hi:[0,1]\n\t"
    "v_pk_mul_f32 v[6:7], v[216:217], v[6:7] op_sel_hi:[0,1]\n\t"
    "v_pk_mul_f32 v[8:9], v[216:217], v[8:9] op_sel_hi:[0,1]\n\t"
    "v_pk_mul_f32 v[10:11], v[216:217], v[10:11] op_sel_hi:[0,1]\n\t"
    "v_pk_mul_f32 v[12:13], v[216:217], v[12:13] op_sel_hi:[0,1]\n\t"
    "v_pk_mul_f32 v[14:15], v[216:217], v[14:15] op_sel_hi:[0,1]\n\t"
    "v_pk_mul_f32 v[16:17], v[216:217], v[16:17] op_sel_hi:[0,1]\n\t"
    "v_pk_mul_f32 v[18:19], v[216:217], v[18:19] op_sel_hi:[0,1]\n\t"
    "v_pk_mul_f32 v[20:21], v[216:217], v[20:21] op_sel_hi:[0,1]\n\t"
    "v_pk_mul_f32 v[22:23], v[216:217], v[22:23] op_sel_hi:[0,1]\n\t"
    "v_pk_mul_f32 v[24:25], v[216:217], v[24:25] op_sel_hi:[0,1]\n\t"
    "v_pk_mul_f32 v[26:27], v[216:217], v[26:27] op_sel_hi:[0,1]\n\t"
    "v_pk_mul_f32 v[28:29], v[216:217], v[28:29] op_sel_hi:[0,1]\n\t"
    "v_pk_mul_f32 v[30:31], v[216:217], v[30:31] op_sel_hi:[0,1]\n\t"
    "v_pk_mul_f32 v[32:33], v[216:217], v[32:33] op_sel_hi:[0,1]\n\t"
    "v_pk_mul_f32 v[34:35], v[216:217], v[34:35] op_sel_hi:[0,1]\n\t"
    "v_pk_mul_f32 v[36:37], v[216:217], v[36:37] op_sel_hi:[0,1]\n\t"
    "v_pk_mul_f32 v[38:39], v[216:217], v[38:39] op_sel_hi:[0,1]\n\t"
    "v_pk_mul_f32 v[40:41], v[216:217], v[40:41] op_sel_hi:[0,1]\n\t"
    "v_pk_mul_f32 v[42:43], v[216:217], v[42:43] op_sel_hi:[0,1]\n\t"
    "v_pk_mul_f32 v[44:45], v[216:217], v[44:45] op_sel_hi:[0,1]\n\t"
    "v_pk_mul_f32 v[46:47], v[216:217], v[46:47] op_sel_hi:[0,1]\n\t"
    "v_pk_mul_f32 v[48:49], v[216:217], v[48:49] op_sel_hi:[0,1]\n\t"
    "v_pk_mul_f32 v[50:51], v[216:217], v[50:51] op_sel_hi:[0,1]\n\t"
    "v_pk_mul_f32 v[52:53], v[216:217], v[52:53] op_sel_hi:[0,1]\n\t"
    "v_pk_mul_f32 v[54:55], v[216:217], v[54:55] op_sel_hi:[0,1]\n\t"
    "v_pk_mul_f32 v[56:57], v[216:217], v[56:57] op_sel_hi:[0,1]\n\t"
    "v_pk_mul_f32 v[58:59], v[216:217], v[58:59] op_sel_hi:[0,1]\n\t"
    "v_pk_mul_f32 v[60:61], v[216:217], v[60:61] op_sel_hi:[0,1]\n\t"
    "v_pk_mul_f32 v[62:63], v[216:217], v[62:63] op_sel_hi:[0,1]\n\t"
    "v_pk_mul_f32 v[64:65], v[216:217], v[64:65] op_sel_hi:[0,1]\n\t"
    "v_pk_mul_f32 v[66:67], v[216:217], v[66:67] op_sel_hi:[0,1]\n\t"
    "v_pk_mul_f32 v[68:69], v[216:217], v[68:69] op_sel_hi:[0,1]\n\t"
    "v_pk_mul_f32 v[70:71], v[216:217], v[70:71] op_sel_hi:[0,1]\n\t"
    "v_pk_mul_f32 v[72:73], v[216:217], v[72:73] op_sel_hi:[0,1]\n\t"
    "v_pk_mul_f32 v[74:75], v[216:217], v[74:75] op_sel_hi:[0,1]\n\t"
    "v_pk_mul_f32 v[76:77], v[216:217], v[76:77] op_sel_hi:[0,1]\n\t"
    "v_pk_mul_f32 v[78:79], v[216:217], v[78:79] op_sel_hi:[0,1]\n\t"
    "v_pk_mul_f32 v[80:81], v[216:217], v[80:81] op_sel_hi:[0,1]\n\t"
    "v_pk_mul_f32 v[82:83], v[216:217], v[82:83] op_sel_hi:[0,1]\n\t"
    "v_pk_mul_f32 v[84:85], v[216:217], v[84:85] op_sel_hi:[0,1]\n\t"
    "v_pk_mul_f32 v[86:87], v[216:217], v[86:87] op_sel_hi:[0,1]\n\t"
    "v_pk_mul_f32 v[88:89], v[216:217], v[88:89] op_sel_hi:[0,1]\n\t"
    "v_pk_mul_f32 v[90:91], v[216:217], v[90:91] op_sel_hi:[0,1]\n\t"
    "v_pk_mul_f32 v[92:93], v[216:217], v[92:93] op_sel_hi:[0,1]\n\t"
    "v_pk_mul_f32 v[94:95], v[216:217], v[94:95] op_sel_hi:[0,1]\n\t"
    "v_max3_f32 v250, v176, v177, v178\n\t"
    "v_max3_f32 v251, v179, v180, v181\n\t"
    "v_max3_f32 v250, v250, v182, v183\n\t"
    "v_max3_f32 v251, v251, v184, v185\n\t"
    "v_max3_f32 v250, v250, v186, v187\n\t"
    "v_max3_f32 v251, v251, v188, v189\n\t"
    "v_max3_f32 v250, v250, v190, v191\n\t"
    "v_max_f32 v250, v250, v251\n\t"
    "v_mov_b32 v251, v250\n\t"
    "s_nop 1\n\t"
    "v_permlane32_swap_b32 v250, v251\n\t"
    "v_max_f32 v250, v250, v251\n\t"
    "v_mul_f32 v250, s63, v250\n\t"
    "v_max_f32_e64 v250, v250, -v225\n\t"
    "v_add_f32 v216, v225, v250\n\t"
    "v_exp_f32_e64 v216, -v216\n\t"
    "v_sub_f32 v225, 0, v250\n\t"
    "v_mul_f32 v227, v216, v227\n\t"
    "v_pk_mul_f32 v[96:97], v[216:217], v[96:97] op_sel_hi:[0,1]\n\t"
    "v_pk_mul_f32 v[98:99], v[216:217], v[98:99] op_sel_hi:[0,1]\n\t"
    "v_pk_mul_f32 v[100:101], v[216:217], v[100:101] op_sel_hi:[0,1]\n\t"
    "v_pk_mul_f32 v[102:103], v[216:217], v[102:103] op_sel_hi:[0,1]\n\t"
    "v_pk_mul_f32 v[104:105], v[216:217], v[104:105] op_sel_hi:[0,1]\n\t"
    "v_pk_mul_f32 v[106:107], v[216:217], v[106:107] op_sel_hi:[0,1]\n\t"
    "v_pk_mul_f32 v[108:109], v[216:217], v[108:109] op_sel_hi:[0,1]\n\t"
    "v_pk_mul_f32 v[110:111], v[216:217], v[110:111] op_sel_hi:[0,1]\n\t"
    "v_pk_mul_f32 v[112:113], v[216:217], v[112:113] op_sel_hi:[0,1]\n\t"
    "v_pk_mul_f32 v[114:115], v[216:217], v[114:115] op_sel_hi:[0,1]\n\t"
    "v_pk_mul_f32 v[116:117], v[216:217], v[116:117] op_sel_hi:[0,1]\n\t"
    "v_pk_mul_f32 v[118:119], v[216:217], v[118:119] op_sel_hi:[0,1]\n\t"
    "v_pk_mul_f32 v[120:121], v[216:217], v[120:121] op_sel_hi:[0,1]\n\t"
    "v_pk_mul_f32 v[122:123], v[216:217], v[122:123] op_sel_hi:[0,1]\n\t"
    "v_pk_mul_f32 v[124:125], v[216:217], v[124:125] op_sel_hi:[0,1]\n\t"
    "v_pk_mul_f32 v[126:127], v[216:217], v[126:127] op_sel_hi:[0,1]\n\t"
    "v_accvgpr_read_b32 v250, a144\n\t"
    "v_mul_f32 v250, v216, v250\n\t"
    "v_accvgpr_write_b32 a144, v250\n\t"
    "v_accvgpr_read_b32 v250, a145\n\t"
    "v_mul_f32 v250, v216, v250\n\t"
    "v_accvgpr_write_b32 a145, v250\n\t"
    "v_accvgpr_read_b32 v250, a146\n\t"
    "v_mul_f32 v250, v216, v250\n\t"
    "v_accvgpr_write_b32 a146, v250\n\t"
    "v_accvgpr_read_b32 v250, a147\n\t"
    "v_mul_f32 v250, v216, v250\n\t"
    "v_accvgpr_write_b32 a147, v250\n\t"
    "v_accvgpr_read_b32 v250, a148\n\t"
    "v_mul_f32 v250, v216, v250\n\t"
    "v_accvgpr_write_b32 a148, v250\n\t"
    "v_accvgpr_read_b32 v250, a149\n\t"
    "v_mul_f32 v250, v216, v250\n\t"
    "v_accvgpr_write_b32 a149, v250\n\t"
    "v_accvgpr_read_b32 v250, a150\n\t"
    "v_mul_f32 v250, v216, v250\n\t"
    "v_accvgpr_write_b32 a150, v250\n\t"
    "v_accvgpr_read_b32 v250, a151\n\t"
    "v_mul_f32 v250, v216, v250\n\t"
    "v_accvgpr_write_b32 a151, v250\n\t"
    "v_accvgpr_read_b32 v250, a152\n\t"
    "v_mul_f32 v250, v216, v250\n\t"
    "v_accvgpr_write_b32 a152, v250\n\t"
    "v_accvgpr_read_b32 v250, a153\n\t"
    "v_mul_f32 v250, v216, v250\n\t"
    "v_accvgpr_write_b32 a153, v250\n\t"
    "v_accvgpr_read_b32 v250, a154\n\t"
    "v_mul_f32 v250, v216, v250\n\t"
    "v_accvgpr_write_b32 a154, v250\n\t"
    "v_accvgpr_read_b32 v250, a155\n\t"
    "v_mul_f32 v250, v216, v250\n\t"
    "v_accvgpr_write_b32 a155, v250\n\t"
    "v_accvgpr_read_b32 v250, a156\n\t"
    "v_mul_f32 v250, v216, v250\n\t"
    "v_accvgpr_write_b32 a156, v250\n\t"
    "v_accvgpr_read_b32 v250, a157\n\t"
    "v_mul_f32 v250, v216, v250\n\t"
    "v_accvgpr_write_b32 a157, v250\n\t"
    "v_accvgpr_read_b32 v250, a158\n\t"
    "v_mul_f32 v250, v216, v250\n\t"
    "v_accvgpr_write_b32 a158, v250\n\t"
    "v_accvgpr_read_b32 v250, a159\n\t"
    "v_mul_f32 v250, v216, v250\n\t"
    "v_accvgpr_write_b32 a159, v250\n\t"
    "v_accvgpr_read_b32 v250, a160\n\t"
    "v_mul_f32 v250, v216, v250\n\t"
    "v_accvgpr_write_b32 a160, v250\n\t"
    "v_accvgpr_read_b32 v250, a161\n\t"
    "v_mul_f32 v250, v216, v250\n\t"
    "v_accvgpr_write_b32 a161, v250\n\t"
    "v_accvgpr_read_b32 v250, a162\n\t"
    "v_mul_f32 v250, v216, v250\n\t"
    "v_accvgpr_write_b32 a162, v250\n\t"
    "v_accvgpr_read_b32 v250, a163\n\t"
    "v_mul_f32 v250, v216, v250\n\t"
    "v_accvgpr_write_b32 a163, v250\n\t"
    "v_accvgpr_read_b32 v250, a164\n\t"
    "v_mul_f32 v250, v216, v250\n\t"
    "v_accvgpr_write_b32 a164, v250\n\t"
    "v_accvgpr_read_b32 v250, a165\n\t"
    "v_mul_f32 v250, v216, v250\n\t"
    "v_accvgpr_write_b32 a165, v250\n\t"
    "v_accvgpr_read_b32 v250, a166\n\t"
    "v_mul_f32 v250, v216, v250\n\t"
    "v_accvgpr_write_b32 a166, v250\n\t"
    "v_accvgpr_read_b32 v250, a167\n\t"
    "v_mul_f32 v250, v216, v250\n\t"
    "v_accvgpr_write_b32 a167, v250\n\t"
    "v_accvgpr_read_b32 v250, a168\n\t"
    "v_mul_f32 v250, v216, v250\n\t"
    "v_accvgpr_write_b32 a168, v250\n\t"
    "v_accvgpr_read_b32 v250, a169\n\t"
    "v_mul_f32 v250, v216, v250\n\t"
    "v_accvgpr_write_b32 a169, v250\n\t"
    "v_accvgpr_read_b32 v250, a170\n\t"
    "v_mul_f32 v250, v216, v250\n\t"
    "v_accvgpr_write_b32 a170, v250\n\t"
    "v_accvgpr_read_b32 v250, a171\n\t"
    "v_mul_f32 v250, v216, v250\n\t"
    "v_accvgpr_write_b32 a171, v250\n\t"
    "v_accvgpr_read_b32 v250, a172\n\t"
    "v_mul_f32 v250, v216, v250\n\t"
    "v_accvgpr_write_b32 a172, v250\n\t"
    "v_accvgpr_read_b32 v250, a173\n\t"
    "v_mul_f32 v250, v216, v250\n\t"
    "v_accvgpr_write_b32 a173, v250\n\t"
    "v_accvgpr_read_b32 v250, a174\n\t"
    "v_mul_f32 v250, v216, v250\n\t"
    "v_accvgpr_write_b32 a174, v250\n\t"
    "v_accvgpr_read_b32 v250, a175\n\t"
    "v_mul_f32 v250, v216, v250\n\t"
    "v_accvgpr_write_b32 a175, v250\n\t"
    "v_accvgpr_read_b32 v250, a176\n\t"
    "v_mul_f32 v250, v216, v250\n\t"
    "v_accvgpr_write_b32 a176, v250\n\t"
    "v_accvgpr_read_b32 v250, a177\n\t"
    "v_mul_f32 v250, v216, v250\n\t"
    "v_accvgpr_write_b32 a177, v250\n\t"
    "v_accvgpr_read_b32 v250, a178\n\t"
    "v_mul_f32 v250, v216, v250\n\t"
    "v_accvgpr_write_b32 a178, v250\n\t"
    "v_accvgpr_read_b32 v250, a179\n\t"
    "v_mul_f32 v250, v216, v250\n\t"
    "v_accvgpr_write_b32 a179, v250\n\t"
    "v_accvgpr_read_b32 v250, a180\n\t"
    "v_mul_f32 v250, v216, v250\n\t"
    "v_accvgpr_write_b32 a180, v250\n\t"
    "v_accvgpr_read_b32 v250, a181\n\t"
    "v_mul_f32 v250, v216, v250\n\t"
    "v_accvgpr_write_b32 a181, v250\n\t"
    "v_accvgpr_read_b32 v250, a182\n\t"
    "v_mul_f32 v250, v216, v250\n\t"
    "v_accvgpr_write_b32 a182, v250\n\t"
    "v_accvgpr_read_b32 v250, a183\n\t"
    "v_mul_f32 v250, v216, v250\n\t"
    "v_accvgpr_write_b32 a183, v250\n\t"
    "v_accvgpr_read_b32 v250, a184\n\t"
    "v_mul_f32 v250, v216, v250\n\t"
    "v_accvgpr_write_b32 a184, v250\n\t"
    "v_accvgpr_read_b32 v250, a185\n\t"
    "v_mul_f32 v250, v216, v250\n\t"
    "v_accvgpr_write_b32 a185, v250\n\t"
    "v_accvgpr_read_b32 v250, a186\n\t"
    "v_mul_f32 v250, v216, v250\n\t"
    "v_accvgpr_write_b32 a186, v250\n\t"
    "v_accvgpr_read_b32 v250, a187\n\t"
    "v_mul_f32 v250, v216, v250\n\t"
    "v_accvgpr_write_b32 a187, v250\n\t"
    "v_accvgpr_read_b32 v250, a188\n\t"
    "v_mul_f32 v250, v216, v250\n\t"
    "v_accvgpr_write_b32 a188, v250\n\t"
    "v_accvgpr_read_b32 v250, a189\n\t"
    "v_mul_f32 v250, v216, v250\n\t"
    "v_accvgpr_write_b32 a189, v250\n\t"
    "v_accvgpr_read_b32 v250, a190\n\t"
    "v_mul_f32 v250, v216, v250\n\t"
    "v_accvgpr_write_b32 a190, v250\n\t"
    "v_accvgpr_read_b32 v250, a191\n\t"
    "v_mul_f32 v250, v216, v250\n\t"
    "v_accvgpr_write_b32 a191, v250\n\t"
    "v_accvgpr_read_b32 v250, a192\n\t"
    "v_mul_f32 v250, v216, v250\n\t"
    "v_accvgpr_write_b32 a192, v250\n\t"
    "v_accvgpr_read_b32 v250, a193\n\t"
    "v_mul_f32 v250, v216, v250\n\t"
    "v_accvgpr_write_b32 a193, v250\n\t"
    "v_accvgpr_read_b32 v250, a194\n\t"
    "v_mul_f32 v250, v216, v250\n\t"
    "v_accvgpr_write_b32 a194, v250\n\t"
    "v_accvgpr_read_b32 v250, a195\n\t"
    "v_mul_f32 v250, v216, v250\n\t"
    "v_accvgpr_write_b32 a195, v250\n\t"
    "v_accvgpr_read_b32 v250, a196\n\t"
    "v_mul_f32 v250, v216, v250\n\t"
    "v_accvgpr_write_b32 a196, v250\n\t"
    "v_accvgpr_read_b32 v250, a197\n\t"
    "v_mul_f32 v250, v216, v250\n\t"
    "v_accvgpr_write_b32 a197, v250\n\t"
    "v_accvgpr_read_b32 v250, a198\n\t"
    "v_mul_f32 v250, v216, v250\n\t"
    "v_accvgpr_write_b32 a198, v250\n\t"
    "v_accvgpr_read_b32 v250, a199\n\t"
    "v_mul_f32 v250, v216, v250\n\t"
    "v_accvgpr_write_b32 a199, v250\n\t"
    "v_accvgpr_read_b32 v250, a200\n\t"
    "v_mul_f32 v250, v216, v250\n\t"
    "v_accvgpr_write_b32 a200, v250\n\t"
    "v_accvgpr_read_b32 v250, a201\n\t"
    "v_mul_f32 v250, v216, v250\n\t"
    "v_accvgpr_write_b32 a201, v250\n\t"
    "v_accvgpr_read_b32 v250, a202\n\t"
    "v_mul_f32 v250, v216, v250\n\t"
    "v_accvgpr_write_b32 a202, v250\n\t"
    "v_accvgpr_read_b32 v250, a203\n\t"
    "v_mul_f32 v250, v216, v250\n\t"
    "v_accvgpr_write_b32 a203, v250\n\t"
    "v_accvgpr_read_b32 v250, a204\n\t"
    "v_mul_f32 v250, v216, v250\n\t"
    "v_accvgpr_write_b32 a204, v250\n\t"
    "v_accvgpr_read_b32 v250, a205\n\t"
    "v_mul_f32 v250, v216, v250\n\t"
    "v_accvgpr_write_b32 a205, v250\n\t"
    "v_accvgpr_read_b32 v250, a206\n\t"
    "v_mul_f32 v250, v216, v250\n\t"
    "v_accvgpr_write_b32 a206, v250\n\t"
    "v_accvgpr_read_b32 v250, a207\n\t"
    "v_mul_f32 v250, v216, v250\n\t"
    "v_accvgpr_write_b32 a207, v250\n\t"
    "v_fma_f32 v216, v160, s63, v224\n\t"
    "v_fma_f32 v217, v161, s63, v224\n\t"
    "v_exp_f32 v216, v216\n\t"
    "v_exp_f32 v217, v217\n\t"
    "v_fma_f32 v218, v176, s63, v225\n\t"
    "v_fma_f32 v219, v177, s63, v225\n\t"
    "v_exp_f32 v218, v218\n\t"
    "v_exp_f32 v219, v219\n\t"
    "v_cvt_pk_bf16_f32 v200, v216, v217\n\t"
    "v_add_f32 v228, v216, v217\n\t"
    "v_fma_f32 v216, v162, s63, v224\n\t"
    "v_fma_f32 v217, v163, s63, v224\n\t"
    "v_exp_f32 v216, v216\n\t"
    "v_exp_f32 v217, v217\n\t"
    "v_cvt_pk_bf16_f32 v204, v218, v219\n\t"
    "v_add_f32 v229, v218, v219\n\t"
    "v_fma_f32 v218, v178, s63, v225\n\t"
    "v_fma_f32 v219, v179, s63, v225\n\t"
    "v_exp_f32 v218, v218\n\t"
    "v_exp_f32 v219, v219\n\t"
    "v_cvt_pk_bf16_f32 v201, v216, v217\n\t"
    "v_add_f32 v228, v228, v216\n\t"
    "v_add_f32 v228, v228, v217\n\t"
    "v_fma_f32 v216, v164, s63, v224\n\t"
    "v_fma_f32 v217, v165, s63, v224\n\t"
    "v_exp_f32 v216, v216\n\t"
    "v_exp_f32 v217, v217\n\t"
    "v_cvt_pk_bf16_f32 v205, v218, v219\n\t"
    "v_add_f32 v229, v229, v218\n\t"
    "v_add_f32 v229, v229, v219\n\t"
    "v_fma_f32 v218, v180, s63, v225\n\t"
    "v_fma_f32 v219, v181, s63, v225\n\t"
    "v_exp_f32 v218, v218\n\t"
    "v_exp_f32 v219, v219\n\t"
    "v_cvt_pk_bf16_f32 v202, v216, v217\n\t"
    "v_add_f32 v228, v228, v216\n\t"
    "v_add_f32 v228, v228, v217\n\t"
    "v_fma_f32 v216, v166, s63, v224\n\t"
    "v_fma_f32 v217, v167, s63, v224\n\t"
    "v_exp_f32 v216, v216\n\t"
    "v_exp_f32 v217, v217\n\t"
    "v_cvt_pk_bf16_f32 v206, v218, v219\n\t"
    "v_add_f32 v229, v229, v218\n\t"
    "v_add_f32 v229, v229, v219\n\t"
    "v_fma_f32 v218, v182, s63, v225\n\t"
    "v_fma_f32 v219, v183, s63, v225\n\t"
    "v_exp_f32 v218, v218\n\t"
    "v_exp_f32 v219, v219\n\t"
    "v_cvt_pk_bf16_f32 v203, v216, v217\n\t"
    "v_add_f32 v228, v228, v216\n\t"
    "v_add_f32 v228, v228, v217\n\t"
    "v_fma_f32 v216, v168, s63, v224\n\t"
    "v_fma_f32 v217, v169, s63, v224\n\t"
    "v_exp_f32 v216, v216\n\t"
    "v_exp_f32 v217, v217\n\t"
    "v_cvt_pk_bf16_f32 v207, v218, v219\n\t"
    "v_add_f32 v229, v229, v218\n\t"
    "v_add_f32 v229, v229, v219\n\t"
    "v_fma_f32 v218, v184, s63, v225\n\t"
    "v_fma_f32 v219, v185, s63, v225\n\t"
    "v_exp_f32 v218, v218\n\t"
    "v_exp_f32 v219, v219\n\t"
    "v_cvt_pk_bf16_f32 v208, v216, v217\n\t"
    "v_add_f32 v228, v228, v216\n\t"
    "v_add_f32 v228, v228, v217\n\t"
    "v_fma_f32 v216, v170, s63, v224\n\t"
    "v_fma_f32 v217, v171, s63, v224\n\t"
    "v_exp_f32 v216, v216\n\t"
    "v_exp_f32 v217, v217\n\t"
    "v_cvt_pk_bf16_f32 v212, v218, v219\n\t"
    "v_add_f32 v229, v229, v218\n\t"
    "v_add_f32 v229, v229, v219\n\t"
    "v_fma_f32 v218, v186, s63, v225\n\t"
    "v_fma_f32 v219, v187, s63, v225\n\t"
    "v_exp_f32 v218, v218\n\t"
    "v_exp_f32 v219, v219\n\t"
    "v_cvt_pk_bf16_f32 v209, v216, v217\n\t"
    "v_add_f32 v228, v228, v216\n\t"
    "v_add_f32 v228, v228, v217\n\t"
    "v_fma_f32 v216, v172, s63, v224\n\t"
    "v_fma_f32 v217, v173, s63, v224\n\t"
    "v_exp_f32 v216, v216\n\t"
    "v_exp_f32 v217, v217\n\t"
    "v_cvt_pk_bf16_f32 v213, v218, v219\n\t"
    "v_add_f32 v229, v229, v218\n\t"
    "v_add_f32 v229, v229, v219\n\t"
    "v_fma_f32 v218, v188, s63, v225\n\t"
    "v_fma_f32 v219, v189, s63, v225\n\t"
    "v_exp_f32 v218, v218\n\t"
    "v_exp_f32 v219, v219\n\t"
    "v_cvt_pk_bf16_f32 v210, v216, v217\n\t"
    "v_add_f32 v228, v228, v216\n\t"
    "v_add_f32 v228, v228, v217\n\t"
    "v_fma_f32 v216, v174, s63, v224\n\t"
    "v_fma_f32 v217, v175, s63, v224\n\t"
    "v_exp_f32 v216, v216\n\t"
    "v_exp_f32 v217, v217\n\t"
    "v_cvt_pk_bf16_f32 v214, v218, v219\n\t"
    "v_add_f32 v229, v229, v218\n\t"
    "v_add_f32 v229, v229, v219\n\t"
    "v_fma_f32 v218, v190, s63, v225\n\t"
    "v_fma_f32 v219, v191, s63, v225\n\t"
    "v_exp_f32 v218, v218\n\t"
    "v_exp_f32 v219, v219\n\t"
    "v_cvt_pk_bf16_f32 v211, v216, v217\n\t"
    "v_add_f32 v228, v228, v216\n\t"
    "v_add_f32 v228, v228, v217\n\t"
    "s_nop 0\n\t"
    "v_cvt_pk_bf16_f32 v215, v218, v219\n\t"
    "v_add_f32 v229, v229, v218\n\t"
    "v_add_f32 v229, v229, v219\n\t"
    "s_nop 1\n\t"
    "s_cmp_eq_u32 s65, 0\n\t"
    "s_cbranch_scc1 .Ld_back0\n\t"
    "s_cmp_eq_u32 s65, 2\n\t"
    "s_cbranch_scc1 .Ld_back2\n\t"
    "s_cmp_eq_u32 s65, 4\n\t"
    "s_cbranch_scc1 .Ld_back4\n\t"
    "s_cmp_eq_u32 s65, 6\n\t"
    "s_cbranch_scc1 .Ld_back6\n\t"
    "s_cmp_eq_u32 s65, 8\n\t"
    "s_cbranch_scc1 .Ld_back8\n\t"
    "s_cmp_eq_u32 s65, 10\n\t"
    "s_cbranch_scc1 .Ld_back10\n\t"
    "s_cmp_eq_u32 s65, 12\n\t"
    "s_cbranch_scc1 .Ld_back12\n\t"
    "s_cmp_eq_u32 s65, 15\n\t"
    "s_cbranch_scc1 .Ld_back15\n\t"
    "s_cmp_eq_u32 s65, 17\n\t"
    "s_cbranch_scc1 .Ld_back17\n\t"
    "s_cmp_eq_u32 s65, 19\n\t"
    "s_cbranch_scc1 .Ld_back19\n\t"
    "s_cmp_eq_u32 s65, 21\n\t"
    "s_cbranch_scc1 .Ld_back21\n\t"
    "s_cmp_eq_u32 s65, 23\n\t"
    "s_cbranch_scc1 .Ld_back23\n\t"
    "s_cmp_eq_u32 s65, 25\n\t"
    "s_cbranch_scc1 .Ld_back25\n\t"
    "s_branch .Ld_back27\n\t"
    ".Ld_rare2:\n\t"
    "s_nop 7\n\t"
    "s_nop 7\n\t"
    "v_max3_f32 v250, v160, v161, v162\n\t"
    "v_max3_f32 v251, v163, v164, v165\n\t"
    "v_max3_f32 v250, v250, v166, v167\n\t"
    "v_max3_f32 v251, v251, v168, v169\n\t"
    "v_max3_f32 v250, v250, v170, v171\n\t"
    "v_max3_f32 v251, v251, v172, v173\n\t"
    "v_max3_f32 v250, v250, v174, v175\n\t"
    "v_max_f32 v250, v250, v251\n\t"
    "v_mov_b32 v251, v250\n\t"
    "s_nop 1\n\t"
    "v_permlane32_swap_b32 v250, v251\n\t"
    "v_max_f32 v250, v250, v251\n\t"
    "v_mul_f32 v250, s63, v250\n\t"
    "v_max_f32_e64 v250, v250, -v224\n\t"
    "v_add_f32 v216, v224, v250\n\t"
    "v_exp_f32_e64 v216, -v216\n\t"
    "v_sub_f32 v224, 0, v250\n\t"
    "v_mul_f32 v226, v216, v226\n\t"
    "v_pk_mul_f32 v[0:1], v[216:217], v[0:1] op_sel_hi:[0,1]\n\t"
    "v_pk_mul_f32 v[2:3], v[216:217], v[2:3] op_sel_hi:[0,1]\n\t"
    "v_pk_mul_f32 v[4:5], v[216:217], v[4:5] op_sel_hi:[0,1]\n\t"
    "v_pk_mul_f32 v[6:7], v[216:217], v[6:7] op_sel_hi:[0,1]\n\t"
    "v_pk_mul_f32 v[8:9], v[216:217], v[8:9] op_sel_hi:[0,1]\n\t"
    "v_pk_mul_f32 v[10:11], v[216:217], v[10:11] op_sel_hi:[0,1]\n\t"
    "v_pk_mul_f32 v[12:13], v[216:217], v[12:13] op_sel_hi:[0,1]\n\t"
    "v_pk_mul_f32 v[14:15], v[216:217], v[14:15] op_sel_hi:[0,1]\n\t"
    "v_pk_mul_f32 v[16:17], v[216:217], v[16:17] op_sel_hi:[0,1]\n\t"
    "v_pk_mul_f32 v[18:19], v[216:217], v[18:19] op_sel_hi:[0,1]\n\t"
    "v_pk_mul_f32 v[20:21], v[216:217], v[20:21] op_sel_hi:[0,1]\n\t"
    "v_pk_mul_f32 v[22:23], v[216:217], v[22:23] op_sel_hi:[0,1]\n\t"
    "v_pk_mul_f32 v[24:25], v[216:217], v[24:25] op_sel_hi:[0,1]\n\t"
    "v_pk_mul_f32 v[26:27], v[216:217], v[26:27] op_sel_hi:[0,1]\n\t"
    "v_pk_mul_f32 v[28:29], v[216:217], v[28:29] op_sel_hi:[0,1]\n\t"
    "v_pk_mul_f32 v[30:31], v[216:217], v[30:31] op_sel_hi:[0,1]\n\t"
    "v_pk_mul_f32 v[32:33], v[216:217], v[32:33] op_sel_hi:[0,1]\n\t"
    "v_pk_mul_f32 v[34:35], v[216:217], v[34:35] op_sel_hi:[0,1]\n\t"
    "v_pk_mul_f32 v[36:37], v[216:217], v[36:37] op_sel_hi:[0,1]\n\t"
    "v_pk_mul_f32 v[38:39], v[216:217], v[38:39] op_sel_hi:[0,1]\n\t"
    "v_pk_mul_f32 v[40:41], v[216:217], v[40:41] op_sel_hi:[0,1]\n\t"
    "v_pk_mul_f32 v[42:43], v[216:217], v[42:43] op_sel_hi:[0,1]\n\t"
    "v_pk_mul_f32 v[44:45], v[216:217], v[44:45] op_sel_hi:[0,1]\n\t"
    "v_pk_mul_f32 v[46:47], v[216:217], v[46:47] op_sel_hi:[0,1]\n\t"
    "v_pk_mul_f32 v[48:49], v[216:217], v[48:49] op_sel_hi:[0,1]\n\t"
    "v_pk_mul_f32 v[50:51], v[216:217], v[50:51] op_sel_hi:[0,1]\n\t"
    "v_pk_mul_f32 v[52:53], v[216:217], v[52:53] op_sel_hi:[0,1]\n\t"
    "v_pk_mul_f32 v[54:55], v[216:217], v[54:55] op_sel_hi:[0,1]\n\t"
    "v_pk_mul_f32 v[56:57], v[216:217], v[56:57] op_sel_hi:[0,1]\n\t"
    "v_pk_mul_f32 v[58:59], v[216:217], v[58:59] op_sel_hi:[0,1]\n\t"
    "v_pk_mul_f32 v[60:61], v[216:217], v[60:61] op_sel_hi:[0,1]\n\t"
    "v_pk_mul_f32 v[62:63], v[216:217], v[62:63] op_sel_hi:[0,1]\n\t"
    "v_pk_mul_f32 v[64:65], v[216:217], v[64:65] op_sel_hi:[0,1]\n\t"
    "v_pk_mul_f32 v[66:67], v[216:217], v[66:67] op_sel_hi:[0,1]\n\t"
    "v_pk_mul_f32 v[68:69], v[216:217], v[68:69] op_sel_hi:[0,1]\n\t"
    "v_pk_mul_f32 v[70:71], v[216:217], v[70:71] op_sel_hi:[0,1]\n\t"
    "v_pk_mul_f32 v[72:73], v[216:217], v[72:73] op_sel_hi:[0,1]\n\t"
    "v_pk_mul_f32 v[74:75], v[216:217], v[74:75] op_sel_hi:[0,1]\n\t"
    "v_pk_mul_f32 v[76:77], v[216:217], v[76:77] op_sel_hi:[0,1]\n\t"
    "v_pk_mul_f32 v[78:79], v[216:217], v[78:79] op_sel_hi:[0,1]\n\t"
    "v_pk_mul_f32 v[80:81], v[216:217], v[80:81] op_sel_hi:[0,1]\n\t"
    "v_pk_mul_f32 v[82:83], v[216:217], v[82:83] op_sel_hi:[0,1]\n\t"
    "v_pk_mul_f32 v[84:85], v[216:217], v[84:85] op_sel_hi:[0,1]\n\t"
    "v_pk_mul_f32 v[86:87], v[216:217], v[86:87] op_sel_hi:[0,1]\n\t"
    "v_pk_mul_f32 v[88:89], v[216:217], v[88:89] op_sel_hi:[0,1]\n\t"
    "v_pk_mul_f32 v[90:91], v[216:217], v[90:91] op_sel_hi:[0,1]\n\t"
    "v_pk_mul_f32 v[92:93], v[216:217], v[92:93] op_sel_hi:[0,1]\n\t"
    "v_pk_mul_f32 v[94:95], v[216:217], v[94:95] op_sel_hi:[0,1]\n\t"
    "v_max3_f32 v250, v176, v177, v178\n\t"
    "v_max3_f32 v251, v179, v180, v181\n\t"
    "v_max3_f32 v250, v250, v182, v183\n\t"
    "v_max3_f32 v251, v251, v184, v185\n\t"
    "v_max3_f32 v250, v250, v186, v187\n\t"
    "v_max3_f32 v251, v251, v188, v189\n\t"
    "v_max3_f32 v250, v250, v190, v191\n\t"
    "v_max_f32 v250, v250, v251\n\t"
    "v_mov_b32 v251, v250\n\t"
    "s_nop 1\n\t"
    "v_permlane32_swap_b32 v250, v251\n\t"
    "v_max_f32 v250, v250, v251\n\t"
    "v_mul_f32 v250, s63, v250\n\t"
    "v_max_f32_e64 v250, v250, -v225\n\t"
    "v_add_f32 v216, v225, v250\n\t"
    "v_exp_f32_e64 v216, -v216\n\t"
    "v_sub_f32 v225, 0, v250\n\t"
    "v_mul_f32 v227, v216, v227\n\t"
    "v_pk_mul_f32 v[96:97], v[216:217], v[96:97] op_sel_hi:[0,1]\n\t"
    "v_pk_mul_f32 v[98:99], v[216:217], v[98:99] op_sel_hi:[0,1]\n\t"
    "v_pk_mul_f32 v[100:101], v[216:217], v[100:101] op_sel_hi:[0,1]\n\t"
    "v_pk_mul_f32 v[102:103], v[216:217], v[102:103] op_sel_hi:[0,1]\n\t"
    "v_pk_mul_f32 v[104:105], v[216:217], v[104:105] op_sel_hi:[0,1]\n\t"
    "v_pk_mul_f32 v[106:107], v[216:217], v[106:107] op_sel_hi:[0,1]\n\t"
    "v_pk_mul_f32 v[108:109], v[216:217], v[108:109] op_sel_hi:[0,1]\n\t"
    "v_pk_mul_f32 v[110:111], v[216:217], v[110:111] op_sel_hi:[0,1]\n\t"
    "v_pk_mul_f32 v[112:113], v[216:217], v[112:113] op_sel_hi:[0,1]\n\t"
    "v_pk_mul_f32 v[114:115], v[216:217], v[114:115] op_sel_hi:[0,1]\n\t"
    "v_pk_mul_f32 v[116:117], v[216:217], v[116:117] op_sel_hi:[0,1]\n\t"
    "v_pk_mul_f32 v[118:119], v[216:217], v[118:119] op_sel_hi:[0,1]\n\t"
    "v_pk_mul_f32 v[120:121], v[216:217], v[120:121] op_sel_hi:[0,1]\n\t"
    "v_pk_mul_f32 v[122:123], v[216:217], v[122:123] op_sel_hi:[0,1]\n\t"
    "v_pk_mul_f32 v[124:125], v[216:217], v[124:125] op_sel_hi:[0,1]\n\t"
    "v_pk_mul_f32 v[126:127], v[216:217], v[126:127] op_sel_hi:[0,1]\n\t"
    "v_accvgpr_read_b32 v250, a144\n\t"
    "v_mul_f32 v250, v216, v250\n\t"
    "v_accvgpr_write_b32 a144, v250\n\t"
    "v_accvgpr_read_b32 v250, a145\n\t"
    "v_mul_f32 v250, v216, v250\n\t"
    "v_accvgpr_write_b32 a145, v250\n\t"
    "v_accvgpr_read_b32 v250, a146\n\t"
    "v_mul_f32 v250, v216, v250\n\t"
    "v_accvgpr_write_b32 a146, v250\n\t"
    "v_accvgpr_read_b32 v250, a147\n\t"
    "v_mul_f32 v250, v216, v250\n\t"
    "v_accvgpr_write_b32 a147, v250\n\t"
    "v_accvgpr_read_b32 v250, a148\n\t"
    "v_mul_f32 v250, v216, v250\n\t"
    "v_accvgpr_write_b32 a148, v250\n\t"
    "v_accvgpr_read_b32 v250, a149\n\t"
    "v_mul_f32 v250, v216, v250\n\t"
    "v_accvgpr_write_b32 a149, v250\n\t"
    "v_accvgpr_read_b32 v250, a150\n\t"
    "v_mul_f32 v250, v216, v250\n\t"
    "v_accvgpr_write_b32 a150, v250\n\t"
    "v_accvgpr_read_b32 v250, a151\n\t"
    "v_mul_f32 v250, v216, v250\n\t"
    "v_accvgpr_write_b32 a151, v250\n\t"
    "v_accvgpr_read_b32 v250, a152\n\t"
    "v_mul_f32 v250, v216, v250\n\t"
    "v_accvgpr_write_b32 a152, v250\n\t"
    "v_accvgpr_read_b32 v250, a153\n\t"
    "v_mul_f32 v250, v216, v250\n\t"
    "v_accvgpr_write_b32 a153, v250\n\t"
    "v_accvgpr_read_b32 v250, a154\n\t"
    "v_mul_f32 v250, v216, v250\n\t"
    "v_accvgpr_write_b32 a154, v250\n\t"
    "v_accvgpr_read_b32 v250, a155\n\t"
    "v_mul_f32 v250, v216, v250\n\t"
    "v_accvgpr_write_b32 a155, v250\n\t"
    "v_accvgpr_read_b32 v250, a156\n\t"
    "v_mul_f32 v250, v216, v250\n\t"
    "v_accvgpr_write_b32 a156, v250\n\t"
    "v_accvgpr_read_b32 v250, a157\n\t"
    "v_mul_f32 v250, v216, v250\n\t"
    "v_accvgpr_write_b32 a157, v250\n\t"
    "v_accvgpr_read_b32 v250, a158\n\t"
    "v_mul_f32 v250, v216, v250\n\t"
    "v_accvgpr_write_b32 a158, v250\n\t"
    "v_accvgpr_read_b32 v250, a159\n\t"
    "v_mul_f32 v250, v216, v250\n\t"
    "v_accvgpr_write_b32 a159, v250\n\t"
    "v_accvgpr_read_b32 v250, a160\n\t"
    "v_mul_f32 v250, v216, v250\n\t"
    "v_accvgpr_write_b32 a160, v250\n\t"
    "v_accvgpr_read_b32 v250, a161\n\t"
    "v_mul_f32 v250, v216, v250\n\t"
    "v_accvgpr_write_b32 a161, v250\n\t"
    "v_accvgpr_read_b32 v250, a162\n\t"
    "v_mul_f32 v250, v216, v250\n\t"
    "v_accvgpr_write_b32 a162, v250\n\t"
    "v_accvgpr_read_b32 v250, a163\n\t"
    "v_mul_f32 v250, v216, v250\n\t"
    "v_accvgpr_write_b32 a163, v250\n\t"
    "v_accvgpr_read_b32 v250, a164\n\t"
    "v_mul_f32 v250, v216, v250\n\t"
    "v_accvgpr_write_b32 a164, v250\n\t"
    "v_accvgpr_read_b32 v250, a165\n\t"
    "v_mul_f32 v250, v216, v250\n\t"
    "v_accvgpr_write_b32 a165, v250\n\t"
    "v_accvgpr_read_b32 v250, a166\n\t"
    "v_mul_f32 v250, v216, v250\n\t"
    "v_accvgpr_write_b32 a166, v250\n\t"
    "v_accvgpr_read_b32 v250, a167\n\t"
    "v_mul_f32 v250, v216, v250\n\t"
    "v_accvgpr_write_b32 a167, v250\n\t"
    "v_accvgpr_read_b32 v250, a168\n\t"
    "v_mul_f32 v250, v216, v250\n\t"
    "v_accvgpr_write_b32 a168, v250\n\t"
    "v_accvgpr_read_b32 v250, a169\n\t"
    "v_mul_f32 v250, v216, v250\n\t"
    "v_accvgpr_write_b32 a169, v250\n\t"
    "v_accvgpr_read_b32 v250, a170\n\t"
    "v_mul_f32 v250, v216, v250\n\t"
    "v_accvgpr_write_b32 a170, v250\n\t"
    "v_accvgpr_read_b32 v250, a171\n\t"
    "v_mul_f32 v250, v216, v250\n\t"
    "v_accvgpr_write_b32 a171, v250\n\t"
    "v_accvgpr_read_b32 v250, a172\n\t"
    "v_mul_f32 v250, v216, v250\n\t"
    "v_accvgpr_write_b32 a172, v250\n\t"
    "v_accvgpr_read_b32 v250, a173\n\t"
    "v_mul_f32 v250, v216, v250\n\t"
    "v_accvgpr_write_b32 a173, v250\n\t"
    "v_accvgpr_read_b32 v250, a174\n\t"
    "v_mul_f32 v250, v216, v250\n\t"
    "v_accvgpr_write_b32 a174, v250\n\t"
    "v_accvgpr_read_b32 v250, a175\n\t"
    "v_mul_f32 v250, v216, v250\n\t"
    "v_accvgpr_write_b32 a175, v250\n\t"
    "v_accvgpr_read_b32 v250, a176\n\t"
    "v_mul_f32 v250, v216, v250\n\t"
    "v_accvgpr_write_b32 a176, v250\n\t"
    "v_accvgpr_read_b32 v250, a177\n\t"
    "v_mul_f32 v250, v216, v250\n\t"
    "v_accvgpr_write_b32 a177, v250\n\t"
    "v_accvgpr_read_b32 v250, a178\n\t"
    "v_mul_f32 v250, v216, v250\n\t"
    "v_accvgpr_write_b32 a178, v250\n\t"
    "v_accvgpr_read_b32 v250, a179\n\t"
    "v_mul_f32 v250, v216, v250\n\t"
    "v_accvgpr_write_b32 a179, v250\n\t"
    "v_accvgpr_read_b32 v250, a180\n\t"
    "v_mul_f32 v250, v216, v250\n\t"
    "v_accvgpr_write_b32 a180, v250\n\t"
    "v_accvgpr_read_b32 v250, a181\n\t"
    "v_mul_f32 v250, v216, v250\n\t"
    "v_accvgpr_write_b32 a181, v250\n\t"
    "v_accvgpr_read_b32 v250, a182\n\t"
    "v_mul_f32 v250, v216, v250\n\t"
    "v_accvgpr_write_b32 a182, v250\n\t"
    "v_accvgpr_read_b32 v250, a183\n\t"
    "v_mul_f32 v250, v216, v250\n\t"
    "v_accvgpr_write_b32 a183, v250\n\t"
    "v_accvgpr_read_b32 v250, a184\n\t"
    "v_mul_f32 v250, v216, v250\n\t"
    "v_accvgpr_write_b32 a184, v250\n\t"
    "v_accvgpr_read_b32 v250, a185\n\t"
    "v_mul_f32 v250, v216, v250\n\t"
    "v_accvgpr_write_b32 a185, v250\n\t"
    "v_accvgpr_read_b32 v250, a186\n\t"
    "v_mul_f32 v250, v216, v250\n\t"
    "v_accvgpr_write_b32 a186, v250\n\t"
    "v_accvgpr_read_b32 v250, a187\n\t"
    "v_mul_f32 v250, v216, v250\n\t"
    "v_accvgpr_write_b32 a187, v250\n\t"
    "v_accvgpr_read_b32 v250, a188\n\t"
    "v_mul_f32 v250, v216, v250\n\t"
    "v_accvgpr_write_b32 a188, v250\n\t"
    "v_accvgpr_read_b32 v250, a189\n\t"
    "v_mul_f32 v250, v216, v250\n\t"
    "v_accvgpr_write_b32 a189, v250\n\t"
    "v_accvgpr_read_b32 v250, a190\n\t"
    "v_mul_f32 v250, v216, v250\n\t"
    "v_accvgpr_write_b32 a190, v250\n\t"
    "v_accvgpr_read_b32 v250, a191\n\t"
    "v_mul_f32 v250, v216, v250\n\t"
    "v_accvgpr_write_b32 a191, v250\n\t"
    "v_accvgpr_read_b32 v250, a192\n\t"
    "v_mul_f32 v250, v216, v250\n\t"
    "v_accvgpr_write_b32 a192, v250\n\t"
    "v_accvgpr_read_b32 v250, a193\n\t"
    "v_mul_f32 v250, v216, v250\n\t"
    "v_accvgpr_write_b32 a193, v250\n\t"
    "v_accvgpr_read_b32 v250, a194\n\t"
    "v_mul_f32 v250, v216, v250\n\t"
    "v_accvgpr_write_b32 a194, v250\n\t"
    "v_accvgpr_read_b32 v250, a195\n\t"
    "v_mul_f32 v250, v216, v250\n\t"
    "v_accvgpr_write_b32 a195, v250\n\t"
    "v_accvgpr_read_b32 v250, a196\n\t"
    "v_mul_f32 v250, v216, v250\n\t"
    "v_accvgpr_write_b32 a196, v250\n\t"
    "v_accvgpr_read_b32 v250, a197\n\t"
    "v_mul_f32 v250, v216, v250\n\t"
    "v_accvgpr_write_b32 a197, v250\n\t"
    "v_accvgpr_read_b32 v250, a198\n\t"
    "v_mul_f32 v250, v216, v250\n\t"
    "v_accvgpr_write_b32 a198, v250\n\t"
    "v_accvgpr_read_b32 v250, a199\n\t"
    "v_mul_f32 v250, v216, v250\n\t"
    "v_accvgpr_write_b32 a199, v250\n\t"
    "v_accvgpr_read_b32 v250, a200\n\t"
    "v_mul_f32 v250, v216, v250\n\t"
    "v_accvgpr_write_b32 a200, v250\n\t"
    "v_accvgpr_read_b32 v250, a201\n\t"
    "v_mul_f32 v250, v216, v250\n\t"
    "v_accvgpr_write_b32 a201, v250\n\t"
    "v_accvgpr_read_b32 v250, a202\n\t"
    "v_mul_f32 v250, v216, v250\n\t"
    "v_accvgpr_write_b32 a202, v250\n\t"
    "v_accvgpr_read_b32 v250, a203\n\t"
    "v_mul_f32 v250, v216, v250\n\t"
    "v_accvgpr_write_b32 a203, v250\n\t"
    "v_accvgpr_read_b32 v250, a204\n\t"
    "v_mul_f32 v250, v216, v250\n\t"
    "v_accvgpr_write_b32 a204, v250\n\t"
    "v_accvgpr_read_b32 v250, a205\n\t"
    "v_mul_f32 v250, v216, v250\n\t"
    "v_accvgpr_write_b32 a205, v250\n\t"
    "v_accvgpr_read_b32 v250, a206\n\t"
    "v_mul_f32 v250, v216, v250\n\t"
    "v_accvgpr_write_b32 a206, v250\n\t"
    "v_accvgpr_read_b32 v250, a207\n\t"
    "v_mul_f32 v250, v216, v250\n\t"
    "v_accvgpr_write_b32 a207, v250\n\t"
    "v_fma_f32 v216, v160, s63, v224\n\t"
    "v_fma_f32 v217, v161, s63, v224\n\t"
    "v_exp_f32 v216, v216\n\t"
    "v_exp_f32 v217, v217\n\t"
    "v_fma_f32 v218, v176, s63, v225\n\t"
    "v_fma_f32 v219, v177, s63, v225\n\t"
    "v_exp_f32 v218, v218\n\t"
    "v_exp_f32 v219, v219\n\t"
    "v_cvt_pk_bf16_f32 v200, v216, v217\n\t"
    "v_add_f32 v228, v216, v217\n\t"
    "v_fma_f32 v216, v162, s63, v224\n\t"
    "v_fma_f32 v217, v163, s63, v224\n\t"
    "v_exp_f32 v216, v216\n\t"
    "v_exp_f32 v217, v217\n\t"
    "v_cvt_pk_bf16_f32 v204, v218, v219\n\t"
    "v_add_f32 v229, v218, v219\n\t"
    "v_fma_f32 v218, v178, s63, v225\n\t"
    "v_fma_f32 v219, v179, s63, v225\n\t"
    "v_exp_f32 v218, v218\n\t"
    "v_exp_f32 v219, v219\n\t"
    "v_cvt_pk_bf16_f32 v201, v216, v217\n\t"
    "v_add_f32 v228, v228, v216\n\t"
    "v_add_f32 v228, v228, v217\n\t"
    "v_fma_f32 v216, v164, s63, v224\n\t"
    "v_fma_f32 v217, v165, s63, v224\n\t"
    "v_exp_f32 v216, v216\n\t"
    "v_exp_f32 v217, v217\n\t"
    "v_cvt_pk_bf16_f32 v205, v218, v219\n\t"
    "v_add_f32 v229, v229, v218\n\t"
    "v_add_f32 v229, v229, v219\n\t"
    "v_fma_f32 v218, v180, s63, v225\n\t"
    "v_fma_f32 v219, v181, s63, v225\n\t"
    "v_exp_f32 v218, v218\n\t"
    "v_exp_f32 v219, v219\n\t"
    "v_cvt_pk_bf16_f32 v202, v216, v217\n\t"
    "v_add_f32 v228, v228, v216\n\t"
    "v_add_f32 v228, v228, v217\n\t"
    "v_fma_f32 v216, v166, s63, v224\n\t"
    "v_fma_f32 v217, v167, s63, v224\n\t"
    "v_exp_f32 v216, v216\n\t"
    "v_exp_f32 v217, v217\n\t"
    "v_cvt_pk_bf16_f32 v206, v218, v219\n\t"
    "v_add_f32 v229, v229, v218\n\t"
    "v_add_f32 v229, v229, v219\n\t"
    "v_fma_f32 v218, v182, s63, v225\n\t"
    "v_fma_f32 v219, v183, s63, v225\n\t"
    "v_exp_f32 v218, v218\n\t"
    "v_exp_f32 v219, v219\n\t"
    "v_cvt_pk_bf16_f32 v203, v216, v217\n\t"
    "v_add_f32 v228, v228, v216\n\t"
    "v_add_f32 v228, v228, v217\n\t"
    "v_fma_f32 v216, v168, s63, v224\n\t"
    "v_fma_f32 v217, v169, s63, v224\n\t"
    "v_exp_f32 v216, v216\n\t"
    "v_exp_f32 v217, v217\n\t"
    "v_cvt_pk_bf16_f32 v207, v218, v219\n\t"
    "v_add_f32 v229, v229, v218\n\t"
    "v_add_f32 v229, v229, v219\n\t"
    "v_fma_f32 v218, v184, s63, v225\n\t"
    "v_fma_f32 v219, v185, s63, v225\n\t"
    "v_exp_f32 v218, v218\n\t"
    "v_exp_f32 v219, v219\n\t"
    "v_cvt_pk_bf16_f32 v192, v216, v217\n\t"
    "v_add_f32 v228, v228, v216\n\t"
    "v_add_f32 v228, v228, v217\n\t"
    "v_fma_f32 v216, v170, s63, v224\n\t"
    "v_fma_f32 v217, v171, s63, v224\n\t"
    "v_exp_f32 v216, v216\n\t"
    "v_exp_f32 v217, v217\n\t"
    "v_cvt_pk_bf16_f32 v196, v218, v219\n\t"
    "v_add_f32 v229, v229, v218\n\t"
    "v_add_f32 v229, v229, v219\n\t"
    "v_fma_f32 v218, v186, s63, v225\n\t"
    "v_fma_f32 v219, v187, s63, v225\n\t"
    "v_exp_f32 v218, v218\n\t"
    "v_exp_f32 v219, v219\n\t"
    "v_cvt_pk_bf16_f32 v193, v216, v217\n\t"
    "v_add_f32 v228, v228, v216\n\t"
    "v_add_f32 v228, v228, v217\n\t"
    "v_fma_f32 v216, v172, s63, v224\n\t"
    "v_fma_f32 v217, v173, s63, v224\n\t"
    "v_exp_f32 v216, v216\n\t"
    "v_exp_f32 v217, v217\n\t"
    "v_cvt_pk_bf16_f32 v197, v218, v219\n\t"
    "v_add_f32 v229, v229, v218\n\t"
    "v_add_f32 v229, v229, v219\n\t"
    "v_fma_f32 v218, v188, s63, v225\n\t"
    "v_fma_f32 v219, v189, s63, v225\n\t"
    "v_exp_f32 v218, v218\n\t"
    "v_exp_f32 v219, v219\n\t"
    "v_cvt_pk_bf16_f32 v194, v216, v217\n\t"
    "v_add_f32 v228, v228, v216\n\t"
    "v_add_f32 v228, v228, v217\n\t"
    "v_fma_f32 v216, v174, s63, v224\n\t"
    "v_fma_f32 v217, v175, s63, v224\n\t"
    "v_exp_f32 v216, v216\n\t"
    "v_exp_f32 v217, v217\n\t"
    "v_cvt_pk_bf16_f32 v198, v218, v219\n\t"
    "v_add_f32 v229, v229, v218\n\t"
    "v_add_f32 v229, v229, v219\n\t"
    "v_fma_f32 v218, v190, s63, v225\n\t"
    "v_fma_f32 v219, v191, s63, v225\n\t"
    "v_exp_f32 v218, v218\n\t"
    "v_exp_f32 v219, v219\n\t"
    "v_cvt_pk_bf16_f32 v195, v216, v217\n\t"
    "v_add_f32 v228, v228, v216\n\t"
    "v_add_f32 v228, v228, v217\n\t"
    "s_nop 0\n\t"
    "v_cvt_pk_bf16_f32 v199, v218, v219\n\t"
    "v_add_f32 v229, v229, v218\n\t"
    "v_add_f32 v229, v229, v219\n\t"
    "s_nop 1\n\t"
    "s_cmp_eq_u32 s65, 14\n\t"
    "s_cbranch_scc1 .Ld_back14\n\t"
    "s_branch .Ld_back29\n\t"
    ".Ld_end:\n\t"
    : "+{v230}"(kb[0]), "+{v231}"(kb[1]), "+{v232}"(kb[2]), "+{v233}"(kb[3]), "+{v234}"(vb), "+{v235}"(kv[0]), "+{v236}"(kv[1]), "+{v237}"(kv[2]), "+{v238}"(vv[0]), "+{v239}"(vv[1]), "+{v240}"(vv[2]), "+{v244}"(fa[0]), "+{v245}"(fa[1]), "+{v246}"(fa[2]), "+{v247}"(fa[3]), "+{v241}"(la), "+{v248}"(ga[0]), "+{v249}"(ga[1]), "+{s[16:19]}"(srdK), "+{s[20:23]}"(srdV), "+{s[82:83]}"(qptr), "+{s[84:85]}"(optr), "+{s46}"(kwb), "+{s49}"(vwb), "+{s81}"(grp), "+{s61}"(buf), "+{s66}"(ba[0]), "+{s67}"(ba[1]), "+{s68}"(ba[2]), "+{s69}"(bb[0]), "+{s70}"(bb[1]), "+{s71}"(bb[2]), "+{s72}"(st[0]), "+{s73}"(st[1]), "+{s74}"(st[2]), "+{s75}"(qo[0]), "+{s76}"(qo[1]), "+{s77}"(qo[2]), "+{s78}"(qn[0]), "+{s79}"(qn[1]), "+{s80}"(qn[2])
    :
    : "v0", "v1", "v2", "v3", "v4", "v5", "v6", "v7", "v8", "v9", "v10", "v11", "v12", "v13", "v14", "v15", "v16", "v17", "v18", "v19", "v20", "v21", "v22", "v23", "v24", "v25", "v26", "v27", "v28", "v29", "v30", "v31", "v32", "v33", "v34", "v35", "v36", "v37", "v38", "v39", "v40", "v41", "v42", "v43", "v44", "v45", "v46", "v47", "v48", "v49", "v50", "v51", "v52", "v53", "v54", "v55", "v56", "v57", "v58", "v59", "v60", "v61", "v62", "v63", "v64", "v65", "v66", "v67", "v68", "v69", "v70", "v71", "v72", "v73", "v74", "v75", "v76", "v77", "v78", "v79", "v80", "v81", "v82", "v83", "v84", "v85", "v86", "v87", "v88", "v89", "v90", "v91", "v92", "v93", "v94", "v95", "v96", "v97", "v98", "v99", "v100", "v101", "v102", "v103", "v104", "v105", "v106", "v107", "v108", "v109", "v110", "v111", "v112", "v113", "v114", "v115", "v116", "v117", "v118", "v119", "v120", "v121", "v122", "v123", "v124", "v125", "v126", "v127", "v128", "v129", "v130", "v131", "v132", "v133", "v134", "v135", "v136", "v137", "v138", "v139", "v140", "v141", "v142", "v143", "v144", "v145", "v146", "v147", "v148", "v149", "v150", "v151", "v152", "v153", "v154", "v155", "v156", "v157", "v158", "v159", "v160", "v161", "v162", "v163", "v164", "v165", "v166", "v167", "v168", "v169", "v170", "v171", "v172", "v173", "v174", "v175", "v176", "v177", "v178", "v179", "v180", "v181", "v182", "v183", "v184", "v185", "v186", "v187", "v188", "v189", "v190", "v191", "v192", "v193", "v194", "v195", "v196", "v197", "v198", "v199", "v200", "v201", "v202", "v203", "v204", "v205", "v206", "v207", "v208", "v209", "v210", "v211", "v212", "v213", "v214", "v215", "v216", "v217", "v218", "v219", "v220", "v221", "v222", "v223", "v224", "v225", "v226", "v227", "v228", "v229", "v242", "v243", "v250", "v251", "v252", "v253", "v254", "v255", "a0", "a1", "a2", "a3", "a4", "a5", "a6", "a7", "a8", "a9", "a10", "a11", "a12", "a13", "a14", "a15", "a16", "a17", "a18", "a19", "a20", "a21", "a22", "a23", "a24", "a25", "a26", "a27", "a28", "a29", "a30", "a31", "a32", "a33", "a34", "a35", "a36", "a37", "a38", "a39", "a40", "a41", "a42", "a43", "a44", "a45", "a46", "a47", "a48", "a49", "a50", "a51", "a52", "a53", "a54", "a55", "a56", "a57", "a58", "a59", "a60", "a61", "a62", "a63", "a64", "a65", "a66", "a67", "a68", "a69", "a70", "a71", "a72", "a73", "a74", "a75", "a76", "a77", "a78", "a79", "a80", "a81", "a82", "a83", "a84", "a85", "a86", "a87", "a88", "a89", "a90", "a91", "a92", "a93", "a94", "a95", "a96", "a97", "a98", "a99", "a100", "a101", "a102", "a103", "a104", "a105", "a106", "a107", "a108", "a109", "a110", "a111", "a112", "a113", "a114", "a115", "a116", "a117", "a118", "a119", "a120", "a121", "a122", "a123", "a124", "a125", "a126", "a127", "a128", "a129", "a130", "a131", "a132", "a133", "a134", "a135", "a136", "a137", "a138", "a139", "a140", "a141", "a142", "a143", "a144", "a145", "a146", "a147", "a148", "a149", "a150", "a151", "a152", "a153", "a154", "a155", "a156", "a157", "a158", "a159", "a160", "a161", "a162", "a163", "a164", "a165", "a166", "a167", "a168", "a169", "a170", "a171", "a172", "a173", "a174", "a175", "a176", "a177", "a178", "a179", "a180", "a181", "a182", "a183", "a184", "a185", "a186", "a187", "a188", "a189", "a190", "a191", "a192", "a193", "a194", "a195", "a196", "a197", "a198", "a199", "a200", "a201", "a202", "a203", "a204", "a205", "a206", "a207", "a208", "a209", "a210", "a211", "a212", "a213", "a214", "a215", "a216", "a217", "a218", "a219", "a220", "a221", "a222", "a223", "a224", "a225", "a226", "a227", "a228", "a229", "a230", "a231", "a232", "a233", "a234", "a235", "a236", "a237", "a238", "a239", "a240", "a241", "a242", "a243", "a244", "a245", "a246", "a247", "a248", "a249", "a250", "a251", "a252", "a253", "a254", "a255", "s24", "s25", "s26", "s27", "s28", "s29", "s30", "s31", "s32", "s33", "s34", "s35", "s36", "s37", "s38", "s39", "s40", "s41", "s42", "s43", "s44", "s45", "s47", "s48", "s50", "s51", "s52", "s53", "s54", "s55", "s56", "s57", "s58", "s59", "s60", "s62", "s63", "s64", "s65", "s86", "s87", "vcc", "scc", "memory");
}
}

extern "C" void kernel_launch(void* const* d_in, const int* in_sizes, int n_in, void* d_out, int out_size,
                              void* d_ws, size_t ws_size, hipStream_t stream) {
  (void)in_sizes; (void)n_in; (void)out_size; (void)d_ws; (void)ws_size;
  (void)hipFuncSetAttribute((const void*)attn_fwd, hipFuncAttributeMaxDynamicSharedMemorySize, (int)LDS_BYTES);
  hipLaunchKernelGGL(attn_fwd, dim3(256), dim3(256), LDS_BYTES, stream, d_in[0], d_in[1], d_in[2], d_out);
}
```
